```python
import torch

def _softmax(x, axis=-1):
    return torch.softmax(x, dim=axis)
def _gelu(x):
    return torch.nn.functional.gelu(x, approximate="tanh")
def _silu(x):
    return torch.nn.functional.silu(x)
def _sigmoid(x):
    return torch.sigmoid(x)
def _layernorm(x, axis=-1, eps=1e-5):
    m = x.mean(dim=axis, keepdim=True)
    v = x.var(dim=axis, keepdim=True, unbiased=False)
    return (x - m) / torch.sqrt(v + eps)
def _scatter_add(tgt, idx, src, axis=0):
    return tgt.clone().index_add_(axis, idx, src)
def _segment_sum(src, seg, n):
    out = torch.zeros((n,) + src.shape[1:], dtype=src.dtype)
    return out.index_add_(0, seg, src)
def _pad(x, pw):
    flat = tuple(v for pair in reversed(pw) for v in pair)
    return torch.nn.functional.pad(x, flat)

kernel_name = 'interleave_layers__spectral_fnet__moe_arch_mod__prompt'

def setup_inputs():

    _w = (lambda a: (lambda t: t.to(torch.bfloat16) if t.is_floating_point() else t)(torch.as_tensor(a)))
    def _A_setup():
        B, S, D = 1, 2048, 640
        ns = torch.arange(S)[:, None] * torch.arange(S)[None, :]
        nd = torch.arange(D)[:, None] * torch.arange(D)[None, :]
        Ws_re = torch.cos(-2.0 * torch.pi * ns / S).to(float)
        Ws_im = torch.sin(-2.0 * torch.pi * ns / S).to(float)
        Wd_re = torch.cos(-2.0 * torch.pi * nd / D).to(float)
        Wd_im = torch.sin(-2.0 * torch.pi * nd / D).to(float)
        return {
            "x": _w((lambda *s: torch.randn(*s, dtype=torch.bfloat16) * 0.1)(B, S, D)),
            "dft_seq_re": _w(Ws_re),
            "dft_seq_im": _w(Ws_im),
            "dft_hid_re": _w(Wd_re),
            "dft_hid_im": _w(Wd_im),
        }
    _A_in = _A_setup()

    def _B_setup():
        B, S, D, F, C = 1, 2048, 640, 1728, 8
        return {
            "x": _w((lambda *s: torch.randn(*s, dtype=torch.bfloat16) * 0.1)(B, S, D)),
            "w_r": _w((lambda *s: torch.randn(*s, dtype=torch.bfloat16) * 1.6)(D)),
            "w_gate": _w((lambda *s: torch.randn(*s, dtype=torch.bfloat16) * 0.4)(D, F)),
            "w_up": _w((lambda *s: torch.randn(*s, dtype=torch.bfloat16) * 0.4)(D, F)),
            "w_down": _w((lambda *s: torch.randn(*s, dtype=torch.bfloat16) * 0.4)(F, D)),
        }
    _B_in = _B_setup()

    return {
        "A_x": _A_in["x"],
        "A_dft_seq_re": _A_in["dft_seq_re"],
        "A_dft_seq_im": _A_in["dft_seq_im"],
        "A_dft_hid_re": _A_in["dft_hid_re"],
        "A_dft_hid_im": _A_in["dft_hid_im"],
        "B_x": _B_in["x"],
        "B_w_r": _B_in["w_r"],
        "B_w_gate": _B_in["w_gate"],
        "B_w_up": _B_in["w_up"],
        "B_w_down": _B_in["w_down"],
    }

def reference(A_x, A_dft_seq_re, A_dft_seq_im, A_dft_hid_re, A_dft_hid_im, B_x, B_w_r, B_w_gate, B_w_up, B_w_down):
    def _A_ref(x, dft_seq_re, dft_seq_im, dft_hid_re, dft_hid_im):
        a = torch.einsum("bsd,ed->bse", x, dft_hid_re)
        b = torch.einsum("bsd,ed->bse", x, dft_hid_im)
        y = torch.einsum("ts,bse->bte", dft_seq_re, a) - torch.einsum("ts,bse->bte", dft_seq_im, b)
        return y

    def _B_ref(x, w_r, w_gate, w_up, w_down):
        B, S, D = x.shape
        C = 8
        r = x @ w_r
        idx = torch.argsort(-r, axis=-1)[:, :C]
        idx_e = (lambda x, n, axis: torch.repeat_interleave(x, n, dim=axis))(idx.reshape(B, C, 1), D, axis=2)
        x_sel = (lambda x, i, axis: torch.take_along_dim(x, i, dim=axis))(x, idx_e, axis=1)
        g = _silu(x_sel @ w_gate)
        u = x_sel @ w_up
        y = (g * u) @ w_down
        r_sel = _sigmoid((lambda x, i, axis: torch.take_along_dim(x, i, dim=axis))(r, idx, axis=1))
        y = y * r_sel.reshape(B, C, 1)
        b_off = (torch.arange(B) * S).reshape(B, 1)
        flat_idx = (idx + b_off).reshape(B * C)
        zero = (x * 0.0).reshape(B * S, D)
        upd = _scatter_add(zero, flat_idx, y.reshape(B * C, D), axis=0)
        return x + upd.reshape(B, S, D)

    x = A_x
    x = x + _A_ref(_layernorm(x, axis=-1), A_dft_seq_re, A_dft_seq_im, A_dft_hid_re, A_dft_hid_im)
    x = x + _B_ref(_layernorm(x, axis=-1), B_w_r, B_w_gate, B_w_up, B_w_down)
    x = x + _A_ref(_layernorm(x, axis=-1), A_dft_seq_re, A_dft_seq_im, A_dft_hid_re, A_dft_hid_im)
    x = x + _B_ref(_layernorm(x, axis=-1), B_w_r, B_w_gate, B_w_up, B_w_down)
    return x
```

```cpp
#include <hip/hip_runtime.h>
#include <hip/hip_bf16.h>
#include <stdint.h>

typedef unsigned short u16;
using bf16x8 = __attribute__((ext_vector_type(8))) short;
using f32x4 = __attribute__((ext_vector_type(4))) float;
using u32x4 = __attribute__((ext_vector_type(4))) unsigned;
using u32x2 = __attribute__((ext_vector_type(2))) unsigned;

constexpr int S = 2048, D = 640, F = 1728, CAP = 8;

__device__ __forceinline__ float bf2f(u16 v) { return __uint_as_float(((unsigned)v) << 16); }
__device__ __forceinline__ u16 f2bf(float f) {
    __hip_bfloat16 b = __float2bfloat16(f);
    return *reinterpret_cast<u16*>(&b);
}
__device__ __forceinline__ float rbf(float f) { return bf2f(f2bf(f)); }
__device__ __forceinline__ float wave_sum(float v) {
#pragma unroll
    for (int o = 32; o > 0; o >>= 1) v += __shfl_xor(v, o);
    return v;
}

template <int MODE>
__global__ __launch_bounds__(256) void k_ln(const u16* __restrict__ x, u16* __restrict__ xn,
                                            const u16* __restrict__ wr, float* __restrict__ r,
                                            u16* __restrict__ x2) {
    int row = blockIdx.x * 4 + (threadIdx.x >> 6), lane = threadIdx.x & 63;
    const u16* xr = x + (size_t)row * D;
    float v[10];
#pragma unroll
    for (int i = 0; i < 5; ++i) {
        unsigned p = *reinterpret_cast<const unsigned*>(xr + i * 128 + lane * 2);
        v[2 * i] = __uint_as_float(p << 16);
        v[2 * i + 1] = __uint_as_float(p & 0xffff0000u);
    }
    float s = 0;
#pragma unroll
    for (int i = 0; i < 10; ++i) s += v[i];
    s = wave_sum(s);
    float mu = s * (1.0f / D);
    float q = 0;
#pragma unroll
    for (int i = 0; i < 10; ++i) q += (v[i] - mu) * (v[i] - mu);
    q = wave_sum(q);
    float var = rbf(q * (1.0f / D));
    float m = rbf(mu);
    float sd = rbf(sqrtf(rbf(var + 1e-5f)));
    float dot = 0;
#pragma unroll
    for (int i = 0; i < 5; ++i) {
        float a = rbf(rbf(v[2 * i] - m) / sd), b = rbf(rbf(v[2 * i + 1] - m) / sd);
        unsigned o = (unsigned)f2bf(a) | ((unsigned)f2bf(b) << 16);
        *reinterpret_cast<unsigned*>(xn + (size_t)row * D + i * 128 + lane * 2) = o;
        if (MODE == 1) {
            unsigned w = *reinterpret_cast<const unsigned*>(wr + i * 128 + lane * 2);
            dot += a * __uint_as_float(w << 16) + b * __uint_as_float(w & 0xffff0000u);
            unsigned o2 = (unsigned)f2bf(v[2 * i] + a) | ((unsigned)f2bf(v[2 * i + 1] + b) << 16);
            *reinterpret_cast<unsigned*>(x2 + (size_t)row * D + i * 128 + lane * 2) = o2;
        }
    }
    if (MODE == 1) {
        dot = wave_sum(dot);
        if (lane == 0) r[row] = rbf(dot);
    }
}

template <int ROWS>
__device__ __forceinline__ void stage_tile(const u16* __restrict__ g, int ld, char* lds, int wid, int lane) {
    constexpr int NI = ROWS / 8;
#pragma unroll
    for (int i = 0; i < (NI + 3) / 4; ++i) {
        int ins = i * 4 + wid;
        if (ins < NI) {
            int row = ins * 8 + (lane >> 3);
            int c = (lane & 7) ^ ((row >> 1) & 7);
            __builtin_amdgcn_global_load_lds((const unsigned*)(g + (size_t)row * ld + c * 8),
                                             (unsigned*)(lds + ins * 1024), 16, 0, 0);
        }
    }
}
__device__ __forceinline__ bf16x8 lds_frag(const char* lds, int row, int c) {
    return *reinterpret_cast<const bf16x8*>(lds + row * 128 + ((c ^ ((row >> 1) & 7)) << 4));
}

__global__ __launch_bounds__(256) void k_gemm1(const u16* __restrict__ xn, const u16* __restrict__ dre,
                                               const u16* __restrict__ dim, u16* __restrict__ abT) {
    constexpr int BM = 128, BN = 80;
    __shared__ __attribute__((aligned(16))) char lds[2 * (BM + BN) * 128];
    int wid = threadIdx.x >> 6, lane = threadIdx.x & 63;
    int bs = blockIdx.x & 15, be = blockIdx.x >> 4;
    const u16* A = xn + (size_t)bs * BM * D;
    const u16* B = (be < 8 ? dre + (size_t)be * BN * D : dim + (size_t)(be - 8) * BN * D);
    f32x4 acc[2][5] = {};
    constexpr int NT = D / 64;
    stage_tile<BM>(A, D, lds, wid, lane);
    stage_tile<BN>(B, D, lds + BM * 128, wid, lane);
    for (int t = 0; t < NT; ++t) {
        asm volatile("s_waitcnt vmcnt(0)" ::: "memory");
        __syncthreads();
        char* cur = lds + (t & 1) * (BM + BN) * 128;
        if (t + 1 < NT) {
            char* nxt = lds + ((t + 1) & 1) * (BM + BN) * 128;
            stage_tile<BM>(A + (t + 1) * 64, D, nxt, wid, lane);
            stage_tile<BN>(B + (t + 1) * 64, D, nxt + BM * 128, wid, lane);
        }
#pragma unroll
        for (int kk = 0; kk < 2; ++kk) {
            bf16x8 a[2], b[5];
#pragma unroll
            for (int m = 0; m < 2; ++m) a[m] = lds_frag(cur, wid * 32 + m * 16 + (lane & 15), kk * 4 + (lane >> 4));
#pragma unroll
            for (int n = 0; n < 5; ++n) b[n] = lds_frag(cur + BM * 128, n * 16 + (lane & 15), kk * 4 + (lane >> 4));
#pragma unroll
            for (int m = 0; m < 2; ++m)
#pragma unroll
                for (int n = 0; n < 5; ++n)
                    acc[m][n] = __builtin_amdgcn_mfma_f32_16x16x32_bf16(a[m], b[n], acc[m][n], 0, 0, 0);
        }
    }
#pragma unroll
    for (int m = 0; m < 2; ++m)
#pragma unroll
        for (int n = 0; n < 5; ++n) {
            int e = be * BN + n * 16 + (lane & 15);
            int s = bs * BM + wid * 32 + m * 16 + (lane >> 4) * 4;
            float sg = be < 8 ? 1.f : -1.f;
            u32x2 o;
            o.x = (unsigned)f2bf(sg * acc[m][n][0]) | ((unsigned)f2bf(sg * acc[m][n][1]) << 16);
            o.y = (unsigned)f2bf(sg * acc[m][n][2]) | ((unsigned)f2bf(sg * acc[m][n][3]) << 16);
            *reinterpret_cast<u32x2*>(abT + (size_t)e * S + s) = o;
        }
}

__global__ __launch_bounds__(256) void k_gemm2(const u16* __restrict__ cre, const u16* __restrict__ cim,
                                               const u16* __restrict__ abT, const u16* __restrict__ xi,
                                               u16* __restrict__ xo) {
    constexpr int BT = 64, BE = 80;
    __shared__ __attribute__((aligned(16))) char lds[2 * (BT + BE) * 128];
    int wid = threadIdx.x >> 6, lane = threadIdx.x & 63;
    int xcd = blockIdx.x & 7, loc = blockIdx.x >> 3;
    int bt = xcd * 4 + (loc & 3), be = loc >> 2;
    f32x4 acc[5] = {};
    constexpr int NT = 2 * S / 64;
    auto stage = [&](int t, char* dst) {
        int half = t >= NT / 2;
        int k0 = (t - half * (NT / 2)) * 64;
        const u16* Cm = (half ? cim : cre) + (size_t)bt * BT * S + k0;
        const u16* Bm = abT + (size_t)(half * D + be * BE) * S + k0;
        stage_tile<BT>(Cm, S, dst, wid, lane);
        stage_tile<BE>(Bm, S, dst + BT * 128, wid, lane);
    };
    stage(0, lds);
    for (int t = 0; t < NT; ++t) {
        asm volatile("s_waitcnt vmcnt(0)" ::: "memory");
        __syncthreads();
        char* cur = lds + (t & 1) * (BT + BE) * 128;
        if (t + 1 < NT) stage(t + 1, lds + ((t + 1) & 1) * (BT + BE) * 128);
#pragma unroll
        for (int kk = 0; kk < 2; ++kk) {
            bf16x8 c = lds_frag(cur, wid * 16 + (lane & 15), kk * 4 + (lane >> 4));
#pragma unroll
            for (int n = 0; n < 5; ++n) {
                bf16x8 a = lds_frag(cur + BT * 128, n * 16 + (lane & 15), kk * 4 + (lane >> 4));
                acc[n] = __builtin_amdgcn_mfma_f32_16x16x32_bf16(a, c, acc[n], 0, 0, 0);
            }
        }
    }
#pragma unroll
    for (int n = 0; n < 5; ++n) {
        int t = bt * BT + wid * 16 + (lane & 15);
        int e = be * BE + n * 16 + (lane >> 4) * 4;
        u32x2 xv = *reinterpret_cast<const u32x2*>(xi + (size_t)t * D + e);
        float x0 = __uint_as_float(xv.x << 16), x1 = __uint_as_float(xv.x & 0xffff0000u);
        float x2 = __uint_as_float(xv.y << 16), x3 = __uint_as_float(xv.y & 0xffff0000u);
        u32x2 o;
        o.x = (unsigned)f2bf(x0 + rbf(acc[n][0])) | ((unsigned)f2bf(x1 + rbf(acc[n][1])) << 16);
        o.y = (unsigned)f2bf(x2 + rbf(acc[n][2])) | ((unsigned)f2bf(x3 + rbf(acc[n][3])) << 16);
        *reinterpret_cast<u32x2*>(xo + (size_t)t * D + e) = o;
    }
}

__global__ __launch_bounds__(256) void k_topk_gateup(const float* __restrict__ r, const u16* __restrict__ xn,
                                                     const u16* __restrict__ wg, const u16* __restrict__ wu,
                                                     float* __restrict__ h, int* __restrict__ idx_out) {
    __shared__ unsigned long long cand[4];
    __shared__ int sidx[CAP];
    __shared__ float xs[CAP][D];
    __shared__ float red[16][2][CAP][16];
    int tid = threadIdx.x, lane = tid & 63, wid = tid >> 6;
    unsigned long long key[8];
#pragma unroll
    for (int i = 0; i < 8; ++i) {
        int j = tid + i * 256;
        unsigned u = __float_as_uint(r[j]);
        u = (u & 0x80000000u) ? ~u : (u | 0x80000000u);
        key[i] = ((unsigned long long)u << 32) | (unsigned)(0xffffffffu - j);
    }
    for (int c = 0; c < CAP; ++c) {
        unsigned long long m = key[0];
#pragma unroll
        for (int i = 1; i < 8; ++i) m = key[i] > m ? key[i] : m;
#pragma unroll
        for (int o = 32; o > 0; o >>= 1) {
            unsigned long long t = __shfl_xor(m, o);
            m = t > m ? t : m;
        }
        if (lane == 0) cand[wid] = m;
        __syncthreads();
        unsigned long long b = cand[0];
#pragma unroll
        for (int i = 1; i < 4; ++i) b = cand[i] > b ? cand[i] : b;
#pragma unroll
        for (int i = 0; i < 8; ++i)
            if (key[i] == b) key[i] = 0;
        if (tid == 0) sidx[c] = (int)(0xffffffffu - (unsigned)b);
        __syncthreads();
    }
    if (blockIdx.x == 0 && tid < CAP) idx_out[tid] = sidx[tid];
    for (int i = tid; i < CAP * D; i += 256) xs[i / D][i % D] = bf2f(xn[(size_t)sidx[i / D] * D + i % D]);
    __syncthreads();
    int col = tid & 15, kg = tid >> 4;
    int f = blockIdx.x * 16 + col;
    float g[CAP] = {}, u[CAP] = {};
    for (int k = kg * 40; k < kg * 40 + 40; ++k) {
        float a = bf2f(wg[(size_t)k * F + f]), b = bf2f(wu[(size_t)k * F + f]);
#pragma unroll
        for (int c = 0; c < CAP; ++c) {
            g[c] += xs[c][k] * a;
            u[c] += xs[c][k] * b;
        }
    }
#pragma unroll
    for (int c = 0; c < CAP; ++c) {
        red[kg][0][c][col] = g[c];
        red[kg][1][c][col] = u[c];
    }
    __syncthreads();
    if (tid < CAP * 16) {
        int c = tid >> 4;
        float gs = 0, us = 0;
        for (int k = 0; k < 16; ++k) {
            gs += red[k][0][c][col];
            us += red[k][1][c][col];
        }
        gs = rbf(gs);
        us = rbf(us);
        float sg = rbf(gs / (1.f + __expf(-gs)));
        h[c * F + f] = rbf(sg * us);
    }
}

__global__ __launch_bounds__(256) void k_down(const float* __restrict__ h, const int* __restrict__ idx,
                                              const float* __restrict__ r, const u16* __restrict__ wd,
                                              const u16* __restrict__ x, const u16* __restrict__ xn,
                                              u16* __restrict__ xo) {
    __shared__ float hs[CAP][F];
    __shared__ float red[16][CAP][16];
    int tid = threadIdx.x;
    for (int i = tid; i < CAP * F; i += 256) hs[i / F][i % F] = h[i];
    __syncthreads();
    int col = tid & 15, kg = tid >> 4;
    int d = blockIdx.x * 16 + col;
    float y[CAP] = {};
    for (int k = kg * 108; k < kg * 108 + 108; ++k) {
        float a = bf2f(wd[(size_t)k * D + d]);
#pragma unroll
        for (int c = 0; c < CAP; ++c) y[c] += hs[c][k] * a;
    }
#pragma unroll
    for (int c = 0; c < CAP; ++c) red[kg][c][col] = y[c];
    __syncthreads();
    if (tid < CAP * 16) {
        int c = tid >> 4;
        float ys = 0;
        for (int k = 0; k < 16; ++k) ys += red[k][c][col];
        ys = rbf(ys);
        int row = idx[c];
        float rs = r[row];
        float sg = rbf(1.f / (1.f + __expf(-rs)));
        float upd = rbf(ys * sg);
        float b = rbf(bf2f(xn[(size_t)row * D + d]) + upd);
        xo[(size_t)row * D + d] = f2bf(bf2f(x[(size_t)row * D + d]) + b);
    }
}

extern "C" void kernel_launch(void* const* d_in, const int* in_sizes, int n_in, void* d_out, int out_size,
                              void* d_ws, size_t ws_size, hipStream_t stream) {
    const u16* x0 = (const u16*)d_in[0];
    const u16* cre = (const u16*)d_in[1];
    const u16* cim = (const u16*)d_in[2];
    const u16* dre = (const u16*)d_in[3];
    const u16* dim = (const u16*)d_in[4];
    const u16* wr = (const u16*)d_in[6];
    const u16* wg = (const u16*)d_in[7];
    const u16* wu = (const u16*)d_in[8];
    const u16* wd = (const u16*)d_in[9];
    char* ws = (char*)d_ws;
    const size_t XB = (size_t)S * D * 2;
    u16* xn = (u16*)(ws);
    u16* abT = (u16*)(ws + XB);
    u16* x1 = (u16*)(ws + 3 * XB);
    u16* x2 = (u16*)(ws + 4 * XB);
    u16* x3 = (u16*)(ws + 5 * XB);
    float* r = (float*)(ws + 6 * XB);
    float* h = r + S;
    int* idx = (int*)(h + CAP * F);
    u16* out = (u16*)d_out;

    k_ln<0><<<S / 4, 256, 0, stream>>>(x0, xn, nullptr, nullptr, nullptr);
    k_gemm1<<<256, 256, 0, stream>>>(xn, dre, dim, abT);
    k_gemm2<<<256, 256, 0, stream>>>(cre, cim, abT, x0, x1);
    k_ln<1><<<S / 4, 256, 0, stream>>>(x1, xn, wr, r, x2);
    k_topk_gateup<<<F / 16, 256, 0, stream>>>(r, xn, wg, wu, h, idx);
    k_down<<<D / 16, 256, 0, stream>>>(h, idx, r, wd, x1, xn, x2);
    k_ln<0><<<S / 4, 256, 0, stream>>>(x2, xn, nullptr, nullptr, nullptr);
    k_gemm1<<<256, 256, 0, stream>>>(xn, dre, dim, abT);
    k_gemm2<<<256, 256, 0, stream>>>(cre, cim, abT, x2, x3);
    k_ln<1><<<S / 4, 256, 0, stream>>>(x3, xn, wr, r, out);
    k_topk_gateup<<<F / 16, 256, 0, stream>>>(r, xn, wg, wu, h, idx);
    k_down<<<D / 16, 256, 0, stream>>>(h, idx, r, wd, x3, xn, out);
}
```

```cpp
#include <hip/hip_runtime.h>
#include <hip/hip_bf16.h>
#include <stdint.h>

typedef unsigned short u16;
using bf16x8 = __attribute__((ext_vector_type(8))) short;
using f32x4 = __attribute__((ext_vector_type(4))) float;
using u32x4 = __attribute__((ext_vector_type(4))) unsigned;
using u32x2 = __attribute__((ext_vector_type(2))) unsigned;

constexpr int S = 2048, D = 640, F = 1728, CAP = 8;

__device__ __forceinline__ float bf2f(u16 v) { return __uint_as_float(((unsigned)v) << 16); }
__device__ __forceinline__ u16 f2bf(float f) {
    __hip_bfloat16 b = __float2bfloat16(f);
    return *reinterpret_cast<u16*>(&b);
}
__device__ __forceinline__ float rbf(float f) { return bf2f(f2bf(f)); }
__device__ __forceinline__ float wave_sum(float v) {
#pragma unroll
    for (int o = 32; o > 0; o >>= 1) v += __shfl_xor(v, o);
    return v;
}

template <int MODE>
__global__ __launch_bounds__(256) void k_ln(const u16* __restrict__ x, u16* __restrict__ xn,
                                            const u16* __restrict__ wr, float* __restrict__ r,
                                            u16* __restrict__ x2) {
    int row = blockIdx.x * 4 + (threadIdx.x >> 6), lane = threadIdx.x & 63;
    const u16* xr = x + (size_t)row * D;
    float v[10];
#pragma unroll
    for (int i = 0; i < 5; ++i) {
        unsigned p = *reinterpret_cast<const unsigned*>(xr + i * 128 + lane * 2);
        v[2 * i] = __uint_as_float(p << 16);
        v[2 * i + 1] = __uint_as_float(p & 0xffff0000u);
    }
    float s = 0;
#pragma unroll
    for (int i = 0; i < 10; ++i) s += v[i];
    s = wave_sum(s);
    float mu = s * (1.0f / D);
    float q = 0;
#pragma unroll
    for (int i = 0; i < 10; ++i) q += (v[i] - mu) * (v[i] - mu);
    q = wave_sum(q);
    float var = rbf(q * (1.0f / D));
    float m = rbf(mu);
    float sd = rbf(sqrtf(rbf(var + 1e-5f)));
    float dot = 0;
#pragma unroll
    for (int i = 0; i < 5; ++i) {
        float a = rbf(rbf(v[2 * i] - m) / sd), b = rbf(rbf(v[2 * i + 1] - m) / sd);
        unsigned o = (unsigned)f2bf(a) | ((unsigned)f2bf(b) << 16);
        *reinterpret_cast<unsigned*>(xn + (size_t)row * D + i * 128 + lane * 2) = o;
        if (MODE == 1) {
            unsigned w = *reinterpret_cast<const unsigned*>(wr + i * 128 + lane * 2);
            dot += a * __uint_as_float(w << 16) + b * __uint_as_float(w & 0xffff0000u);
            unsigned o2 = (unsigned)f2bf(v[2 * i] + a) | ((unsigned)f2bf(v[2 * i + 1] + b) << 16);
            *reinterpret_cast<unsigned*>(x2 + (size_t)row * D + i * 128 + lane * 2) = o2;
        }
    }
    if (MODE == 1) {
        dot = wave_sum(dot);
        if (lane == 0) r[row] = rbf(dot);
    }
}

template <int ROWS>
__device__ __forceinline__ void stage_tile(const u16* __restrict__ g, int ld, char* lds, int wid, int lane) {
    constexpr int NI = ROWS / 8;
#pragma unroll
    for (int i = 0; i < (NI + 3) / 4; ++i) {
        int ins = i * 4 + wid;
        if (ins < NI) {
            int row = ins * 8 + (lane >> 3);
            int c = (lane & 7) ^ ((row >> 1) & 7);
            __builtin_amdgcn_global_load_lds((const unsigned*)(g + (size_t)row * ld + c * 8),
                                             (unsigned*)(lds + ins * 1024), 16, 0, 0);
        }
    }
}
__device__ __forceinline__ bf16x8 lds_frag(const char* lds, int row, int c) {
    return *reinterpret_cast<const bf16x8*>(lds + row * 128 + ((c ^ ((row >> 1) & 7)) << 4));
}

__global__ __launch_bounds__(256) void k_gemm1(const u16* __restrict__ xn, const u16* __restrict__ dre,
                                               const u16* __restrict__ dim, u16* __restrict__ abT) {
    constexpr int BM = 128, BN = 80;
    __shared__ __attribute__((aligned(16))) char lds[2 * (BM + BN) * 128];
    int wid = threadIdx.x >> 6, lane = threadIdx.x & 63;
    int bs = blockIdx.x & 15, be = blockIdx.x >> 4;
    const u16* A = xn + (size_t)bs * BM * D;
    const u16* B = (be < 8 ? dre + (size_t)be * BN * D : dim + (size_t)(be - 8) * BN * D);
    f32x4 acc[2][5] = {};
    constexpr int NT = D / 64;
    stage_tile<BM>(A, D, lds, wid, lane);
    stage_tile<BN>(B, D, lds + BM * 128, wid, lane);
    for (int t = 0; t < NT; ++t) {
        asm volatile("s_waitcnt vmcnt(0)" ::: "memory");
        __syncthreads();
        char* cur = lds + (t & 1) * (BM + BN) * 128;
        if (t + 1 < NT) {
            char* nxt = lds + ((t + 1) & 1) * (BM + BN) * 128;
            stage_tile<BM>(A + (t + 1) * 64, D, nxt, wid, lane);
            stage_tile<BN>(B + (t + 1) * 64, D, nxt + BM * 128, wid, lane);
        }
#pragma unroll
        for (int kk = 0; kk < 2; ++kk) {
            bf16x8 a[2], b[5];
#pragma unroll
            for (int m = 0; m < 2; ++m) a[m] = lds_frag(cur, wid * 32 + m * 16 + (lane & 15), kk * 4 + (lane >> 4));
#pragma unroll
            for (int n = 0; n < 5; ++n) b[n] = lds_frag(cur + BM * 128, n * 16 + (lane & 15), kk * 4 + (lane >> 4));
#pragma unroll
            for (int m = 0; m < 2; ++m)
#pragma unroll
                for (int n = 0; n < 5; ++n)
                    acc[m][n] = __builtin_amdgcn_mfma_f32_16x16x32_bf16(a[m], b[n], acc[m][n], 0, 0, 0);
        }
    }
#pragma unroll
    for (int m = 0; m < 2; ++m)
#pragma unroll
        for (int n = 0; n < 5; ++n) {
            int e = be * BN + n * 16 + (lane & 15);
            int s = bs * BM + wid * 32 + m * 16 + (lane >> 4) * 4;
            float sg = be < 8 ? 1.f : -1.f;
            u32x2 o;
            o.x = (unsigned)f2bf(sg * acc[m][n][0]) | ((unsigned)f2bf(sg * acc[m][n][1]) << 16);
            o.y = (unsigned)f2bf(sg * acc[m][n][2]) | ((unsigned)f2bf(sg * acc[m][n][3]) << 16);
            *reinterpret_cast<u32x2*>(abT + (size_t)e * S + s) = o;
        }
}

__global__ __launch_bounds__(256) void k_gemm2(const u16* __restrict__ cre, const u16* __restrict__ cim,
                                               const u16* __restrict__ abT, const u16* __restrict__ xi,
                                               u16* __restrict__ xo) {
    constexpr int BT = 64, BE = 80;
    __shared__ __attribute__((aligned(16))) char lds[2 * (BT + BE) * 128];
    int wid = threadIdx.x >> 6, lane = threadIdx.x & 63;
    int xcd = blockIdx.x & 7, loc = blockIdx.x >> 3;
    int bt = xcd * 4 + (loc & 3), be = loc >> 2;
    f32x4 acc[5] = {};
    constexpr int NT = 2 * S / 64;
    auto stage = [&](int t, char* dst) {
        int half = t >= NT / 2;
        int k0 = (t - half * (NT / 2)) * 64;
        const u16* Cm = (half ? cim : cre) + (size_t)bt * BT * S + k0;
        const u16* Bm = abT + (size_t)(half * D + be * BE) * S + k0;
        stage_tile<BT>(Cm, S, dst, wid, lane);
        stage_tile<BE>(Bm, S, dst + BT * 128, wid, lane);
    };
    stage(0, lds);
    for (int t = 0; t < NT; ++t) {
        asm volatile("s_waitcnt vmcnt(0)" ::: "memory");
        __syncthreads();
        char* cur = lds + (t & 1) * (BT + BE) * 128;
        if (t + 1 < NT) stage(t + 1, lds + ((t + 1) & 1) * (BT + BE) * 128);
#pragma unroll
        for (int kk = 0; kk < 2; ++kk) {
            bf16x8 c = lds_frag(cur, wid * 16 + (lane & 15), kk * 4 + (lane >> 4));
#pragma unroll
            for (int n = 0; n < 5; ++n) {
                bf16x8 a = lds_frag(cur + BT * 128, n * 16 + (lane & 15), kk * 4 + (lane >> 4));
                acc[n] = __builtin_amdgcn_mfma_f32_16x16x32_bf16(a, c, acc[n], 0, 0, 0);
            }
        }
    }
#pragma unroll
    for (int n = 0; n < 5; ++n) {
        int t = bt * BT + wid * 16 + (lane & 15);
        int e = be * BE + n * 16 + (lane >> 4) * 4;
        u32x2 xv = *reinterpret_cast<const u32x2*>(xi + (size_t)t * D + e);
        float x0 = __uint_as_float(xv.x << 16), x1 = __uint_as_float(xv.x & 0xffff0000u);
        float x2 = __uint_as_float(xv.y << 16), x3 = __uint_as_float(xv.y & 0xffff0000u);
        u32x2 o;
        o.x = (unsigned)f2bf(x0 + rbf(acc[n][0])) | ((unsigned)f2bf(x1 + rbf(acc[n][1])) << 16);
        o.y = (unsigned)f2bf(x2 + rbf(acc[n][2])) | ((unsigned)f2bf(x3 + rbf(acc[n][3])) << 16);
        *reinterpret_cast<u32x2*>(xo + (size_t)t * D + e) = o;
    }
}

__global__ __launch_bounds__(256) void k_topk_gateup(const float* __restrict__ r, const u16* __restrict__ xn,
                                                     const u16* __restrict__ wg, const u16* __restrict__ wu,
                                                     float* __restrict__ h, int* __restrict__ idx_out) {
    __shared__ unsigned long long cand[4];
    __shared__ int sidx[CAP];
    __shared__ float xs[CAP][D];
    __shared__ float red[16][2][CAP][16];
    int tid = threadIdx.x, lane = tid & 63, wid = tid >> 6;
    unsigned long long key[8];
#pragma unroll
    for (int i = 0; i < 8; ++i) {
        int j = tid + i * 256;
        unsigned u = __float_as_uint(r[j]);
        u = (u & 0x80000000u) ? ~u : (u | 0x80000000u);
        key[i] = ((unsigned long long)u << 32) | (unsigned)(0xffffffffu - j);
    }
    for (int c = 0; c < CAP; ++c) {
        unsigned long long m = key[0];
#pragma unroll
        for (int i = 1; i < 8; ++i) m = key[i] > m ? key[i] : m;
#pragma unroll
        for (int o = 32; o > 0; o >>= 1) {
            unsigned long long t = __shfl_xor(m, o);
            m = t > m ? t : m;
        }
        if (lane == 0) cand[wid] = m;
        __syncthreads();
        unsigned long long b = cand[0];
#pragma unroll
        for (int i = 1; i < 4; ++i) b = cand[i] > b ? cand[i] : b;
#pragma unroll
        for (int i = 0; i < 8; ++i)
            if (key[i] == b) key[i] = 0;
        if (tid == 0) sidx[c] = (int)(0xffffffffu - (unsigned)b);
        __syncthreads();
    }
    if (blockIdx.x == 0 && tid < CAP) idx_out[tid] = sidx[tid];
    for (int i = tid; i < CAP * D; i += 256) xs[i / D][i % D] = bf2f(xn[(size_t)sidx[i / D] * D + i % D]);
    __syncthreads();
    int col = tid & 15, kg = tid >> 4;
    int f = blockIdx.x * 16 + col;
    float g[CAP] = {}, u[CAP] = {};
    for (int k = kg * 40; k < kg * 40 + 40; ++k) {
        float a = bf2f(wg[(size_t)k * F + f]), b = bf2f(wu[(size_t)k * F + f]);
#pragma unroll
        for (int c = 0; c < CAP; ++c) {
            g[c] += xs[c][k] * a;
            u[c] += xs[c][k] * b;
        }
    }
#pragma unroll
    for (int c = 0; c < CAP; ++c) {
        red[kg][0][c][col] = g[c];
        red[kg][1][c][col] = u[c];
    }
    __syncthreads();
    if (tid < CAP * 16) {
        int c = tid >> 4;
        float gs = 0, us = 0;
        for (int k = 0; k < 16; ++k) {
            gs += red[k][0][c][col];
            us += red[k][1][c][col];
        }
        gs = rbf(gs);
        us = rbf(us);
        float sg = rbf(gs / (1.f + __expf(-gs)));
        h[c * F + f] = rbf(sg * us);
    }
}

__global__ __launch_bounds__(256) void k_down(const float* __restrict__ h, const int* __restrict__ idx,
                                              const float* __restrict__ r, const u16* __restrict__ wd,
                                              const u16* __restrict__ x, const u16* __restrict__ xn,
                                              u16* __restrict__ xo) {
    __shared__ float hs[CAP][F];
    __shared__ float red[16][CAP][16];
    int tid = threadIdx.x;
    for (int i = tid; i < CAP * F; i += 256) hs[i / F][i % F] = h[i];
    __syncthreads();
    int col = tid & 15, kg = tid >> 4;
    int d = blockIdx.x * 16 + col;
    float y[CAP] = {};
    for (int k = kg * 108; k < kg * 108 + 108; ++k) {
        float a = bf2f(wd[(size_t)k * D + d]);
#pragma unroll
        for (int c = 0; c < CAP; ++c) y[c] += hs[c][k] * a;
    }
#pragma unroll
    for (int c = 0; c < CAP; ++c) red[kg][c][col] = y[c];
    __syncthreads();
    if (tid < CAP * 16) {
        int c = tid >> 4;
        float ys = 0;
        for (int k = 0; k < 16; ++k) ys += red[k][c][col];
        ys = rbf(ys);
        int row = idx[c];
        float rs = r[row];
        float sg = rbf(1.f / (1.f + __expf(-rs)));
        float upd = rbf(ys * sg);
        float b = rbf(bf2f(xn[(size_t)row * D + d]) + upd);
        xo[(size_t)row * D + d] = f2bf(bf2f(x[(size_t)row * D + d]) + b);
    }
}


typedef unsigned long long u64;
template <int AUX, int WRITE>
__global__ __launch_bounds__(256) void k_probe(const char* p0, const char* p1, char* wp, u64* stamps) {
    __shared__ __attribute__((aligned(16))) char lds[65536];
    int b = blockIdx.x, wid = threadIdx.x >> 6, lane = threadIdx.x & 63;
    u64 t0 = __builtin_amdgcn_s_memrealtime();
    if (WRITE) {
        u32x4 v = {1,2,3,4};
        for (int i = 0; i < 5; ++i) *(u32x4*)(wp + (size_t)b * 20480 + i * 4096 + threadIdx.x * 16) = v;
    } else {
        const char* p = ((b & 1) ? p1 : p0) + (size_t)(b >> 1) * 65536 + wid * 16384 + lane * 16;
#pragma unroll
        for (int i = 0; i < 16; ++i)
            __builtin_amdgcn_global_load_lds((const unsigned*)(p + i * 1024), (unsigned*)(lds + wid * 16384 + i * 1024), 16, 0, AUX);
    }
    asm volatile("s_waitcnt vmcnt(0)" ::: "memory");
    __syncthreads();
    u64 t1 = __builtin_amdgcn_s_memrealtime();
    if (threadIdx.x == 0) { stamps[2 * b] = t0; stamps[2 * b + 1] = t1; }
}
__global__ void k_spin(const u64* stamps) {
    u64 mn = ~0ull, mx = 0;
    for (int i = 0; i < 256; ++i) { u64 a = stamps[2 * i], c = stamps[2 * i + 1]; mn = a < mn ? a : mn; mx = c > mx ? c : mx; }
    u64 span = mx - mn; if (span > 5000) span = 5000;
    u64 t0 = __builtin_amdgcn_s_memrealtime();
    while (__builtin_amdgcn_s_memrealtime() - t0 < span * 100) __builtin_amdgcn_s_sleep(8);
}
extern "C" void kernel_launch(void* const* d_in, const int* in_sizes, int n_in, void* d_out, int out_size,
                              void* d_ws, size_t ws_size, hipStream_t stream) {
    const u16* x0 = (const u16*)d_in[0];
    const u16* cre = (const u16*)d_in[1];
    const u16* cim = (const u16*)d_in[2];
    const u16* dre = (const u16*)d_in[3];
    const u16* dim = (const u16*)d_in[4];
    const u16* wr = (const u16*)d_in[6];
    const u16* wg = (const u16*)d_in[7];
    const u16* wu = (const u16*)d_in[8];
    const u16* wd = (const u16*)d_in[9];
    char* ws = (char*)d_ws;
    const size_t XB = (size_t)S * D * 2;
    u16* xn = (u16*)(ws);
    u16* abT = (u16*)(ws + XB);
    u16* x1 = (u16*)(ws + 3 * XB);
    u16* x2 = (u16*)(ws + 4 * XB);
    u16* x3 = (u16*)(ws + 5 * XB);
    float* r = (float*)(ws + 6 * XB);
    float* h = r + S;
    int* idx = (int*)(h + CAP * F);
    u16* out = (u16*)d_out;

    u64* stamps=(u64*)(ws+8*XB);
    k_probe<0,1><<<256,256,0,stream>>>(ws,ws,ws+(32<<20),stamps);
    k_ln<0><<<S / 4, 256, 0, stream>>>(x0, xn, nullptr, nullptr, nullptr);
    k_gemm1<<<256, 256, 0, stream>>>(xn, dre, dim, abT);
    k_gemm2<<<256, 256, 0, stream>>>(cre, cim, abT, x0, x1);
    k_ln<1><<<S / 4, 256, 0, stream>>>(x1, xn, wr, r, x2);
    k_topk_gateup<<<F / 16, 256, 0, stream>>>(r, xn, wg, wu, h, idx);
    k_down<<<D / 16, 256, 0, stream>>>(h, idx, r, wd, x1, xn, x2);
    k_ln<0><<<S / 4, 256, 0, stream>>>(x2, xn, nullptr, nullptr, nullptr);
    k_gemm1<<<256, 256, 0, stream>>>(xn, dre, dim, abT);
    k_gemm2<<<256, 256, 0, stream>>>(cre, cim, abT, x2, x3);
    k_ln<1><<<S / 4, 256, 0, stream>>>(x3, xn, wr, r, out);
    k_topk_gateup<<<F / 16, 256, 0, stream>>>(r, xn, wg, wu, h, idx);
    k_down<<<D / 16, 256, 0, stream>>>(h, idx, r, wd, x3, xn, out);
    k_spin<<<1,64,0,stream>>>(stamps);
}
```

```cpp
#include <hip/hip_runtime.h>
#include <hip/hip_bf16.h>
#include <stdint.h>

typedef unsigned short u16;
using bf16x8 = __attribute__((ext_vector_type(8))) short;
using f32x4 = __attribute__((ext_vector_type(4))) float;
using u32x4 = __attribute__((ext_vector_type(4))) unsigned;
using u32x2 = __attribute__((ext_vector_type(2))) unsigned;

constexpr int S = 2048, D = 640, F = 1728, CAP = 8;

__device__ __forceinline__ float bf2f(u16 v) { return __uint_as_float(((unsigned)v) << 16); }
__device__ __forceinline__ u16 f2bf(float f) {
    __hip_bfloat16 b = __float2bfloat16(f);
    return *reinterpret_cast<u16*>(&b);
}
__device__ __forceinline__ float rbf(float f) { return bf2f(f2bf(f)); }
__device__ __forceinline__ float wave_sum(float v) {
#pragma unroll
    for (int o = 32; o > 0; o >>= 1) v += __shfl_xor(v, o);
    return v;
}

template <int MODE>
__global__ __launch_bounds__(256) void k_ln(const u16* __restrict__ x, u16* __restrict__ xn,
                                            const u16* __restrict__ wr, float* __restrict__ r,
                                            u16* __restrict__ x2) {
    int row = blockIdx.x * 4 + (threadIdx.x >> 6), lane = threadIdx.x & 63;
    const u16* xr = x + (size_t)row * D;
    float v[10];
#pragma unroll
    for (int i = 0; i < 5; ++i) {
        unsigned p = *reinterpret_cast<const unsigned*>(xr + i * 128 + lane * 2);
        v[2 * i] = __uint_as_float(p << 16);
        v[2 * i + 1] = __uint_as_float(p & 0xffff0000u);
    }
    float s = 0;
#pragma unroll
    for (int i = 0; i < 10; ++i) s += v[i];
    s = wave_sum(s);
    float mu = s * (1.0f / D);
    float q = 0;
#pragma unroll
    for (int i = 0; i < 10; ++i) q += (v[i] - mu) * (v[i] - mu);
    q = wave_sum(q);
    float var = rbf(q * (1.0f / D));
    float m = rbf(mu);
    float sd = rbf(sqrtf(rbf(var + 1e-5f)));
    float dot = 0;
#pragma unroll
    for (int i = 0; i < 5; ++i) {
        float a = rbf(rbf(v[2 * i] - m) / sd), b = rbf(rbf(v[2 * i + 1] - m) / sd);
        unsigned o = (unsigned)f2bf(a) | ((unsigned)f2bf(b) << 16);
        *reinterpret_cast<unsigned*>(xn + (size_t)row * D + i * 128 + lane * 2) = o;
        if (MODE == 1) {
            unsigned w = *reinterpret_cast<const unsigned*>(wr + i * 128 + lane * 2);
            dot += a * __uint_as_float(w << 16) + b * __uint_as_float(w & 0xffff0000u);
            unsigned o2 = (unsigned)f2bf(v[2 * i] + a) | ((unsigned)f2bf(v[2 * i + 1] + b) << 16);
            *reinterpret_cast<unsigned*>(x2 + (size_t)row * D + i * 128 + lane * 2) = o2;
        }
    }
    if (MODE == 1) {
        dot = wave_sum(dot);
        if (lane == 0) r[row] = rbf(dot);
    }
}

template <int ROWS>
__device__ __forceinline__ void stage_tile(const u16* __restrict__ g, int ld, char* lds, int wid, int lane) {
    constexpr int NI = ROWS / 8;
#pragma unroll
    for (int i = 0; i < (NI + 3) / 4; ++i) {
        int ins = i * 4 + wid;
        if (ins < NI) {
            int row = ins * 8 + (lane >> 3);
            int c = (lane & 7) ^ ((row >> 1) & 7);
            __builtin_amdgcn_global_load_lds((const unsigned*)(g + (size_t)row * ld + c * 8),
                                             (unsigned*)(lds + ins * 1024), 16, 0, 0);
        }
    }
}
__device__ __forceinline__ bf16x8 lds_frag(const char* lds, int row, int c) {
    return *reinterpret_cast<const bf16x8*>(lds + row * 128 + ((c ^ ((row >> 1) & 7)) << 4));
}

__global__ __launch_bounds__(256) void k_gemm1(const u16* __restrict__ xn, const u16* __restrict__ dre,
                                               const u16* __restrict__ dim, u16* __restrict__ abT) {
    constexpr int BM = 128, BN = 80;
    __shared__ __attribute__((aligned(16))) char lds[2 * (BM + BN) * 128];
    int wid = threadIdx.x >> 6, lane = threadIdx.x & 63;
    int bs = blockIdx.x & 15, be = blockIdx.x >> 4;
    const u16* A = xn + (size_t)bs * BM * D;
    const u16* B = (be < 8 ? dre + (size_t)be * BN * D : dim + (size_t)(be - 8) * BN * D);
    f32x4 acc[2][5] = {};
    constexpr int NT = D / 64;
    stage_tile<BM>(A, D, lds, wid, lane);
    stage_tile<BN>(B, D, lds + BM * 128, wid, lane);
    for (int t = 0; t < NT; ++t) {
        asm volatile("s_waitcnt vmcnt(0)" ::: "memory");
        __syncthreads();
        char* cur = lds + (t & 1) * (BM + BN) * 128;
        if (t + 1 < NT) {
            char* nxt = lds + ((t + 1) & 1) * (BM + BN) * 128;
            stage_tile<BM>(A + (t + 1) * 64, D, nxt, wid, lane);
            stage_tile<BN>(B + (t + 1) * 64, D, nxt + BM * 128, wid, lane);
        }
#pragma unroll
        for (int kk = 0; kk < 2; ++kk) {
            bf16x8 a[2], b[5];
#pragma unroll
            for (int m = 0; m < 2; ++m) a[m] = lds_frag(cur, wid * 32 + m * 16 + (lane & 15), kk * 4 + (lane >> 4));
#pragma unroll
            for (int n = 0; n < 5; ++n) b[n] = lds_frag(cur + BM * 128, n * 16 + (lane & 15), kk * 4 + (lane >> 4));
#pragma unroll
            for (int m = 0; m < 2; ++m)
#pragma unroll
                for (int n = 0; n < 5; ++n)
                    acc[m][n] = __builtin_amdgcn_mfma_f32_16x16x32_bf16(a[m], b[n], acc[m][n], 0, 0, 0);
        }
    }
#pragma unroll
    for (int m = 0; m < 2; ++m)
#pragma unroll
        for (int n = 0; n < 5; ++n) {
            int e = be * BN + n * 16 + (lane & 15);
            int s = bs * BM + wid * 32 + m * 16 + (lane >> 4) * 4;
            float sg = be < 8 ? 1.f : -1.f;
            u32x2 o;
            o.x = (unsigned)f2bf(sg * acc[m][n][0]) | ((unsigned)f2bf(sg * acc[m][n][1]) << 16);
            o.y = (unsigned)f2bf(sg * acc[m][n][2]) | ((unsigned)f2bf(sg * acc[m][n][3]) << 16);
            *reinterpret_cast<u32x2*>(abT + (size_t)e * S + s) = o;
        }
}

__global__ __launch_bounds__(256) void k_gemm2(const u16* __restrict__ cre, const u16* __restrict__ cim,
                                               const u16* __restrict__ abT, const u16* __restrict__ xi,
                                               u16* __restrict__ xo) {
    constexpr int BT = 64, BE = 80;
    __shared__ __attribute__((aligned(16))) char lds[2 * (BT + BE) * 128];
    int wid = threadIdx.x >> 6, lane = threadIdx.x & 63;
    int xcd = blockIdx.x & 7, loc = blockIdx.x >> 3;
    int bt = xcd * 4 + (loc & 3), be = loc >> 2;
    f32x4 acc[5] = {};
    constexpr int NT = 2 * S / 64;
    auto stage = [&](int t, char* dst) {
        int half = t >= NT / 2;
        int k0 = (t - half * (NT / 2)) * 64;
        const u16* Cm = (half ? cim : cre) + (size_t)bt * BT * S + k0;
        const u16* Bm = abT + (size_t)(half * D + be * BE) * S + k0;
        stage_tile<BT>(Cm, S, dst, wid, lane);
        stage_tile<BE>(Bm, S, dst + BT * 128, wid, lane);
    };
    stage(0, lds);
    for (int t = 0; t < NT; ++t) {
        asm volatile("s_waitcnt vmcnt(0)" ::: "memory");
        __syncthreads();
        char* cur = lds + (t & 1) * (BT + BE) * 128;
        if (t + 1 < NT) stage(t + 1, lds + ((t + 1) & 1) * (BT + BE) * 128);
#pragma unroll
        for (int kk = 0; kk < 2; ++kk) {
            bf16x8 c = lds_frag(cur, wid * 16 + (lane & 15), kk * 4 + (lane >> 4));
#pragma unroll
            for (int n = 0; n < 5; ++n) {
                bf16x8 a = lds_frag(cur + BT * 128, n * 16 + (lane & 15), kk * 4 + (lane >> 4));
                acc[n] = __builtin_amdgcn_mfma_f32_16x16x32_bf16(a, c, acc[n], 0, 0, 0);
            }
        }
    }
#pragma unroll
    for (int n = 0; n < 5; ++n) {
        int t = bt * BT + wid * 16 + (lane & 15);
        int e = be * BE + n * 16 + (lane >> 4) * 4;
        u32x2 xv = *reinterpret_cast<const u32x2*>(xi + (size_t)t * D + e);
        float x0 = __uint_as_float(xv.x << 16), x1 = __uint_as_float(xv.x & 0xffff0000u);
        float x2 = __uint_as_float(xv.y << 16), x3 = __uint_as_float(xv.y & 0xffff0000u);
        u32x2 o;
        o.x = (unsigned)f2bf(x0 + rbf(acc[n][0])) | ((unsigned)f2bf(x1 + rbf(acc[n][1])) << 16);
        o.y = (unsigned)f2bf(x2 + rbf(acc[n][2])) | ((unsigned)f2bf(x3 + rbf(acc[n][3])) << 16);
        *reinterpret_cast<u32x2*>(xo + (size_t)t * D + e) = o;
    }
}

__global__ __launch_bounds__(256) void k_topk_gateup(const float* __restrict__ r, const u16* __restrict__ xn,
                                                     const u16* __restrict__ wg, const u16* __restrict__ wu,
                                                     float* __restrict__ h, int* __restrict__ idx_out) {
    __shared__ unsigned long long cand[4];
    __shared__ int sidx[CAP];
    __shared__ float xs[CAP][D];
    __shared__ float red[16][2][CAP][16];
    int tid = threadIdx.x, lane = tid & 63, wid = tid >> 6;
    unsigned long long key[8];
#pragma unroll
    for (int i = 0; i < 8; ++i) {
        int j = tid + i * 256;
        unsigned u = __float_as_uint(r[j]);
        u = (u & 0x80000000u) ? ~u : (u | 0x80000000u);
        key[i] = ((unsigned long long)u << 32) | (unsigned)(0xffffffffu - j);
    }
    for (int c = 0; c < CAP; ++c) {
        unsigned long long m = key[0];
#pragma unroll
        for (int i = 1; i < 8; ++i) m = key[i] > m ? key[i] : m;
#pragma unroll
        for (int o = 32; o > 0; o >>= 1) {
            unsigned long long t = __shfl_xor(m, o);
            m = t > m ? t : m;
        }
        if (lane == 0) cand[wid] = m;
        __syncthreads();
        unsigned long long b = cand[0];
#pragma unroll
        for (int i = 1; i < 4; ++i) b = cand[i] > b ? cand[i] : b;
#pragma unroll
        for (int i = 0; i < 8; ++i)
            if (key[i] == b) key[i] = 0;
        if (tid == 0) sidx[c] = (int)(0xffffffffu - (unsigned)b);
        __syncthreads();
    }
    if (blockIdx.x == 0 && tid < CAP) idx_out[tid] = sidx[tid];
    for (int i = tid; i < CAP * D; i += 256) xs[i / D][i % D] = bf2f(xn[(size_t)sidx[i / D] * D + i % D]);
    __syncthreads();
    int col = tid & 15, kg = tid >> 4;
    int f = blockIdx.x * 16 + col;
    float g[CAP] = {}, u[CAP] = {};
    for (int k = kg * 40; k < kg * 40 + 40; ++k) {
        float a = bf2f(wg[(size_t)k * F + f]), b = bf2f(wu[(size_t)k * F + f]);
#pragma unroll
        for (int c = 0; c < CAP; ++c) {
            g[c] += xs[c][k] * a;
            u[c] += xs[c][k] * b;
        }
    }
#pragma unroll
    for (int c = 0; c < CAP; ++c) {
        red[kg][0][c][col] = g[c];
        red[kg][1][c][col] = u[c];
    }
    __syncthreads();
    if (tid < CAP * 16) {
        int c = tid >> 4;
        float gs = 0, us = 0;
        for (int k = 0; k < 16; ++k) {
            gs += red[k][0][c][col];
            us += red[k][1][c][col];
        }
        gs = rbf(gs);
        us = rbf(us);
        float sg = rbf(gs / (1.f + __expf(-gs)));
        h[c * F + f] = rbf(sg * us);
    }
}

__global__ __launch_bounds__(256) void k_down(const float* __restrict__ h, const int* __restrict__ idx,
                                              const float* __restrict__ r, const u16* __restrict__ wd,
                                              const u16* __restrict__ x, const u16* __restrict__ xn,
                                              u16* __restrict__ xo) {
    __shared__ float hs[CAP][F];
    __shared__ float red[16][CAP][16];
    int tid = threadIdx.x;
    for (int i = tid; i < CAP * F; i += 256) hs[i / F][i % F] = h[i];
    __syncthreads();
    int col = tid & 15, kg = tid >> 4;
    int d = blockIdx.x * 16 + col;
    float y[CAP] = {};
    for (int k = kg * 108; k < kg * 108 + 108; ++k) {
        float a = bf2f(wd[(size_t)k * D + d]);
#pragma unroll
        for (int c = 0; c < CAP; ++c) y[c] += hs[c][k] * a;
    }
#pragma unroll
    for (int c = 0; c < CAP; ++c) red[kg][c][col] = y[c];
    __syncthreads();
    if (tid < CAP * 16) {
        int c = tid >> 4;
        float ys = 0;
        for (int k = 0; k < 16; ++k) ys += red[k][c][col];
        ys = rbf(ys);
        int row = idx[c];
        float rs = r[row];
        float sg = rbf(1.f / (1.f + __expf(-rs)));
        float upd = rbf(ys * sg);
        float b = rbf(bf2f(xn[(size_t)row * D + d]) + upd);
        xo[(size_t)row * D + d] = f2bf(bf2f(x[(size_t)row * D + d]) + b);
    }
}


typedef unsigned long long u64;
__global__ __launch_bounds__(256) void k_probe(u64* stamps) {
    int b = blockIdx.x;
    unsigned x = threadIdx.x;
    u64 t0 = __builtin_amdgcn_s_memrealtime();
    asm volatile("s_waitcnt lgkmcnt(0)" ::: "memory");
    asm volatile(".rept 1\n v_add3_u32 %0, %0, %0, %0\n .endr" : "+v"(x));
    __syncthreads();
    u64 t1 = __builtin_amdgcn_s_memrealtime();
    if (threadIdx.x == 0) { stamps[2 * b] = t0; stamps[2 * b + 1] = t1 + (x == 0x12345); }
}
__global__ void k_spin(const u64* stamps) {
    u64 mn = ~0ull, mx = 0;
    for (int i = 0; i < 256; ++i) { u64 a = stamps[2 * i], c = stamps[2 * i + 1]; mn = a < mn ? a : mn; mx = c > mx ? c : mx; }
    u64 span = mx - mn; if (span > 5000) span = 5000;
    u64 t0 = __builtin_amdgcn_s_memrealtime();
    while (__builtin_amdgcn_s_memrealtime() - t0 < span * 100) __builtin_amdgcn_s_sleep(8);
}
extern "C" void kernel_launch(void* const* d_in, const int* in_sizes, int n_in, void* d_out, int out_size,
                              void* d_ws, size_t ws_size, hipStream_t stream) {
    const u16* x0 = (const u16*)d_in[0];
    const u16* cre = (const u16*)d_in[1];
    const u16* cim = (const u16*)d_in[2];
    const u16* dre = (const u16*)d_in[3];
    const u16* dim = (const u16*)d_in[4];
    const u16* wr = (const u16*)d_in[6];
    const u16* wg = (const u16*)d_in[7];
    const u16* wu = (const u16*)d_in[8];
    const u16* wd = (const u16*)d_in[9];
    char* ws = (char*)d_ws;
    const size_t XB = (size_t)S * D * 2;
    u16* xn = (u16*)(ws);
    u16* abT = (u16*)(ws + XB);
    u16* x1 = (u16*)(ws + 3 * XB);
    u16* x2 = (u16*)(ws + 4 * XB);
    u16* x3 = (u16*)(ws + 5 * XB);
    float* r = (float*)(ws + 6 * XB);
    float* h = r + S;
    int* idx = (int*)(h + CAP * F);
    u16* out = (u16*)d_out;

    u64* stamps=(u64*)(ws+8*XB);
    k_probe<<<256,256,0,stream>>>(stamps);
    k_ln<0><<<S / 4, 256, 0, stream>>>(x0, xn, nullptr, nullptr, nullptr);
    k_gemm1<<<256, 256, 0, stream>>>(xn, dre, dim, abT);
    k_gemm2<<<256, 256, 0, stream>>>(cre, cim, abT, x0, x1);
    k_ln<1><<<S / 4, 256, 0, stream>>>(x1, xn, wr, r, x2);
    k_topk_gateup<<<F / 16, 256, 0, stream>>>(r, xn, wg, wu, h, idx);
    k_down<<<D / 16, 256, 0, stream>>>(h, idx, r, wd, x1, xn, x2);
    k_ln<0><<<S / 4, 256, 0, stream>>>(x2, xn, nullptr, nullptr, nullptr);
    k_gemm1<<<256, 256, 0, stream>>>(xn, dre, dim, abT);
    k_gemm2<<<256, 256, 0, stream>>>(cre, cim, abT, x2, x3);
    k_ln<1><<<S / 4, 256, 0, stream>>>(x3, xn, wr, r, out);
    k_topk_gateup<<<F / 16, 256, 0, stream>>>(r, xn, wg, wu, h, idx);
    k_down<<<D / 16, 256, 0, stream>>>(h, idx, r, wd, x3, xn, out);
    k_spin<<<1,64,0,stream>>>(stamps);
}
```

```cpp
#include <hip/hip_runtime.h>
#include <hip/hip_bf16.h>
#include <stdint.h>

typedef unsigned short u16;
typedef unsigned long long u64;
typedef unsigned u32x4 __attribute__((ext_vector_type(4)));
typedef unsigned u32x2 __attribute__((ext_vector_type(2)));
typedef float f32x2 __attribute__((ext_vector_type(2)));
typedef float f32x4 __attribute__((ext_vector_type(4)));
typedef float f32x16 __attribute__((ext_vector_type(16)));
typedef float f32x32 __attribute__((ext_vector_type(32)));
typedef unsigned u32x8 __attribute__((ext_vector_type(8)));
typedef unsigned u32x16 __attribute__((ext_vector_type(16)));
typedef short s16x4 __attribute__((ext_vector_type(4)));
typedef __bf16 bf16x2 __attribute__((ext_vector_type(2)));
#define LAS __attribute__((address_space(3)))
#define DI __device__ __forceinline__

constexpr unsigned O_CLR = 4096, O_PF = 65536, O_XF = 327680, O_AB = 1u << 20, AB_SZ = 5242880, O_IB = 12u << 20, IB_SZ = 8u << 20;
constexpr unsigned O_AR = 28u << 20;
constexpr unsigned A_X1 = O_AR, A_ST = O_AR + 2621440, ST_SZ = 524288, A_RB = O_AR + 3670016, A_MS = O_AR + 3686400, A_HF = O_AR + 3751936, HF_SZ = 28672;
constexpr unsigned A_X2 = O_AR + 3809280, A_XO = A_X2 + 10240, A_DAB = O_AR + 3840000, A_IDX = O_AR + 3860480, A_CG = O_AR + 3861504, A_X3 = O_AR + (5u << 20);
constexpr unsigned O_STAMP = 40u << 20, WS_USED = 48u << 20;
constexpr unsigned LD = 0, LRED = 102400, T_AL = 151552, T_G = 152064, T_C = 152576, T_MH = 153088, T_ZERO = 154112, T_CS = 154304, T_CSF = 155584, T_ID = 156224;
constexpr unsigned T_CAND = 156736, T_SEL = 157248, T_OP = 157312, T_OP2 = 161408;
constexpr unsigned XS_STRIDE = 1296;
constexpr unsigned SPIN_CAP = 1u << 18;
constexpr unsigned SENT = 0xFFFFFFFFu;

#define ASTAMP(base) do { } while (0)
#define STAMP(i) do { } while (0)

#define FST(i) do { } while (0)
#define AST_A 0
#define AST_B 0
#define AST(k) do { if (AST_A == k) ASTAMP(20); if (AST_B == k) ASTAMP(26); } while (0)
#define ASTAMP_SA AST(1)
#define ASTAMP_SB AST(2)
#define ASTAMP_G2A AST(3)
#define ASTAMP_G1B AST(4)
#define ASTAMP_G2B AST(5)
DI float bflo(unsigned u) { return __uint_as_float(u << 16); }
DI float bfhi(unsigned u) { return __uint_as_float(u & 0xffff0000u); }
DI unsigned pk(float lo, float hi) { f32x2 v = {lo, hi}; return __builtin_bit_cast(unsigned, __builtin_convertvector(v, bf16x2)); }
DI float rbf(float f) { return bflo(pk(f, f)); }
DI float dot2(unsigned a, unsigned b, float c) { return __builtin_amdgcn_fdot2_f32_bf16(__builtin_bit_cast(bf16x2, a), __builtin_bit_cast(bf16x2, b), c, false); }
DI float sum16(float v) { auto p = __builtin_amdgcn_permlane16_swap(__float_as_uint(v), __float_as_uint(v), false, false); return __uint_as_float(p[0]) + __uint_as_float(p[1]); }
DI float sum32(float v) { auto p = __builtin_amdgcn_permlane32_swap(__float_as_uint(v), __float_as_uint(v), false, false); return __uint_as_float(p[0]) + __uint_as_float(p[1]); }
template <int C> DI float dppf(float v) { return __int_as_float(__builtin_amdgcn_update_dpp(0, __float_as_int(v), C, 0xf, 0xf, true)); }
DI float sumrow(float v) { v += dppf<0x128>(v); v += dppf<0x124>(v); v += dppf<0x122>(v); v += dppf<0x121>(v); return v; }
DI unsigned nosent(unsigned u) { return u < SENT - 1 ? u : SENT - 1; }
DI void bar() { asm volatile("s_waitcnt lgkmcnt(0)\n\ts_barrier" ::: "memory"); }
template <class T> DI T ldsr(unsigned a) { return *(const LAS T*)a; }
template <class T> DI void ldsw(unsigned a, T v) { *(LAS T*)a = v; }
typedef __amdgpu_buffer_rsrc_t rsrc_t;
DI u32x4 ld16(rsrc_t r, unsigned o) { return __builtin_amdgcn_raw_buffer_load_b128(r, o, 0, 16); }
DI u32x2 ld8(rsrc_t r, unsigned o) { return __builtin_amdgcn_raw_buffer_load_b64(r, o, 0, 16); }
DI unsigned ld4(rsrc_t r, unsigned o) { return __builtin_amdgcn_raw_buffer_load_b32(r, o, 0, 16); }
DI void st16(rsrc_t r, unsigned o, u32x4 v) { __builtin_amdgcn_raw_buffer_store_b128(v, r, o, 0, 16); }
DI void st8(rsrc_t r, unsigned o, u32x2 v) { __builtin_amdgcn_raw_buffer_store_b64(v, r, o, 0, 16); }
DI void st4(rsrc_t r, unsigned o, unsigned v) { __builtin_amdgcn_raw_buffer_store_b32(v, r, o, 0, 16); }
DI unsigned mx4(u32x4 v, unsigned m = 0) { return max(max(max(v.x, v.y), m), max(v.z, v.w)); }
#define SPIN(n) _Pragma("clang loop unroll(disable)") for (unsigned n = 0;;)
DI bool giveup(unsigned& n) { asm volatile("s_sleep 1" ::: "memory"); return ++n > SPIN_CAP; }
DI void mfma(f32x4& acc, u32x4 a, u32x4 b) { asm volatile("s_nop 1\n\tv_mfma_f32_16x16x32_bf16 %0, %1, %2, %0" : "+v"(acc) : "v"(a), "v"(b)); }
DI void mfma_done(f32x4& acc) { asm volatile("s_nop 7" : "+v"(acc)); }
DI u32x4 tr_frag(unsigned a) {
    s16x4 lo = __builtin_amdgcn_ds_read_tr16_b64_v4i16((LAS s16x4*)a), hi = __builtin_amdgcn_ds_read_tr16_b64_v4i16((LAS s16x4*)(a + 128));
    u32x2 l = __builtin_bit_cast(u32x2, lo), h = __builtin_bit_cast(u32x2, hi);
    return u32x4{l.x, l.y, h.x, h.y};
}
DI void ln_params(float mu, float var, float& m, float& rs) {
    m = rbf(mu);
    rs = __builtin_amdgcn_rcpf(rbf(__builtin_amdgcn_sqrtf(rbf(rbf(fmaxf(var, 0.f)) + 1e-5f))));
}
DI float ln1(float x, float m, float rs) { return rbf(rbf(x - m) * rs); }
DI u32x4 widen(unsigned p0, unsigned p1, unsigned q0, unsigned q1) {
    auto a = __builtin_amdgcn_permlane32_swap(p0, q0, false, false);
    auto b = __builtin_amdgcn_permlane32_swap(p1, q1, false, false);
    auto c = __builtin_amdgcn_permlane16_swap(a[0], a[1], false, false);
    auto d = __builtin_amdgcn_permlane16_swap(b[0], b[1], false, false);
    return u32x4{c[0], d[0], c[1], d[1]};
}
template <int C> DI unsigned dppu(unsigned v) { return (unsigned)__builtin_amdgcn_update_dpp(0, (int)v, C, 0xf, 0xf, true); }
DI float key_score(unsigned k) { return __uint_as_float(((k & 0x80000000u) ? k ^ 0x80000000u : ~k) & 0xffff0000u); }
DI unsigned topk(const u32x4 a, const u32x4 b, unsigned cand, int w, int l, unsigned& rank) {
    const unsigned k[8] = {a.x, a.y, a.z, a.w, b.x, b.y, b.z, b.w};
    unsigned t = max(mx4(a), mx4(b));
    t = max(t, dppu<0x128>(t)); t = max(t, dppu<0x124>(t)); t = max(t, dppu<0x122>(t)); t = max(t, dppu<0x121>(t));
    { auto p = __builtin_amdgcn_permlane16_swap(t, t, false, false); t = max(p[0], p[1]); }
    { auto p = __builtin_amdgcn_permlane32_swap(t, t, false, false); t = min(p[0], p[1]); }
    if (l == 0) ldsw<unsigned>(cand + 144 + w * 4, t);
    bar();
    { const u32x4 v = ldsr<u32x4>(cand + 144); t = min(min(v.x, v.y), min(v.z, v.w)); }
    unsigned me;
    SPIN(it) {
        unsigned pos[8], n = 0;
#pragma unroll
        for (int i = 0; i < 8; ++i) {
            const u64 m = __ballot(k[i] >= t);
            pos[i] = n + __builtin_amdgcn_mbcnt_hi((unsigned)(m >> 32), __builtin_amdgcn_mbcnt_lo((unsigned)m, 0));
            n += __builtin_popcountll(m);
        }
        unsigned base = 0;
        if (l == 0) base = __hip_atomic_fetch_add((LAS unsigned*)(cand + 136), n, __ATOMIC_RELAXED, __HIP_MEMORY_SCOPE_WORKGROUP);
        base = __builtin_amdgcn_readfirstlane(base);
#pragma unroll
        for (int i = 0; i < 8; ++i) ldsw<unsigned>(cand + (k[i] >= t ? min(base + pos[i], 32u) : 32u) * 4, k[i]);
        bar();
        me = ldsr<unsigned>(cand + (l & 31) * 4);
        const unsigned total = ldsr<unsigned>(cand + 136);
        rank = 0;
#pragma unroll
        for (int i = 0; i < 8; ++i) {
            const u32x4 c = ldsr<u32x4>(cand + i * 16);
            rank += (c.x > me) + (c.y > me) + (c.z > me) + (c.w > me);
        }
        if (__builtin_amdgcn_readfirstlane(total) <= 32 || ++it > 256) break;
        t = __builtin_amdgcn_readlane(me, __builtin_ctzll(__ballot(rank == 7) | (1ull << 63)));
        bar();
        if (w == 0 && l < 35) ldsw<unsigned>(cand + l * 4, 0u);
        bar();
    }
    if (w == 0 && l < 32 && rank < 8) ldsw<unsigned>(T_SEL + rank * 4, (0xffffu - (me & 0xffffu)) & 2047u);
    return me;
}
DI unsigned sel_key(unsigned me, unsigned rank, int c) { return __builtin_amdgcn_readlane(me, __builtin_ctzll(__ballot(rank == (unsigned)c) | (1ull << 63))); }
DI unsigned key_row(unsigned k) { return (0xffffu - (k & 0xffffu)) & 2047u; }
DI unsigned xf_off(unsigned row, unsigned chunk) { return ((row >> 4) * 20 + (chunk >> 2)) * 1024 + ((chunk & 3) * 16 + (row & 15)) * 16; }

template <int NP> DI void stage_w(const u16* g, int ld, unsigned dst, int w, int l) {
    const char* p = (const char*)(g + (size_t)(w * 32 + (l >> 1)) * ld + (l & 1) * 8);
#pragma unroll 2
    for (int i = 0; i < (NP + 3) / 4; ++i)
        if (NP % 4 == 0 || i * 4 + w < NP)
            __builtin_amdgcn_global_load_lds((const unsigned*)(p + (size_t)i * 256 * ld), (LAS unsigned*)(dst + (i * 4 + w) * 1024), 16, 0, 0);
}

template <int LY>
DI void ffn1(rsrc_t R, unsigned me, unsigned rank, int f1, unsigned lw, unsigned lxs, const u16* cre, const u16* cim, int w, int l, char* ws, int tid) {
    const unsigned r0 = key_row(sel_key(me, rank, 2 * w)), r1 = key_row(sel_key(me, rank, 2 * w + 1)), row = l < 32 ? r0 : r1, li = l & 31;
    u32x4 x[3], ms;
    SPIN(n) {
        ms = ld16(R, A_MS + LY * 32768 + row * 16);
        unsigned mx = mx4(ms);
#pragma unroll
        for (int i = 0; i < 3; ++i) { x[i] = ld16(R, (LY ? A_X3 : A_X1) + xf_off(row, li + 32 * i < 80 ? li + 32 * i : li)); mx = mx4(x[i], mx); }
        if (__all(mx != SENT) || giveup(n)) break;
    }
    FST(LY ? 29 : 23);
    const unsigned ta = lw + ((l >> 4) * 8 + ((l & 15) >> 2)) * 32 + (l & 3) * 8 + w * 5120, fr = lxs + 12288;
    const float m = __uint_as_float(ms.x), rs = __uint_as_float(ms.y);
#pragma unroll
    for (int i = 0; i < 3; ++i) {
        u32x4 o;
#pragma unroll
        for (int e = 0; e < 4; ++e) o[e] = pk(rbf(bflo(x[i][e]) - m) * rs, rbf(bfhi(x[i][e]) - m) * rs);
        if (i < 2 || li < 16) ldsw<u32x4>(lxs + (2 * w + (l >> 5)) * XS_STRIDE + (li + 32 * i) * 16, o);
    }
    bar();
    f32x4 g = {0, 0, 0, 0}, u = {0, 0, 0, 0};
    const unsigned xa = lxs + (l & 7) * XS_STRIDE + (l >> 4) * 16 + w * 320;
    u32x4 xb[5], wg_[5], wu_[5];
#pragma unroll
    for (int i = 0; i < 5; ++i) { xb[i] = ldsr<u32x4>(xa + i * 64); wg_[i] = tr_frag(ta + i * 1024); wu_[i] = tr_frag(ta + 20480 + i * 1024); }
#pragma unroll
    for (int i = 0; i < 5; ++i) { mfma(g, wg_[i], xb[i]); mfma(u, wu_[i], xb[i]); }
    asm volatile("s_nop 7" : "+v"(g), "+v"(u));
    ldsw<f32x4>(fr + w * 2048 + l * 16, g); ldsw<f32x4>(fr + w * 2048 + 1024 + l * 16, u);
    bar();
    if (w == 0) {
        g = ldsr<f32x4>(fr + l * 16); u = ldsr<f32x4>(fr + 1024 + l * 16);
#pragma unroll
        for (int i = 1; i < 4; ++i) { g += ldsr<f32x4>(fr + i * 2048 + l * 16); u += ldsr<f32x4>(fr + i * 2048 + 1024 + l * 16); }
        float h[4];
#pragma unroll
        for (int i = 0; i < 4; ++i) { float gg = rbf(g[i]); h[i] = rbf(gg * __builtin_amdgcn_rcpf(1.f + __expf(-gg))) * rbf(u[i]); }
        const int q = l >> 4;
        if ((l & 15) < 8)
            st8(R, A_HF + LY * HF_SZ + (f1 >> 1) * 512 + ((f1 & 1) * 2 + (q >> 1)) * 128 + (l & 7) * 16 + (q & 1) * 8, u32x2{nosent(pk(h[0], h[1])), nosent(pk(h[2], h[3]))});
    } else if (LY == 0 && w < 3 && f1 < 64) {
        const u32x4 i0 = ldsr<u32x4>(T_SEL), i1 = ldsr<u32x4>(T_SEL + 16);
        const u16* cp = ((l & 16) ? cim : cre) + (size_t)((2 * f1 + w - 1) * 16 + (l & 15)) * 2048;
        u32x4 c = {(unsigned)cp[i0.x] | ((unsigned)cp[i0.y] << 16), (unsigned)cp[i0.z] | ((unsigned)cp[i0.w] << 16),
                   (unsigned)cp[i1.x] | ((unsigned)cp[i1.y] << 16), (unsigned)cp[i1.z] | ((unsigned)cp[i1.w] << 16)};
#pragma unroll
        for (int e = 0; e < 4; ++e) c[e] = l < 32 ? nosent(c[e]) : 0u;
        st16(R, A_CG + (2 * f1 + w - 1) * 1024 + l * 16, c);
    } else if (LY == 0 && f1 == 64 && w == 1 && l < 8) {
        st4(R, A_IDX + l * 4, ldsr<unsigned>(T_SEL + l * 4));
    }
}

template <int LY>
DI void ffn2(rsrc_t R, int f2, unsigned lw, u16* out, int w, int l) {
    const int k0 = w * 14 - (w > 2 ? 1 : 0), c = l & 7, q = l >> 4, col = f2 * 16 + q * 4;
    const unsigned row = ldsr<unsigned>(T_SEL + c * 4), fr = lw + 57344;
    const unsigned ta = lw + ((l >> 4) * 8 + ((l & 15) >> 2)) * 32 + (l & 3) * 8 + k0 * 1024;
    u32x4 wf[14], hb[14], ms;
    u32x2 x;
    unsigned key;
    if (LY) {
#pragma unroll
        for (int i = 0; i < 14; ++i) wf[i] = tr_frag(ta + i * 1024);
    }
    SPIN(n) {
        x = ld8(R, (LY ? A_X3 : A_X1) + xf_off(row, col >> 3) + (q & 1) * 8); ms = ld16(R, A_MS + LY * 32768 + row * 16); key = ld4(R, A_RB + LY * 8192 + row * 4);
        unsigned mx = mx4(ms, max(max(x.x, x.y), key));
#pragma unroll
        for (int i = 0; i < 14; ++i) { hb[i] = ld16(R, A_HF + LY * HF_SZ + min(k0 + i, 53) * 512 + q * 128 + c * 16); mx = mx4(hb[i], mx); }
        if (__all(mx != SENT) || giveup(n)) break;
    }
    if (w >= 2) hb[13] = u32x4{0, 0, 0, 0};
    f32x4 y = {0, 0, 0, 0}, y2 = {0, 0, 0, 0};
#pragma unroll
    for (int i = 0; i < 14; i += 2) { mfma(y, LY ? wf[i] : tr_frag(ta + i * 1024), hb[i]); mfma(y2, LY ? wf[i + 1] : tr_frag(ta + i * 1024 + 1024), hb[i + 1]); }
    asm volatile("s_nop 7" : "+v"(y), "+v"(y2));
    ldsw<f32x4>(fr + w * 1024 + l * 16, y + y2);
    bar();
    if (w == 0 && (l & 15) < 8) {
        y = ldsr<f32x4>(fr + l * 16);
#pragma unroll
        for (int i = 1; i < 4; ++i) y += ldsr<f32x4>(fr + i * 1024 + l * 16);
        const float m = __uint_as_float(ms.x), rs = __uint_as_float(ms.y), al = __uint_as_float(ms.z), be = __uint_as_float(ms.w);
        const float sg = rbf(__builtin_amdgcn_rcpf(1.f + __expf(-key_score(key))));
        const float xv[4] = {bflo(x.x), bfhi(x.x), bflo(x.y), bfhi(x.y)};
        float o[4];
#pragma unroll
        for (int i = 0; i < 4; ++i) o[i] = xv[i] + rbf(ln1(xv[i], m, rs) + rbf(rbf(y[i]) * sg));
        const u32x2 ov = {pk(o[0], o[1]), pk(o[2], o[3])};
        if (LY == 0) {
            st8(R, A_X2 + (c * 640 + col) * 2, u32x2{nosent(ov.x), nosent(ov.y)});
            u32x4 xo;
#pragma unroll
            for (int i = 0; i < 4; ++i) xo[i] = nosent(__float_as_uint(al * xv[i] + be));
            st16(R, A_XO + (c * 640 + col) * 4, xo);
        } else {
            *(u32x2*)(out + (size_t)row * 640 + col) = ov;
        }
    }
}

DI void ffn3(rsrc_t R, int bep, int j, int w, int l) {
    const int c = 2 * w + (l >> 5), li = l & 31, row = l & 15;
    u32x4 x[3], xo[3][2], df[5];
#pragma unroll
    for (int g = 0; g < 5; ++g) df[g] = ldsr<u32x4>(LD + (j * 16 + row) * 1280 + (((4 * (4 * g + w) + (l >> 4)) ^ row) << 4));
    SPIN(n) {
        unsigned mx = 0;
#pragma unroll
        for (int i = 0; i < 3; ++i) {
            int ch = li + 32 * i; ch = ch < 80 ? ch : li;
            x[i] = ld16(R, A_X2 + c * 1280 + ch * 16); xo[i][0] = ld16(R, A_XO + c * 2560 + ch * 32); xo[i][1] = ld16(R, A_XO + c * 2560 + ch * 32 + 16);
            mx = mx4(x[i], mx4(xo[i][0], mx4(xo[i][1], mx)));
        }
        if (__all(mx != SENT) || giveup(n)) break;
    }
    float s1 = 0, s2 = 0;
#pragma unroll
    for (int i = 0; i < 3; ++i) {
        float a = 0, b2 = 0;
#pragma unroll
        for (int e = 0; e < 4; ++e) { const float lo = bflo(x[i][e]), hi = bfhi(x[i][e]); a += lo + hi; b2 = fmaf(lo, lo, fmaf(hi, hi, b2)); }
        if (i < 2 || li < 16) { s1 += a; s2 += b2; }
    }
    s1 = sum16(sumrow(s1)); s2 = sum16(sumrow(s2));
    const float mu = s1 * (1.f / 640);
    float m, rs;
    ln_params(mu, s2 * (1.f / 640) - mu * mu, m, rs);
    const unsigned xs = LRED + 24576, fr = xs + 12288;
#pragma unroll
    for (int i = 0; i < 3; ++i) {
        u32x4 o;
#pragma unroll
        for (int e = 0; e < 4; ++e)
            o[e] = pk(ln1(bflo(x[i][e]), m, rs) - __uint_as_float(xo[i][e >> 1][(e & 1) * 2]), ln1(bfhi(x[i][e]), m, rs) - __uint_as_float(xo[i][e >> 1][(e & 1) * 2 + 1]));
        if (i < 2 || li < 16) ldsw<u32x4>(xs + c * XS_STRIDE + (li + 32 * i) * 16, o);
    }
    bar();
    f32x4 d = {0, 0, 0, 0};
#pragma unroll
    for (int g = 0; g < 5; ++g) mfma(d, ldsr<u32x4>(xs + (l & 7) * XS_STRIDE + (l >> 4) * 16 + (4 * g + w) * 64), df[g]);
    mfma_done(d);
    ldsw<f32x4>(fr + w * 1024 + l * 16, d);
    bar();
    if (w == 0 && l < 32) {
        d = ldsr<f32x4>(fr + l * 16);
#pragma unroll
        for (int i = 1; i < 4; ++i) d += ldsr<f32x4>(fr + i * 1024 + l * 16);
        if (bep >= 8) d = -d;
        st8(R, A_DAB + (bep * 80 + j * 16 + row) * 16 + (l >> 4) * 8, u32x2{nosent(pk(d[0], d[1])), nosent(pk(d[2], d[3]))});
    }
}

DI bool ondiag(int l) { return ((l >> 2) & 3) == (l >> 4); }
DI float diag(float a, float b, float c, float d, int l) { const float x = (l & 1) ? b : a, y = (l & 1) ? d : c; return (l & 2) ? y : x; }
DI unsigned affine2(unsigned x, float g, float c) { return pk(fmaf(bflo(x), g, c), fmaf(bfhi(x), g, c)); }

__global__ __launch_bounds__(256, 1) void fnet_mod_mk(const u16* __restrict__ x0, const u16* __restrict__ cre, const u16* __restrict__ cim,
                                                      const u16* __restrict__ dre, const u16* __restrict__ dim, const u16* __restrict__ wr,
                                                      const u16* __restrict__ wg, const u16* __restrict__ wu, const u16* __restrict__ wd,
                                                      u16* __restrict__ out, char* __restrict__ ws) {
    const int b = blockIdx.x, w = __builtin_amdgcn_readfirstlane(threadIdx.x >> 6);
    const int q = b & 3, bt = ((b >> 2) & 1) * 8 + ((b >> 3) & 7), be4 = b >> 6, bep = q * 4 + be4;
    int l, tid;
#define ARG(i) ({ const u64 __attribute__((address_space(4)))* ka_ = (const u64 __attribute__((address_space(4)))*)__builtin_amdgcn_kernarg_segment_ptr(); asm volatile("" : "+s"(ka_)); (const u16*)(const u16 __attribute__((address_space(1)))*)ka_[i]; })
#define LANE() do { asm volatile("v_mbcnt_lo_u32_b32 %0, -1, 0\n\tv_mbcnt_hi_u32_b32 %0, -1, %0" : "=v"(l)); __builtin_assume(l >= 0 && l < 64); tid = w * 64 + l; } while (0)
    LANE();
    STAMP(0);
    u64 non;
    const rsrc_t srdx = __builtin_amdgcn_make_buffer_rsrc((void*)(x0 + (size_t)(bt * 128 + w * 32) * 640), 0, 32 * 1280, 0x00020000);
    {
        unsigned vd[5];
#pragma unroll
        for (int i = 0; i < 5; ++i) { unsigned p = i * 64 + l, r = (p * 205) >> 14, sl = p - r * 80; r += 4 * w; vd[i] = r * 1280 + ((sl ^ r) << 4); }
        const u16* dsrc = (bep < 8 ? dre : dim) + (size_t)(bep & 7) * 80 * 640;
        asm volatile(
        "s_nop 4\n"
        "v_mov_b32 v20, 0\n"
        "global_load_dwordx2 v[14:15], v20, %[sws]\n"
        "s_mov_b64 s[68:69], %[sd]\n"
        "s_add_u32 m0, %[sm0], 0\n"
        "s_nop 0\n"
        "global_load_lds_dwordx4 %[vd0], s[68:69]\n"
        "s_add_u32 m0, %[sm0], 1024\n"
        "s_nop 0\n"
        "global_load_lds_dwordx4 %[vd1], s[68:69]\n"
        "s_add_u32 m0, %[sm0], 2048\n"
        "s_nop 0\n"
        "global_load_lds_dwordx4 %[vd2], s[68:69]\n"
        "s_add_u32 m0, %[sm0], 3072\n"
        "s_nop 0\n"
        "global_load_lds_dwordx4 %[vd3], s[68:69]\n"
        "s_add_u32 m0, %[sm0], 4096\n"
        "s_nop 0\n"
        "global_load_lds_dwordx4 %[vd4], s[68:69]\n"
        "s_add_u32 s68, s68, 20480\n"
        "s_addc_u32 s69, s69, 0\n"
        "s_add_u32 m0, %[sm0], 20480\n"
        "s_nop 0\n"
        "global_load_lds_dwordx4 %[vd0], s[68:69]\n"
        "s_add_u32 m0, %[sm0], 21504\n"
        "s_nop 0\n"
        "global_load_lds_dwordx4 %[vd1], s[68:69]\n"
        "s_add_u32 m0, %[sm0], 22528\n"
        "s_nop 0\n"
        "global_load_lds_dwordx4 %[vd2], s[68:69]\n"
        "s_add_u32 m0, %[sm0], 23552\n"
        "s_nop 0\n"
        "global_load_lds_dwordx4 %[vd3], s[68:69]\n"
        "s_add_u32 m0, %[sm0], 24576\n"
        "s_nop 0\n"
        "global_load_lds_dwordx4 %[vd4], s[68:69]\n"
        "s_add_u32 s68, s68, 20480\n"
        "s_addc_u32 s69, s69, 0\n"
        "s_add_u32 m0, %[sm0], 40960\n"
        "s_nop 0\n"
        "global_load_lds_dwordx4 %[vd0], s[68:69]\n"
        "s_add_u32 m0, %[sm0], 41984\n"
        "s_nop 0\n"
        "global_load_lds_dwordx4 %[vd1], s[68:69]\n"
        "s_add_u32 m0, %[sm0], 43008\n"
        "s_nop 0\n"
        "global_load_lds_dwordx4 %[vd2], s[68:69]\n"
        "s_add_u32 m0, %[sm0], 44032\n"
        "s_nop 0\n"
        "global_load_lds_dwordx4 %[vd3], s[68:69]\n"
        "s_add_u32 m0, %[sm0], 45056\n"
        "s_nop 0\n"
        "global_load_lds_dwordx4 %[vd4], s[68:69]\n"
        "s_add_u32 s68, s68, 20480\n"
        "s_addc_u32 s69, s69, 0\n"
        "s_add_u32 m0, %[sm0], 61440\n"
        "s_nop 0\n"
        "global_load_lds_dwordx4 %[vd0], s[68:69]\n"
        "s_add_u32 m0, %[sm0], 62464\n"
        "s_nop 0\n"
        "global_load_lds_dwordx4 %[vd1], s[68:69]\n"
        "s_add_u32 m0, %[sm0], 63488\n"
        "s_nop 0\n"
        "global_load_lds_dwordx4 %[vd2], s[68:69]\n"
        "s_add_u32 m0, %[sm0], 64512\n"
        "s_nop 0\n"
        "global_load_lds_dwordx4 %[vd3], s[68:69]\n"
        "s_add_u32 m0, %[sm0], 65536\n"
        "s_nop 0\n"
        "global_load_lds_dwordx4 %[vd4], s[68:69]\n"
        "s_add_u32 s68, s68, 20480\n"
        "s_addc_u32 s69, s69, 0\n"
        "s_add_u32 m0, %[sm0], 81920\n"
        "s_nop 0\n"
        "global_load_lds_dwordx4 %[vd0], s[68:69]\n"
        "s_add_u32 m0, %[sm0], 82944\n"
        "s_nop 0\n"
        "global_load_lds_dwordx4 %[vd1], s[68:69]\n"
        "s_add_u32 m0, %[sm0], 83968\n"
        "s_nop 0\n"
        "global_load_lds_dwordx4 %[vd2], s[68:69]\n"
        "s_add_u32 m0, %[sm0], 84992\n"
        "s_nop 0\n"
        "global_load_lds_dwordx4 %[vd3], s[68:69]\n"
        "s_add_u32 m0, %[sm0], 86016\n"
        "s_nop 0\n"
        "global_load_lds_dwordx4 %[vd4], s[68:69]\n"
        "v_xor_b32 v12, 64, %[vxe]\n"
        "s_add_u32 m0, %[sring], 0\n"
        "s_movk_i32 s58, 0\n"
        "buffer_load_dwordx4 %[vxe], %[srdx], s58 offen offset:0 lds\n"
        "s_add_u32 m0, %[sring], 1024\n"
        "s_movk_i32 s58, 10240\n"
        "buffer_load_dwordx4 v12, %[srdx], s58 offen offset:0 lds\n"
        "s_add_u32 m0, %[sring], 2048\n"
        "s_movk_i32 s58, 20480\n"
        "buffer_load_dwordx4 %[vxe], %[srdx], s58 offen offset:0 lds\n"
        "s_add_u32 m0, %[sring], 3072\n"
        "s_movk_i32 s58, 30720\n"
        "buffer_load_dwordx4 v12, %[srdx], s58 offen offset:0 lds\n"
        "s_add_u32 m0, %[sring], 3968\n"
        "s_movk_i32 s58, 0\n"
        "buffer_load_dwordx4 %[vxe], %[srdx], s58 offen offset:128 lds\n"
        "s_add_u32 m0, %[sring], 4992\n"
        "s_movk_i32 s58, 10240\n"
        "buffer_load_dwordx4 v12, %[srdx], s58 offen offset:128 lds\n"
        "s_add_u32 m0, %[sring], 6016\n"
        "s_movk_i32 s58, 20480\n"
        "buffer_load_dwordx4 %[vxe], %[srdx], s58 offen offset:128 lds\n"
        "s_add_u32 m0, %[sring], 7040\n"
        "s_movk_i32 s58, 30720\n"
        "buffer_load_dwordx4 v12, %[srdx], s58 offen offset:128 lds\n"
        "s_add_u32 m0, %[sring], 7936\n"
        "s_movk_i32 s58, 0\n"
        "buffer_load_dwordx4 %[vxe], %[srdx], s58 offen offset:256 lds\n"
        "s_add_u32 m0, %[sring], 8960\n"
        "s_movk_i32 s58, 10240\n"
        "buffer_load_dwordx4 v12, %[srdx], s58 offen offset:256 lds\n"
        "s_add_u32 m0, %[sring], 9984\n"
        "s_movk_i32 s58, 20480\n"
        "buffer_load_dwordx4 %[vxe], %[srdx], s58 offen offset:256 lds\n"
        "s_add_u32 m0, %[sring], 11008\n"
        "s_movk_i32 s58, 30720\n"
        "buffer_load_dwordx4 v12, %[srdx], s58 offen offset:256 lds\n"
        : "=&{v[14:15]}"(non)
        : [vxe] "v"((l >> 3) * 1280 + (((l & 7) ^ (l >> 4)) << 4)),
          [sring] "s"(LRED + w * 12288),
          [sws] "s"(ws),
          [vd0] "v"(vd[0]),
          [vd1] "v"(vd[1]),
          [vd2] "v"(vd[2]),
          [vd3] "v"(vd[3]),
          [vd4] "v"(vd[4]),
          [srdx] "s"(srdx),
          [sd] "s"(dsrc),
          [sm0] "s"(LD + w * 5120)
        : "scc", "vcc", "memory", "s58", "s68", "s69", "v12", "v20", "a0", "a1", "a2", "a3", "a4", "a5", "a6", "a7", "a8", "a9", "a10", "a11", "a12", "a13", "a14", "a15", "a16", "a17", "a18", "a19", "a20", "a21", "a22", "a23", "a24", "a25", "a26", "a27", "a28", "a29", "a30", "a31", "a32", "a33", "a34", "a35", "a36", "a37", "a38", "a39", "a40", "a41", "a42", "a43", "a44", "a45", "a46", "a47", "a48", "a49", "a50", "a51", "a52", "a53", "a54", "a55", "a56", "a57", "a58", "a59", "a60", "a61", "a62", "a63", "a64", "a65", "a66", "a67", "a68", "a69", "a70", "a71", "a72", "a73", "a74", "a75", "a76", "a77", "a78", "a79", "a80", "a81", "a82", "a83", "a84", "a85", "a86", "a87", "a88", "a89", "a90", "a91", "a92", "a93", "a94", "a95", "a96", "a97", "a98", "a99", "a100", "a101", "a102", "a103", "a104", "a105", "a106", "a107", "a108", "a109", "a110", "a111", "a112", "a113", "a114", "a115", "a116", "a117", "a118", "a119", "a120", "a121", "a122", "a123", "a124", "a125", "a126", "a127", "a128", "a129", "a130", "a131", "a132", "a133", "a134", "a135", "a136", "a137", "a138", "a139", "a140", "a141", "a142", "a143", "a144", "a145", "a146", "a147", "a148", "a149", "a150", "a151", "a152", "a153", "a154", "a155", "a156", "a157", "a158", "a159", "a160", "a161", "a162", "a163", "a164", "a165", "a166", "a167", "a168", "a169", "a170", "a171", "a172", "a173", "a174", "a175", "a176", "a177", "a178", "a179", "a180", "a181", "a182", "a183", "a184", "a185", "a186", "a187", "a188", "a189", "a190", "a191", "a192", "a193", "a194", "a195", "a196", "a197", "a198", "a199", "a200", "a201", "a202", "a203", "a204", "a205", "a206", "a207", "a208", "a209", "a210", "a211", "a212", "a213", "a214", "a215", "a216", "a217", "a218", "a219", "a220", "a221", "a222", "a223", "a224", "a225", "a226", "a227", "a228", "a229", "a230", "a231", "a232", "a233", "a234", "a235", "a236", "a237", "a238", "a239", "a240", "a241", "a242", "a243", "a244", "a245", "a246", "a247", "a248", "a249", "a250", "a251", "a252", "a253", "a254", "a255");
    }
    const rsrc_t R = __builtin_amdgcn_make_buffer_rsrc(ws, 0, WS_USED, 0x00020000);
    const rsrc_t srdc = __builtin_amdgcn_make_buffer_rsrc((void*)((q < 2 ? cre : cim) + (size_t)bt * 128 * 2048 + (q & 1) * 1024 + w * 256), 0, 127 * 4096 + 512, 0x00020000);
    const int f1 = ((b & 4) && (b >> 3) < 27) ? q * 27 + (b >> 3) : -1;
    const int f2 = (!(b & 4) && bt >= 5 && (bt - 5) * 4 + be4 < 10) ? q * 10 + (bt - 5) * 4 + be4 : -1;
    unsigned sjo;
    {
        const unsigned i = ((bep >> 3) * 2 + (bt >> 3)) * 2 + (bep & 1);
        const u64 lo = i < 3 ? 0x321042104310432ull : i < 6 ? 0x21434321021043ull : 0x2104343210ull;
        sjo = (unsigned)(lo >> (20 * (i % 3))) & 0xfffffu;
        const unsigned n0 = q == 0 ? 0x53320u : q == 1 ? 0x53110u : q == 2 ? 0x54200u : 0x55420u, n1 = q == 0 ? 0x55322u : q == 1 ? 0x55532u : q == 2 ? 0x54442u : 0x53222u;
        const int pbe = (q >> 1) * 8 + 2 * be4 + ((l >> 3) & 1), pbs = (q & 1) * 8 + 2 * w + ((l >> 2) & 1), qq = l >> 4, e = l & 15;
        ldsw<u32x4>(T_OP + tid * 16, u32x4{O_PF + ((((pbe & 3) * 8 + (pbs & 7)) * 8 + (pbs >> 3) * 4 + (pbe >> 2)) * 4 + (l & 3)) * 128u, ((l & 8) ? n1 : n0) | (l == 0 ? 2u << 20 : 1u << 20) | (l == 1 ? 3u << 24 : 1u << 24),
                                           O_XF + (((b & ~3) | ((q - 1 - (l < 2 ? l : 2)) & 3)) * 4 + w) * 128u, (unsigned)(bt * 128 + w * 32 + e)});
        if (w == 0) {
            ldsw<u32x4>(T_OP2 + l * 16, u32x4{(unsigned)(e * 16 + (qq >> 1) * 256 + (qq & 1) * 8), (unsigned)(e * 1280 + qq * 8), (unsigned)(qq * 8), (unsigned)((qq & 1) * 10240 + e * 16)});
            const unsigned one = qq == (e >> 2) ? 0x3f80u << (16 * (e & 1)) : 0u;
            ldsw<u32x2>(T_ID + l * 8, u32x2{(e & 2) ? 0u : one, (e & 2) ? one : 0u});
            if (l < 48) ldsw<unsigned>(T_ZERO + l * 4, 0u);
            ldsw<u32x2>(T_CAND + l * 8, u32x2{0u, 0u});
        }
    }
    const unsigned shc = 128 + 16 * (q & 1), xrow = (bt * 8 + 2 * w) * 20480;
    u32x16 KP = {O_AB + (unsigned)((q * 4 + w) * 40 + be4 * 10) * 8192 + 4096, O_IB + b * 32768u + w * 2048, O_IB + b * 32768u + 24576 + 4096 + w * 512,
                 (unsigned)(be4 * 160 + q * 32) * 2, (q >> 1) * 20480 + (be4 * 160 + shc) * 2, (unsigned)(be4 * 160 + q * 32) * 2, (be4 * 160 + shc) * 2,
                 A_X1 + xrow + (be4 * 5 + q) * 1024, A_X1 + xrow + (q >> 1) * 20480 + (be4 * 5 + 4) * 1024 + (q & 1) * 512,
                 A_ST + ((bt * 16 + be4 * 4 + q) * 128 + 32 * w) * 16u, O_XF + (b * 4 + w) * 128u, O_CLR + b * 8u,
                 A_DAB + (be4 * 160 + q * 32) * 16u, A_CG + (bt * 8 + 2 * w) * 1024u, A_X2 + (be4 * 160 + q * 32) * 2u, A_RB + b * 2048u + w * 512};
    const u32x4 LB = {LRED + ((w + 1) & 3) * 6144, LRED + ((w + 2) & 3) * 6144 + 2048, LRED + ((w + 3) & 3) * 6144 + 4096, LRED + w * 6144};
    u32x4 DP = {O_IB + (b & ~3) * 32768u + 24576 + q * 2048 + w * 512, O_IB + ((b & ~3) | ((q + 1) & 3)) * 32768u + w * 2048,
                O_IB + ((b & ~3) | ((q + 2) & 3)) * 32768u + 8192 + w * 2048, O_IB + ((b & ~3) | ((q + 3) & 3)) * 32768u + 16384 + w * 2048};
    unsigned abo = O_AB + ((unsigned)(((bep >> 3) * 8 + (bt >> 1)) * 40 + (bep & 7) * 5) * 8 + (bt & 1) * 4 + w) * 1024;
    const u32x8 MP = {4u | (((q + 1) & 3) << 4) | (((q + 2) & 3) << 8) | (((q + 3) & 3) << 12) | (q << 16), (unsigned)q, (shc - q * 32) * 2, (unsigned)w, 1280u, abo + AB_SZ, T_OP + w * 1024u, 0u};
    asm volatile("" : "+s"(KP), "+s"(DP), "+s"(abo));
    f32x32 X0, X1, X2, X3, X4, AC;
    {
        f32x16 SQ;
        asm volatile(
        "s_nop 4\n"
        "v_xor_b32 v11, 64, %[vbr]\n"
        "v_xor_b32 v12, 64, %[vxe]\n"
        "v_xor_b32 v13, 64, %[vce]\n"
        "v_mov_b32 v16, %[vdr]\n"
        "v_xor_b32 v17, 64, %[vdr]\n"
        "v_xor_b32 v18, 0x80, %[vdr]\n"
        "v_xor_b32 v19, 0xc0, %[vdr]\n"
        "s_bfe_u32 s59, %[sjo], 262144\n"
        "s_mul_i32 s60, s59, 20480\n"
        "s_bfe_u32 s59, %[sjo], 262148\n"
        "s_mul_i32 s61, s59, 20480\n"
        "s_bfe_u32 s59, %[sjo], 262152\n"
        "s_mul_i32 s62, s59, 20480\n"
        "s_bfe_u32 s59, %[sjo], 262156\n"
        "s_mul_i32 s63, s59, 20480\n"
        "s_bfe_u32 s59, %[sjo], 262160\n"
        "s_mul_i32 s64, s59, 20480\n"
        "s_mov_b32 s71, 0x3f803f80\n"
        "v_mov_b32 v40, s71\n"
        "v_mov_b32 v41, s71\n"
        "v_mov_b32 v42, s71\n"
        "v_mov_b32 v43, s71\n"
        "s_lshl_b32 s59, %[scw], 0\n"
        "s_lshr_b32 s59, s59, 11\n"
        "v_xor_b32 v64, s59, %[vdr]\n"
        "v_add_u32 v65, 61440, v64\n"
        "s_waitcnt vmcnt(12)\n"
        "s_barrier\n"
        "ds_read_b128 v[44:47], v64 offset:0\n"
        "ds_read_b128 v[48:51], v64 offset:20480\n"
        "ds_read_b128 v[52:55], v64 offset:40960\n"
        "ds_read_b128 v[56:59], v65 offset:0\n"
        "ds_read_b128 v[60:63], v65 offset:20480\n"
        "s_waitcnt lgkmcnt(4)\n"
        "v_mfma_f32_16x16x32_bf16 v[68:71], v[40:43], v[44:47], 0\n"
        "ds_read_b128 v[44:47], v64 offset:256\n"
        "s_waitcnt lgkmcnt(4)\n"
        "v_mfma_f32_16x16x32_bf16 v[72:75], v[40:43], v[48:51], 0\n"
        "ds_read_b128 v[48:51], v64 offset:20736\n"
        "s_waitcnt lgkmcnt(4)\n"
        "v_mfma_f32_16x16x32_bf16 v[76:79], v[40:43], v[52:55], 0\n"
        "ds_read_b128 v[52:55], v64 offset:41216\n"
        "s_waitcnt lgkmcnt(4)\n"
        "v_mfma_f32_16x16x32_bf16 v[80:83], v[40:43], v[56:59], 0\n"
        "ds_read_b128 v[56:59], v65 offset:256\n"
        "s_waitcnt lgkmcnt(4)\n"
        "v_mfma_f32_16x16x32_bf16 v[84:87], v[40:43], v[60:63], 0\n"
        "ds_read_b128 v[60:63], v65 offset:20736\n"
        "s_waitcnt lgkmcnt(4)\n"
        "v_mfma_f32_16x16x32_bf16 v[68:71], v[40:43], v[44:47], v[68:71]\n"
        "ds_read_b128 v[44:47], v64 offset:512\n"
        "s_waitcnt lgkmcnt(4)\n"
        "v_mfma_f32_16x16x32_bf16 v[72:75], v[40:43], v[48:51], v[72:75]\n"
        "ds_read_b128 v[48:51], v64 offset:20992\n"
        "s_waitcnt lgkmcnt(4)\n"
        "v_mfma_f32_16x16x32_bf16 v[76:79], v[40:43], v[52:55], v[76:79]\n"
        "ds_read_b128 v[52:55], v64 offset:41472\n"
        "s_waitcnt lgkmcnt(4)\n"
        "v_mfma_f32_16x16x32_bf16 v[80:83], v[40:43], v[56:59], v[80:83]\n"
        "ds_read_b128 v[56:59], v65 offset:512\n"
        "s_waitcnt lgkmcnt(4)\n"
        "v_mfma_f32_16x16x32_bf16 v[84:87], v[40:43], v[60:63], v[84:87]\n"
        "ds_read_b128 v[60:63], v65 offset:20992\n"
        "s_waitcnt lgkmcnt(4)\n"
        "v_mfma_f32_16x16x32_bf16 v[68:71], v[40:43], v[44:47], v[68:71]\n"
        "ds_read_b128 v[44:47], v64 offset:768\n"
        "s_waitcnt lgkmcnt(4)\n"
        "v_mfma_f32_16x16x32_bf16 v[72:75], v[40:43], v[48:51], v[72:75]\n"
        "ds_read_b128 v[48:51], v64 offset:21248\n"
        "s_waitcnt lgkmcnt(4)\n"
        "v_mfma_f32_16x16x32_bf16 v[76:79], v[40:43], v[52:55], v[76:79]\n"
        "ds_read_b128 v[52:55], v64 offset:41728\n"
        "s_waitcnt lgkmcnt(4)\n"
        "v_mfma_f32_16x16x32_bf16 v[80:83], v[40:43], v[56:59], v[80:83]\n"
        "ds_read_b128 v[56:59], v65 offset:768\n"
        "s_waitcnt lgkmcnt(4)\n"
        "v_mfma_f32_16x16x32_bf16 v[84:87], v[40:43], v[60:63], v[84:87]\n"
        "ds_read_b128 v[60:63], v65 offset:21248\n"
        "s_waitcnt lgkmcnt(4)\n"
        "v_mfma_f32_16x16x32_bf16 v[68:71], v[40:43], v[44:47], v[68:71]\n"
        "ds_read_b128 v[44:47], v64 offset:1024\n"
        "s_waitcnt lgkmcnt(4)\n"
        "v_mfma_f32_16x16x32_bf16 v[72:75], v[40:43], v[48:51], v[72:75]\n"
        "ds_read_b128 v[48:51], v64 offset:21504\n"
        "s_waitcnt lgkmcnt(4)\n"
        "v_mfma_f32_16x16x32_bf16 v[76:79], v[40:43], v[52:55], v[76:79]\n"
        "ds_read_b128 v[52:55], v64 offset:41984\n"
        "s_waitcnt lgkmcnt(4)\n"
        "v_mfma_f32_16x16x32_bf16 v[80:83], v[40:43], v[56:59], v[80:83]\n"
        "ds_read_b128 v[56:59], v65 offset:1024\n"
        "s_waitcnt lgkmcnt(4)\n"
        "v_mfma_f32_16x16x32_bf16 v[84:87], v[40:43], v[60:63], v[84:87]\n"
        "ds_read_b128 v[60:63], v65 offset:21504\n"
        "s_waitcnt lgkmcnt(4)\n"
        "v_mfma_f32_16x16x32_bf16 v[68:71], v[40:43], v[44:47], v[68:71]\n"
        "s_waitcnt lgkmcnt(3)\n"
        "v_mfma_f32_16x16x32_bf16 v[72:75], v[40:43], v[48:51], v[72:75]\n"
        "s_waitcnt lgkmcnt(2)\n"
        "v_mfma_f32_16x16x32_bf16 v[76:79], v[40:43], v[52:55], v[76:79]\n"
        "s_waitcnt lgkmcnt(1)\n"
        "v_mfma_f32_16x16x32_bf16 v[80:83], v[40:43], v[56:59], v[80:83]\n"
        "s_waitcnt lgkmcnt(0)\n"
        "v_mfma_f32_16x16x32_bf16 v[84:87], v[40:43], v[60:63], v[84:87]\n"
        "s_nop 7\n"
        "ds_write_b32 %[vcw], v68 offset:0\n"
        "ds_write_b32 %[vcw], v72 offset:64\n"
        "ds_write_b32 %[vcw], v76 offset:128\n"
        "ds_write_b32 %[vcw], v80 offset:192\n"
        "ds_write_b32 %[vcw], v84 offset:256\n"
        "v_add_u32 v22, s60, v16\n"
        "ds_read_b128 v[44:47], v22 offset:0\n"
        "v_add_u32 v23, s61, v16\n"
        "ds_read_b128 v[48:51], v23 offset:0\n"
        "v_add_u32 v22, s62, v16\n"
        "ds_read_b128 v[52:55], v22 offset:0\n"
        "v_add_u32 v23, s60, v17\n"
        "ds_read_b128 v[56:59], v23 offset:0\n"
        "s_waitcnt vmcnt(10)\n"
        "ds_read_b128 v[96:99], %[vbr] offset:0\n"
        "ds_read_b128 v[104:107], v11 offset:0\n"
        "s_waitcnt vmcnt(8)\n"
        "ds_read_b128 v[100:103], %[vbr] offset:2048\n"
        "ds_read_b128 v[108:111], v11 offset:2048\n"
        "s_waitcnt lgkmcnt(0)\n"
        "s_sub_u32 m0, %[sring], 384\n"
        "s_movk_i32 s58, 0\n"
        "buffer_load_dwordx4 %[vxe], %[srdx], s58 offen offset:384 lds\n"
        "s_add_u32 m0, %[sring], 640\n"
        "s_movk_i32 s58, 10240\n"
        "buffer_load_dwordx4 v12, %[srdx], s58 offen offset:384 lds\n"
        "s_add_u32 m0, %[sring], 1664\n"
        "s_movk_i32 s58, 20480\n"
        "buffer_load_dwordx4 %[vxe], %[srdx], s58 offen offset:384 lds\n"
        "s_add_u32 m0, %[sring], 2688\n"
        "s_movk_i32 s58, 30720\n"
        "buffer_load_dwordx4 v12, %[srdx], s58 offen offset:384 lds\n"
        "s_waitcnt vmcnt(10)\n"
        "ds_read_b128 v[112:115], %[vbr] offset:4096\n"
        "ds_read_b128 v[120:123], v11 offset:4096\n"
        "v_add_u32 v22, s61, v17\n"
        "ds_read_b128 v[60:63], v22 offset:0\n"
        "v_mfma_f32_16x16x32_bf16 v[64:67], v[96:99], v[44:47], 0\n"
        "v_mfma_f32_16x16x32_bf16 v[24:27], v[96:99], v[40:43], 0\n"
        "v_mfma_f32_16x16x32_bf16 v[68:71], v[100:103], v[44:47], 0\n"
        "v_mfma_f32_16x16x32_bf16 v[28:31], v[96:99], v[96:99], 0\n"
        "s_waitcnt vmcnt(8)\n"
        "ds_read_b128 v[116:119], %[vbr] offset:6144\n"
        "ds_read_b128 v[124:127], v11 offset:6144\n"
        "v_add_u32 v23, s62, v17\n"
        "ds_read_b128 v[44:47], v23 offset:0\n"
        "v_mfma_f32_16x16x32_bf16 v[72:75], v[96:99], v[48:51], 0\n"
        "v_mfma_f32_16x16x32_bf16 v[32:35], v[100:103], v[40:43], 0\n"
        "v_mfma_f32_16x16x32_bf16 v[76:79], v[100:103], v[48:51], 0\n"
        "v_mfma_f32_16x16x32_bf16 v[36:39], v[100:103], v[100:103], 0\n"
        "s_waitcnt lgkmcnt(4)\n"
        "s_add_u32 m0, %[sring], 3584\n"
        "s_movk_i32 s58, 0\n"
        "buffer_load_dwordx4 %[vxe], %[srdx], s58 offen offset:512 lds\n"
        "v_add_u32 v22, s60, v18\n"
        "ds_read_b128 v[48:51], v22 offset:0\n"
        "v_mfma_f32_16x16x32_bf16 v[80:83], v[96:99], v[52:55], 0\n"
        "v_mfma_f32_16x16x32_bf16 v[24:27], v[104:107], v[40:43], v[24:27]\n"
        "v_mfma_f32_16x16x32_bf16 v[84:87], v[100:103], v[52:55], 0\n"
        "v_mfma_f32_16x16x32_bf16 v[28:31], v[104:107], v[104:107], v[28:31]\n"
        "s_add_u32 m0, %[sring], 4608\n"
        "s_movk_i32 s58, 10240\n"
        "buffer_load_dwordx4 v12, %[srdx], s58 offen offset:512 lds\n"
        "v_add_u32 v23, s61, v18\n"
        "ds_read_b128 v[52:55], v23 offset:0\n"
        "v_mfma_f32_16x16x32_bf16 v[64:67], v[104:107], v[56:59], v[64:67]\n"
        "v_mfma_f32_16x16x32_bf16 v[32:35], v[108:111], v[40:43], v[32:35]\n"
        "v_mfma_f32_16x16x32_bf16 v[68:71], v[108:111], v[56:59], v[68:71]\n"
        "v_mfma_f32_16x16x32_bf16 v[36:39], v[108:111], v[108:111], v[36:39]\n"
        "s_waitcnt lgkmcnt(3)\n"
        "s_add_u32 m0, %[sring], 5632\n"
        "s_movk_i32 s58, 20480\n"
        "buffer_load_dwordx4 %[vxe], %[srdx], s58 offen offset:512 lds\n"
        "v_add_u32 v22, s62, v18\n"
        "ds_read_b128 v[56:59], v22 offset:0\n"
        "v_mfma_f32_16x16x32_bf16 v[72:75], v[104:107], v[60:63], v[72:75]\n"
        "v_mfma_f32_16x16x32_bf16 v[76:79], v[108:111], v[60:63], v[76:79]\n"
        "s_add_u32 m0, %[sring], 6656\n"
        "s_movk_i32 s58, 30720\n"
        "buffer_load_dwordx4 v12, %[srdx], s58 offen offset:512 lds\n"
        "v_add_u32 v23, s60, v19\n"
        "ds_read_b128 v[60:63], v23 offset:0\n"
        "s_waitcnt lgkmcnt(4)\n"
        "v_mfma_f32_16x16x32_bf16 v[80:83], v[104:107], v[44:47], v[80:83]\n"
        "v_mfma_f32_16x16x32_bf16 v[84:87], v[108:111], v[44:47], v[84:87]\n"
        "s_waitcnt vmcnt(10)\n"
        "ds_read_b128 v[128:131], %[vbr] offset:8192\n"
        "ds_read_b128 v[136:139], v11 offset:8192\n"
        "v_add_u32 v22, s61, v19\n"
        "ds_read_b128 v[44:47], v22 offset:0\n"
        "s_waitcnt lgkmcnt(6)\n"
        "v_mfma_f32_16x16x32_bf16 v[64:67], v[112:115], v[48:51], v[64:67]\n"
        "v_mfma_f32_16x16x32_bf16 v[24:27], v[112:115], v[40:43], v[24:27]\n"
        "v_mfma_f32_16x16x32_bf16 v[68:71], v[116:119], v[48:51], v[68:71]\n"
        "v_mfma_f32_16x16x32_bf16 v[28:31], v[112:115], v[112:115], v[28:31]\n"
        "s_waitcnt vmcnt(8)\n"
        "ds_read_b128 v[132:135], %[vbr] offset:10240\n"
        "ds_read_b128 v[140:143], v11 offset:10240\n"
        "v_add_u32 v23, s62, v19\n"
        "ds_read_b128 v[48:51], v23 offset:0\n"
        "s_waitcnt lgkmcnt(8)\n"
        "v_mfma_f32_16x16x32_bf16 v[72:75], v[112:115], v[52:55], v[72:75]\n"
        "v_mfma_f32_16x16x32_bf16 v[32:35], v[116:119], v[40:43], v[32:35]\n"
        "v_mfma_f32_16x16x32_bf16 v[76:79], v[116:119], v[52:55], v[76:79]\n"
        "v_mfma_f32_16x16x32_bf16 v[36:39], v[116:119], v[116:119], v[36:39]\n"
        "s_waitcnt lgkmcnt(4)\n"
        "s_add_u32 m0, %[sring], 7552\n"
        "s_movk_i32 s58, 0\n"
        "buffer_load_dwordx4 %[vxe], %[srdx], s58 offen offset:640 lds\n"
        "v_add_u32 v22, s60, v16\n"
        "ds_read_b128 v[52:55], v22 offset:256\n"
        "v_mfma_f32_16x16x32_bf16 v[80:83], v[112:115], v[56:59], v[80:83]\n"
        "v_mfma_f32_16x16x32_bf16 v[24:27], v[120:123], v[40:43], v[24:27]\n"
        "v_mfma_f32_16x16x32_bf16 v[84:87], v[116:119], v[56:59], v[84:87]\n"
        "v_mfma_f32_16x16x32_bf16 v[28:31], v[120:123], v[120:123], v[28:31]\n"
        "s_add_u32 m0, %[sring], 8576\n"
        "s_movk_i32 s58, 10240\n"
        "buffer_load_dwordx4 v12, %[srdx], s58 offen offset:640 lds\n"
        "v_add_u32 v23, s61, v16\n"
        "ds_read_b128 v[56:59], v23 offset:256\n"
        "v_mfma_f32_16x16x32_bf16 v[64:67], v[120:123], v[60:63], v[64:67]\n"
        "v_mfma_f32_16x16x32_bf16 v[32:35], v[124:127], v[40:43], v[32:35]\n"
        "v_mfma_f32_16x16x32_bf16 v[68:71], v[124:127], v[60:63], v[68:71]\n"
        "v_mfma_f32_16x16x32_bf16 v[36:39], v[124:127], v[124:127], v[36:39]\n"
        "s_waitcnt lgkmcnt(3)\n"
        "s_add_u32 m0, %[sring], 9600\n"
        "s_movk_i32 s58, 20480\n"
        "buffer_load_dwordx4 %[vxe], %[srdx], s58 offen offset:640 lds\n"
        "v_add_u32 v22, s62, v16\n"
        "ds_read_b128 v[60:63], v22 offset:256\n"
        "v_mfma_f32_16x16x32_bf16 v[72:75], v[120:123], v[44:47], v[72:75]\n"
        "v_mfma_f32_16x16x32_bf16 v[76:79], v[124:127], v[44:47], v[76:79]\n"
        "s_add_u32 m0, %[sring], 10624\n"
        "s_movk_i32 s58, 30720\n"
        "buffer_load_dwordx4 v12, %[srdx], s58 offen offset:640 lds\n"
        "v_add_u32 v23, s60, v17\n"
        "ds_read_b128 v[44:47], v23 offset:256\n"
        "s_waitcnt lgkmcnt(4)\n"
        "v_mfma_f32_16x16x32_bf16 v[80:83], v[120:123], v[48:51], v[80:83]\n"
        "v_mfma_f32_16x16x32_bf16 v[84:87], v[124:127], v[48:51], v[84:87]\n"
        "s_waitcnt vmcnt(10)\n"
        "ds_read_b128 v[144:147], %[vbr] offset:0\n"
        "ds_read_b128 v[152:155], v11 offset:0\n"
        "v_add_u32 v22, s61, v17\n"
        "ds_read_b128 v[48:51], v22 offset:256\n"
        "s_waitcnt lgkmcnt(6)\n"
        "v_mfma_f32_16x16x32_bf16 v[64:67], v[128:131], v[52:55], v[64:67]\n"
        "v_mfma_f32_16x16x32_bf16 v[24:27], v[128:131], v[40:43], v[24:27]\n"
        "v_mfma_f32_16x16x32_bf16 v[68:71], v[132:135], v[52:55], v[68:71]\n"
        "v_mfma_f32_16x16x32_bf16 v[28:31], v[128:131], v[128:131], v[28:31]\n"
        "s_waitcnt vmcnt(8)\n"
        "ds_read_b128 v[148:151], %[vbr] offset:2048\n"
        "ds_read_b128 v[156:159], v11 offset:2048\n"
        "v_add_u32 v23, s62, v17\n"
        "ds_read_b128 v[52:55], v23 offset:256\n"
        "s_waitcnt lgkmcnt(8)\n"
        "v_mfma_f32_16x16x32_bf16 v[72:75], v[128:131], v[56:59], v[72:75]\n"
        "v_mfma_f32_16x16x32_bf16 v[32:35], v[132:135], v[40:43], v[32:35]\n"
        "v_mfma_f32_16x16x32_bf16 v[76:79], v[132:135], v[56:59], v[76:79]\n"
        "v_mfma_f32_16x16x32_bf16 v[36:39], v[132:135], v[132:135], v[36:39]\n"
        "s_waitcnt lgkmcnt(4)\n"
        "s_sub_u32 m0, %[sring], 768\n"
        "s_movk_i32 s58, 0\n"
        "buffer_load_dwordx4 %[vxe], %[srdx], s58 offen offset:768 lds\n"
        "v_add_u32 v22, s60, v18\n"
        "ds_read_b128 v[56:59], v22 offset:256\n"
        "v_mfma_f32_16x16x32_bf16 v[80:83], v[128:131], v[60:63], v[80:83]\n"
        "v_mfma_f32_16x16x32_bf16 v[24:27], v[136:139], v[40:43], v[24:27]\n"
        "v_mfma_f32_16x16x32_bf16 v[84:87], v[132:135], v[60:63], v[84:87]\n"
        "v_mfma_f32_16x16x32_bf16 v[28:31], v[136:139], v[136:139], v[28:31]\n"
        "s_add_u32 m0, %[sring], 256\n"
        "s_movk_i32 s58, 10240\n"
        "buffer_load_dwordx4 v12, %[srdx], s58 offen offset:768 lds\n"
        "v_add_u32 v23, s61, v18\n"
        "ds_read_b128 v[60:63], v23 offset:256\n"
        "v_mfma_f32_16x16x32_bf16 v[64:67], v[136:139], v[44:47], v[64:67]\n"
        "v_mfma_f32_16x16x32_bf16 v[32:35], v[140:143], v[40:43], v[32:35]\n"
        "v_mfma_f32_16x16x32_bf16 v[68:71], v[140:143], v[44:47], v[68:71]\n"
        "v_mfma_f32_16x16x32_bf16 v[36:39], v[140:143], v[140:143], v[36:39]\n"
        "s_waitcnt lgkmcnt(3)\n"
        "s_add_u32 m0, %[sring], 1280\n"
        "s_movk_i32 s58, 20480\n"
        "buffer_load_dwordx4 %[vxe], %[srdx], s58 offen offset:768 lds\n"
        "v_add_u32 v22, s62, v18\n"
        "ds_read_b128 v[44:47], v22 offset:256\n"
        "v_mfma_f32_16x16x32_bf16 v[72:75], v[136:139], v[48:51], v[72:75]\n"
        "v_mfma_f32_16x16x32_bf16 v[76:79], v[140:143], v[48:51], v[76:79]\n"
        "s_add_u32 m0, %[sring], 2304\n"
        "s_movk_i32 s58, 30720\n"
        "buffer_load_dwordx4 v12, %[srdx], s58 offen offset:768 lds\n"
        "v_add_u32 v23, s60, v19\n"
        "ds_read_b128 v[48:51], v23 offset:256\n"
        "s_waitcnt lgkmcnt(4)\n"
        "v_mfma_f32_16x16x32_bf16 v[80:83], v[136:139], v[52:55], v[80:83]\n"
        "v_mfma_f32_16x16x32_bf16 v[84:87], v[140:143], v[52:55], v[84:87]\n"
        "s_waitcnt vmcnt(10)\n"
        "ds_read_b128 v[160:163], %[vbr] offset:4096\n"
        "ds_read_b128 v[168:171], v11 offset:4096\n"
        "v_add_u32 v22, s61, v19\n"
        "ds_read_b128 v[52:55], v22 offset:256\n"
        "s_waitcnt lgkmcnt(6)\n"
        "v_mfma_f32_16x16x32_bf16 v[64:67], v[144:147], v[56:59], v[64:67]\n"
        "v_mfma_f32_16x16x32_bf16 v[24:27], v[144:147], v[40:43], v[24:27]\n"
        "v_mfma_f32_16x16x32_bf16 v[68:71], v[148:151], v[56:59], v[68:71]\n"
        "v_mfma_f32_16x16x32_bf16 v[28:31], v[144:147], v[144:147], v[28:31]\n"
        "s_waitcnt vmcnt(8)\n"
        "ds_read_b128 v[164:167], %[vbr] offset:6144\n"
        "ds_read_b128 v[172:175], v11 offset:6144\n"
        "v_add_u32 v23, s62, v19\n"
        "ds_read_b128 v[56:59], v23 offset:256\n"
        "s_waitcnt lgkmcnt(8)\n"
        "v_mfma_f32_16x16x32_bf16 v[72:75], v[144:147], v[60:63], v[72:75]\n"
        "v_mfma_f32_16x16x32_bf16 v[32:35], v[148:151], v[40:43], v[32:35]\n"
        "v_mfma_f32_16x16x32_bf16 v[76:79], v[148:151], v[60:63], v[76:79]\n"
        "v_mfma_f32_16x16x32_bf16 v[36:39], v[148:151], v[148:151], v[36:39]\n"
        "s_waitcnt lgkmcnt(4)\n"
        "s_add_u32 m0, %[sring], 3200\n"
        "s_movk_i32 s58, 0\n"
        "buffer_load_dwordx4 %[vxe], %[srdx], s58 offen offset:896 lds\n"
        "v_add_u32 v22, s60, v16\n"
        "ds_read_b128 v[60:63], v22 offset:512\n"
        "v_mfma_f32_16x16x32_bf16 v[80:83], v[144:147], v[44:47], v[80:83]\n"
        "v_mfma_f32_16x16x32_bf16 v[24:27], v[152:155], v[40:43], v[24:27]\n"
        "v_mfma_f32_16x16x32_bf16 v[84:87], v[148:151], v[44:47], v[84:87]\n"
        "v_mfma_f32_16x16x32_bf16 v[28:31], v[152:155], v[152:155], v[28:31]\n"
        "s_add_u32 m0, %[sring], 4224\n"
        "s_movk_i32 s58, 10240\n"
        "buffer_load_dwordx4 v12, %[srdx], s58 offen offset:896 lds\n"
        "v_add_u32 v23, s61, v16\n"
        "ds_read_b128 v[44:47], v23 offset:512\n"
        "v_mfma_f32_16x16x32_bf16 v[64:67], v[152:155], v[48:51], v[64:67]\n"
        "v_mfma_f32_16x16x32_bf16 v[32:35], v[156:159], v[40:43], v[32:35]\n"
        "v_mfma_f32_16x16x32_bf16 v[68:71], v[156:159], v[48:51], v[68:71]\n"
        "v_mfma_f32_16x16x32_bf16 v[36:39], v[156:159], v[156:159], v[36:39]\n"
        "s_waitcnt lgkmcnt(3)\n"
        "s_add_u32 m0, %[sring], 5248\n"
        "s_movk_i32 s58, 20480\n"
        "buffer_load_dwordx4 %[vxe], %[srdx], s58 offen offset:896 lds\n"
        "v_add_u32 v22, s62, v16\n"
        "ds_read_b128 v[48:51], v22 offset:512\n"
        "v_mfma_f32_16x16x32_bf16 v[72:75], v[152:155], v[52:55], v[72:75]\n"
        "v_mfma_f32_16x16x32_bf16 v[76:79], v[156:159], v[52:55], v[76:79]\n"
        "s_add_u32 m0, %[sring], 6272\n"
        "s_movk_i32 s58, 30720\n"
        "buffer_load_dwordx4 v12, %[srdx], s58 offen offset:896 lds\n"
        "v_add_u32 v23, s60, v17\n"
        "ds_read_b128 v[52:55], v23 offset:512\n"
        "s_waitcnt lgkmcnt(4)\n"
        "v_mfma_f32_16x16x32_bf16 v[80:83], v[152:155], v[56:59], v[80:83]\n"
        "v_mfma_f32_16x16x32_bf16 v[84:87], v[156:159], v[56:59], v[84:87]\n"
        "s_waitcnt vmcnt(10)\n"
        "ds_read_b128 v[176:179], %[vbr] offset:8192\n"
        "ds_read_b128 v[184:187], v11 offset:8192\n"
        "v_add_u32 v22, s61, v17\n"
        "ds_read_b128 v[56:59], v22 offset:512\n"
        "s_waitcnt lgkmcnt(6)\n"
        "v_mfma_f32_16x16x32_bf16 v[64:67], v[160:163], v[60:63], v[64:67]\n"
        "v_mfma_f32_16x16x32_bf16 v[24:27], v[160:163], v[40:43], v[24:27]\n"
        "v_mfma_f32_16x16x32_bf16 v[68:71], v[164:167], v[60:63], v[68:71]\n"
        "v_mfma_f32_16x16x32_bf16 v[28:31], v[160:163], v[160:163], v[28:31]\n"
        "s_waitcnt vmcnt(8)\n"
        "ds_read_b128 v[180:183], %[vbr] offset:10240\n"
        "ds_read_b128 v[188:191], v11 offset:10240\n"
        "v_add_u32 v23, s62, v17\n"
        "ds_read_b128 v[60:63], v23 offset:512\n"
        "s_waitcnt lgkmcnt(8)\n"
        "v_mfma_f32_16x16x32_bf16 v[72:75], v[160:163], v[44:47], v[72:75]\n"
        "v_mfma_f32_16x16x32_bf16 v[32:35], v[164:167], v[40:43], v[32:35]\n"
        "v_mfma_f32_16x16x32_bf16 v[76:79], v[164:167], v[44:47], v[76:79]\n"
        "v_mfma_f32_16x16x32_bf16 v[36:39], v[164:167], v[164:167], v[36:39]\n"
        "s_waitcnt lgkmcnt(4)\n"
        "s_add_u32 m0, %[sring], 7168\n"
        "s_movk_i32 s58, 0\n"
        "buffer_load_dwordx4 %[vxe], %[srdx], s58 offen offset:1024 lds\n"
        "v_add_u32 v22, s60, v18\n"
        "ds_read_b128 v[44:47], v22 offset:512\n"
        "v_mfma_f32_16x16x32_bf16 v[80:83], v[160:163], v[48:51], v[80:83]\n"
        "v_mfma_f32_16x16x32_bf16 v[24:27], v[168:171], v[40:43], v[24:27]\n"
        "v_mfma_f32_16x16x32_bf16 v[84:87], v[164:167], v[48:51], v[84:87]\n"
        "v_mfma_f32_16x16x32_bf16 v[28:31], v[168:171], v[168:171], v[28:31]\n"
        "s_add_u32 m0, %[sring], 8192\n"
        "s_movk_i32 s58, 10240\n"
        "buffer_load_dwordx4 v12, %[srdx], s58 offen offset:1024 lds\n"
        "v_add_u32 v23, s61, v18\n"
        "ds_read_b128 v[48:51], v23 offset:512\n"
        "v_mfma_f32_16x16x32_bf16 v[64:67], v[168:171], v[52:55], v[64:67]\n"
        "v_mfma_f32_16x16x32_bf16 v[32:35], v[172:175], v[40:43], v[32:35]\n"
        "v_mfma_f32_16x16x32_bf16 v[68:71], v[172:175], v[52:55], v[68:71]\n"
        "v_mfma_f32_16x16x32_bf16 v[36:39], v[172:175], v[172:175], v[36:39]\n"
        "s_waitcnt lgkmcnt(3)\n"
        "s_add_u32 m0, %[sring], 9216\n"
        "s_movk_i32 s58, 20480\n"
        "buffer_load_dwordx4 %[vxe], %[srdx], s58 offen offset:1024 lds\n"
        "v_add_u32 v22, s62, v18\n"
        "ds_read_b128 v[52:55], v22 offset:512\n"
        "v_mfma_f32_16x16x32_bf16 v[72:75], v[168:171], v[56:59], v[72:75]\n"
        "v_mfma_f32_16x16x32_bf16 v[76:79], v[172:175], v[56:59], v[76:79]\n"
        "s_add_u32 m0, %[sring], 10240\n"
        "s_movk_i32 s58, 30720\n"
        "buffer_load_dwordx4 v12, %[srdx], s58 offen offset:1024 lds\n"
        "v_add_u32 v23, s60, v19\n"
        "ds_read_b128 v[56:59], v23 offset:512\n"
        "s_waitcnt lgkmcnt(4)\n"
        "v_mfma_f32_16x16x32_bf16 v[80:83], v[168:171], v[60:63], v[80:83]\n"
        "v_mfma_f32_16x16x32_bf16 v[84:87], v[172:175], v[60:63], v[84:87]\n"
        "s_waitcnt vmcnt(10)\n"
        "ds_read_b128 v[192:195], %[vbr] offset:0\n"
        "ds_read_b128 v[200:203], v11 offset:0\n"
        "v_add_u32 v22, s61, v19\n"
        "ds_read_b128 v[60:63], v22 offset:512\n"
        "s_waitcnt lgkmcnt(6)\n"
        "v_mfma_f32_16x16x32_bf16 v[64:67], v[176:179], v[44:47], v[64:67]\n"
        "v_mfma_f32_16x16x32_bf16 v[24:27], v[176:179], v[40:43], v[24:27]\n"
        "v_mfma_f32_16x16x32_bf16 v[68:71], v[180:183], v[44:47], v[68:71]\n"
        "v_mfma_f32_16x16x32_bf16 v[28:31], v[176:179], v[176:179], v[28:31]\n"
        "s_waitcnt vmcnt(8)\n"
        "ds_read_b128 v[196:199], %[vbr] offset:2048\n"
        "ds_read_b128 v[204:207], v11 offset:2048\n"
        "v_add_u32 v23, s62, v19\n"
        "ds_read_b128 v[44:47], v23 offset:512\n"
        "s_waitcnt lgkmcnt(8)\n"
        "v_mfma_f32_16x16x32_bf16 v[72:75], v[176:179], v[48:51], v[72:75]\n"
        "v_mfma_f32_16x16x32_bf16 v[32:35], v[180:183], v[40:43], v[32:35]\n"
        "v_mfma_f32_16x16x32_bf16 v[76:79], v[180:183], v[48:51], v[76:79]\n"
        "v_mfma_f32_16x16x32_bf16 v[36:39], v[180:183], v[180:183], v[36:39]\n"
        "s_waitcnt lgkmcnt(4)\n"
        "s_sub_u32 m0, %[sring], 1152\n"
        "s_movk_i32 s58, 0\n"
        "buffer_load_dwordx4 %[vxe], %[srdx], s58 offen offset:1152 lds\n"
        "v_add_u32 v22, s60, v16\n"
        "ds_read_b128 v[48:51], v22 offset:768\n"
        "v_mfma_f32_16x16x32_bf16 v[80:83], v[176:179], v[52:55], v[80:83]\n"
        "v_mfma_f32_16x16x32_bf16 v[24:27], v[184:187], v[40:43], v[24:27]\n"
        "v_mfma_f32_16x16x32_bf16 v[84:87], v[180:183], v[52:55], v[84:87]\n"
        "v_mfma_f32_16x16x32_bf16 v[28:31], v[184:187], v[184:187], v[28:31]\n"
        "s_sub_u32 m0, %[sring], 128\n"
        "s_movk_i32 s58, 10240\n"
        "buffer_load_dwordx4 v12, %[srdx], s58 offen offset:1152 lds\n"
        "v_add_u32 v23, s61, v16\n"
        "ds_read_b128 v[52:55], v23 offset:768\n"
        "v_mfma_f32_16x16x32_bf16 v[64:67], v[184:187], v[56:59], v[64:67]\n"
        "v_mfma_f32_16x16x32_bf16 v[32:35], v[188:191], v[40:43], v[32:35]\n"
        "v_mfma_f32_16x16x32_bf16 v[68:71], v[188:191], v[56:59], v[68:71]\n"
        "v_mfma_f32_16x16x32_bf16 v[36:39], v[188:191], v[188:191], v[36:39]\n"
        "s_waitcnt lgkmcnt(3)\n"
        "s_add_u32 m0, %[sring], 896\n"
        "s_movk_i32 s58, 20480\n"
        "buffer_load_dwordx4 %[vxe], %[srdx], s58 offen offset:1152 lds\n"
        "v_add_u32 v22, s62, v16\n"
        "ds_read_b128 v[56:59], v22 offset:768\n"
        "v_mfma_f32_16x16x32_bf16 v[72:75], v[184:187], v[60:63], v[72:75]\n"
        "v_mfma_f32_16x16x32_bf16 v[76:79], v[188:191], v[60:63], v[76:79]\n"
        "s_add_u32 m0, %[sring], 1920\n"
        "s_movk_i32 s58, 30720\n"
        "buffer_load_dwordx4 v12, %[srdx], s58 offen offset:1152 lds\n"
        "v_add_u32 v23, s60, v17\n"
        "ds_read_b128 v[60:63], v23 offset:768\n"
        "s_waitcnt lgkmcnt(4)\n"
        "v_mfma_f32_16x16x32_bf16 v[80:83], v[184:187], v[44:47], v[80:83]\n"
        "v_mfma_f32_16x16x32_bf16 v[84:87], v[188:191], v[44:47], v[84:87]\n"
        "s_waitcnt vmcnt(10)\n"
        "ds_read_b128 v[208:211], %[vbr] offset:4096\n"
        "ds_read_b128 v[216:219], v11 offset:4096\n"
        "v_add_u32 v22, s61, v17\n"
        "ds_read_b128 v[44:47], v22 offset:768\n"
        "s_waitcnt lgkmcnt(6)\n"
        "v_mfma_f32_16x16x32_bf16 v[64:67], v[192:195], v[48:51], v[64:67]\n"
        "v_mfma_f32_16x16x32_bf16 v[24:27], v[192:195], v[40:43], v[24:27]\n"
        "v_mfma_f32_16x16x32_bf16 v[68:71], v[196:199], v[48:51], v[68:71]\n"
        "v_mfma_f32_16x16x32_bf16 v[28:31], v[192:195], v[192:195], v[28:31]\n"
        "s_waitcnt vmcnt(8)\n"
        "ds_read_b128 v[212:215], %[vbr] offset:6144\n"
        "ds_read_b128 v[220:223], v11 offset:6144\n"
        "v_add_u32 v23, s62, v17\n"
        "ds_read_b128 v[48:51], v23 offset:768\n"
        "s_waitcnt lgkmcnt(8)\n"
        "v_mfma_f32_16x16x32_bf16 v[72:75], v[192:195], v[52:55], v[72:75]\n"
        "v_mfma_f32_16x16x32_bf16 v[32:35], v[196:199], v[40:43], v[32:35]\n"
        "v_mfma_f32_16x16x32_bf16 v[76:79], v[196:199], v[52:55], v[76:79]\n"
        "v_mfma_f32_16x16x32_bf16 v[36:39], v[196:199], v[196:199], v[36:39]\n"
        "s_waitcnt lgkmcnt(4)\n"
        "s_add_u32 s58, %[scw], 0\n"
        "s_add_u32 m0, %[sring], 4096\n"
        "s_and_b32 s58, s58, 0x7ffff\n"
        "buffer_load_dwordx4 %[vce], %[srdc], s58 offen offset:0 lds\n"
        "v_add_u32 v22, s60, v18\n"
        "ds_read_b128 v[52:55], v22 offset:768\n"
        "v_mfma_f32_16x16x32_bf16 v[80:83], v[192:195], v[56:59], v[80:83]\n"
        "v_mfma_f32_16x16x32_bf16 v[24:27], v[200:203], v[40:43], v[24:27]\n"
        "v_mfma_f32_16x16x32_bf16 v[84:87], v[196:199], v[56:59], v[84:87]\n"
        "v_mfma_f32_16x16x32_bf16 v[28:31], v[200:203], v[200:203], v[28:31]\n"
        "s_add_u32 s58, %[scw], 32768\n"
        "s_add_u32 m0, %[sring], 5120\n"
        "s_and_b32 s58, s58, 0x7ffff\n"
        "buffer_load_dwordx4 v13, %[srdc], s58 offen offset:0 lds\n"
        "v_add_u32 v23, s61, v18\n"
        "ds_read_b128 v[56:59], v23 offset:768\n"
        "v_mfma_f32_16x16x32_bf16 v[64:67], v[200:203], v[60:63], v[64:67]\n"
        "v_mfma_f32_16x16x32_bf16 v[32:35], v[204:207], v[40:43], v[32:35]\n"
        "v_mfma_f32_16x16x32_bf16 v[68:71], v[204:207], v[60:63], v[68:71]\n"
        "v_mfma_f32_16x16x32_bf16 v[36:39], v[204:207], v[204:207], v[36:39]\n"
        "s_waitcnt lgkmcnt(3)\n"
        "s_add_u32 s58, %[scw], 65536\n"
        "s_add_u32 m0, %[sring], 6144\n"
        "s_and_b32 s58, s58, 0x7ffff\n"
        "buffer_load_dwordx4 %[vce], %[srdc], s58 offen offset:0 lds\n"
        "v_add_u32 v22, s62, v18\n"
        "ds_read_b128 v[60:63], v22 offset:768\n"
        "v_mfma_f32_16x16x32_bf16 v[72:75], v[200:203], v[44:47], v[72:75]\n"
        "v_mfma_f32_16x16x32_bf16 v[76:79], v[204:207], v[44:47], v[76:79]\n"
        "s_add_u32 s58, %[scw], 98304\n"
        "s_add_u32 m0, %[sring], 7168\n"
        "s_and_b32 s58, s58, 0x7ffff\n"
        "buffer_load_dwordx4 v13, %[srdc], s58 offen offset:0 lds\n"
        "v_add_u32 v23, s60, v19\n"
        "ds_read_b128 v[44:47], v23 offset:768\n"
        "s_waitcnt lgkmcnt(4)\n"
        "v_mfma_f32_16x16x32_bf16 v[80:83], v[200:203], v[48:51], v[80:83]\n"
        "v_mfma_f32_16x16x32_bf16 v[84:87], v[204:207], v[48:51], v[84:87]\n"
        "s_waitcnt vmcnt(10)\n"
        "ds_read_b128 v[224:227], %[vbr] offset:8192\n"
        "ds_read_b128 v[232:235], v11 offset:8192\n"
        "v_add_u32 v22, s61, v19\n"
        "ds_read_b128 v[48:51], v22 offset:768\n"
        "s_waitcnt lgkmcnt(6)\n"
        "v_mfma_f32_16x16x32_bf16 v[64:67], v[208:211], v[52:55], v[64:67]\n"
        "v_mfma_f32_16x16x32_bf16 v[24:27], v[208:211], v[40:43], v[24:27]\n"
        "v_mfma_f32_16x16x32_bf16 v[68:71], v[212:215], v[52:55], v[68:71]\n"
        "v_mfma_f32_16x16x32_bf16 v[28:31], v[208:211], v[208:211], v[28:31]\n"
        "s_waitcnt vmcnt(8)\n"
        "ds_read_b128 v[228:231], %[vbr] offset:10240\n"
        "ds_read_b128 v[236:239], v11 offset:10240\n"
        "v_add_u32 v23, s62, v19\n"
        "ds_read_b128 v[52:55], v23 offset:768\n"
        "s_waitcnt lgkmcnt(8)\n"
        "v_mfma_f32_16x16x32_bf16 v[72:75], v[208:211], v[56:59], v[72:75]\n"
        "v_mfma_f32_16x16x32_bf16 v[32:35], v[212:215], v[40:43], v[32:35]\n"
        "v_mfma_f32_16x16x32_bf16 v[76:79], v[212:215], v[56:59], v[76:79]\n"
        "v_mfma_f32_16x16x32_bf16 v[36:39], v[212:215], v[212:215], v[36:39]\n"
        "s_waitcnt lgkmcnt(4)\n"
        "s_add_u32 s58, %[scw], 131072\n"
        "s_add_u32 m0, %[sring], 8192\n"
        "s_and_b32 s58, s58, 0x7ffff\n"
        "buffer_load_dwordx4 %[vce], %[srdc], s58 offen offset:0 lds\n"
        "v_add_u32 v22, s60, v16\n"
        "ds_read_b128 v[56:59], v22 offset:1024\n"
        "v_mfma_f32_16x16x32_bf16 v[80:83], v[208:211], v[60:63], v[80:83]\n"
        "v_mfma_f32_16x16x32_bf16 v[24:27], v[216:219], v[40:43], v[24:27]\n"
        "v_mfma_f32_16x16x32_bf16 v[84:87], v[212:215], v[60:63], v[84:87]\n"
        "v_mfma_f32_16x16x32_bf16 v[28:31], v[216:219], v[216:219], v[28:31]\n"
        "s_add_u32 s58, %[scw], 163840\n"
        "s_add_u32 m0, %[sring], 9216\n"
        "s_and_b32 s58, s58, 0x7ffff\n"
        "buffer_load_dwordx4 v13, %[srdc], s58 offen offset:0 lds\n"
        "v_add_u32 v23, s61, v16\n"
        "ds_read_b128 v[60:63], v23 offset:1024\n"
        "v_mfma_f32_16x16x32_bf16 v[64:67], v[216:219], v[44:47], v[64:67]\n"
        "v_mfma_f32_16x16x32_bf16 v[32:35], v[220:223], v[40:43], v[32:35]\n"
        "v_mfma_f32_16x16x32_bf16 v[68:71], v[220:223], v[44:47], v[68:71]\n"
        "v_mfma_f32_16x16x32_bf16 v[36:39], v[220:223], v[220:223], v[36:39]\n"
        "s_waitcnt lgkmcnt(3)\n"
        "s_add_u32 s58, %[scw], 196608\n"
        "s_add_u32 m0, %[sring], 10240\n"
        "s_and_b32 s58, s58, 0x7ffff\n"
        "buffer_load_dwordx4 %[vce], %[srdc], s58 offen offset:0 lds\n"
        "v_add_u32 v22, s62, v16\n"
        "ds_read_b128 v[44:47], v22 offset:1024\n"
        "v_mfma_f32_16x16x32_bf16 v[72:75], v[216:219], v[48:51], v[72:75]\n"
        "v_mfma_f32_16x16x32_bf16 v[76:79], v[220:223], v[48:51], v[76:79]\n"
        "s_add_u32 s58, %[scw], 229376\n"
        "s_add_u32 m0, %[sring], 11264\n"
        "s_and_b32 s58, s58, 0x7ffff\n"
        "buffer_load_dwordx4 v13, %[srdc], s58 offen offset:0 lds\n"
        "v_add_u32 v23, s60, v17\n"
        "ds_read_b128 v[48:51], v23 offset:1024\n"
        "s_waitcnt lgkmcnt(4)\n"
        "v_mfma_f32_16x16x32_bf16 v[80:83], v[216:219], v[52:55], v[80:83]\n"
        "v_mfma_f32_16x16x32_bf16 v[84:87], v[220:223], v[52:55], v[84:87]\n"
        "s_waitcnt vmcnt(10)\n"
        "ds_read_b128 v[240:243], %[vbr] offset:0\n"
        "ds_read_b128 v[248:251], v11 offset:0\n"
        "v_add_u32 v22, s61, v17\n"
        "ds_read_b128 v[52:55], v22 offset:1024\n"
        "s_waitcnt lgkmcnt(6)\n"
        "v_mfma_f32_16x16x32_bf16 v[64:67], v[224:227], v[56:59], v[64:67]\n"
        "v_mfma_f32_16x16x32_bf16 v[24:27], v[224:227], v[40:43], v[24:27]\n"
        "v_mfma_f32_16x16x32_bf16 v[68:71], v[228:231], v[56:59], v[68:71]\n"
        "v_mfma_f32_16x16x32_bf16 v[28:31], v[224:227], v[224:227], v[28:31]\n"
        "s_waitcnt vmcnt(8)\n"
        "ds_read_b128 v[244:247], %[vbr] offset:2048\n"
        "ds_read_b128 v[252:255], v11 offset:2048\n"
        "v_add_u32 v23, s62, v17\n"
        "ds_read_b128 v[56:59], v23 offset:1024\n"
        "s_waitcnt lgkmcnt(8)\n"
        "v_mfma_f32_16x16x32_bf16 v[72:75], v[224:227], v[60:63], v[72:75]\n"
        "v_mfma_f32_16x16x32_bf16 v[32:35], v[228:231], v[40:43], v[32:35]\n"
        "v_mfma_f32_16x16x32_bf16 v[76:79], v[228:231], v[60:63], v[76:79]\n"
        "v_mfma_f32_16x16x32_bf16 v[36:39], v[228:231], v[228:231], v[36:39]\n"
        "s_waitcnt lgkmcnt(4)\n"
        "s_add_u32 s58, %[scw], 262144\n"
        "s_add_u32 m0, %[sring], 0\n"
        "s_and_b32 s58, s58, 0x7ffff\n"
        "buffer_load_dwordx4 %[vce], %[srdc], s58 offen offset:0 lds\n"
        "v_add_u32 v22, s60, v18\n"
        "ds_read_b128 v[60:63], v22 offset:1024\n"
        "v_mfma_f32_16x16x32_bf16 v[80:83], v[224:227], v[44:47], v[80:83]\n"
        "v_mfma_f32_16x16x32_bf16 v[24:27], v[232:235], v[40:43], v[24:27]\n"
        "v_mfma_f32_16x16x32_bf16 v[84:87], v[228:231], v[44:47], v[84:87]\n"
        "v_mfma_f32_16x16x32_bf16 v[28:31], v[232:235], v[232:235], v[28:31]\n"
        "s_add_u32 s58, %[scw], 294912\n"
        "s_add_u32 m0, %[sring], 1024\n"
        "s_and_b32 s58, s58, 0x7ffff\n"
        "buffer_load_dwordx4 v13, %[srdc], s58 offen offset:0 lds\n"
        "v_add_u32 v23, s61, v18\n"
        "ds_read_b128 v[44:47], v23 offset:1024\n"
        "v_mfma_f32_16x16x32_bf16 v[64:67], v[232:235], v[48:51], v[64:67]\n"
        "v_mfma_f32_16x16x32_bf16 v[32:35], v[236:239], v[40:43], v[32:35]\n"
        "v_mfma_f32_16x16x32_bf16 v[68:71], v[236:239], v[48:51], v[68:71]\n"
        "v_mfma_f32_16x16x32_bf16 v[36:39], v[236:239], v[236:239], v[36:39]\n"
        "s_waitcnt lgkmcnt(3)\n"
        "s_add_u32 s58, %[scw], 327680\n"
        "s_add_u32 m0, %[sring], 2048\n"
        "s_and_b32 s58, s58, 0x7ffff\n"
        "buffer_load_dwordx4 %[vce], %[srdc], s58 offen offset:0 lds\n"
        "v_add_u32 v22, s62, v18\n"
        "ds_read_b128 v[48:51], v22 offset:1024\n"
        "v_mfma_f32_16x16x32_bf16 v[72:75], v[232:235], v[52:55], v[72:75]\n"
        "v_mfma_f32_16x16x32_bf16 v[76:79], v[236:239], v[52:55], v[76:79]\n"
        "s_add_u32 s58, %[scw], 360448\n"
        "s_add_u32 m0, %[sring], 3072\n"
        "s_and_b32 s58, s58, 0x7ffff\n"
        "buffer_load_dwordx4 v13, %[srdc], s58 offen offset:0 lds\n"
        "v_add_u32 v23, s60, v19\n"
        "ds_read_b128 v[52:55], v23 offset:1024\n"
        "s_waitcnt lgkmcnt(4)\n"
        "v_mfma_f32_16x16x32_bf16 v[80:83], v[232:235], v[56:59], v[80:83]\n"
        "v_mfma_f32_16x16x32_bf16 v[84:87], v[236:239], v[56:59], v[84:87]\n"
        "v_add_u32 v22, s61, v19\n"
        "ds_read_b128 v[56:59], v22 offset:1024\n"
        "s_waitcnt lgkmcnt(4)\n"
        "v_mfma_f32_16x16x32_bf16 v[64:67], v[240:243], v[60:63], v[64:67]\n"
        "v_mfma_f32_16x16x32_bf16 v[24:27], v[240:243], v[40:43], v[24:27]\n"
        "v_mfma_f32_16x16x32_bf16 v[68:71], v[244:247], v[60:63], v[68:71]\n"
        "v_mfma_f32_16x16x32_bf16 v[28:31], v[240:243], v[240:243], v[28:31]\n"
        "v_add_u32 v23, s62, v19\n"
        "ds_read_b128 v[60:63], v23 offset:1024\n"
        "s_waitcnt lgkmcnt(4)\n"
        "v_mfma_f32_16x16x32_bf16 v[72:75], v[240:243], v[44:47], v[72:75]\n"
        "v_mfma_f32_16x16x32_bf16 v[32:35], v[244:247], v[40:43], v[32:35]\n"
        "v_mfma_f32_16x16x32_bf16 v[76:79], v[244:247], v[44:47], v[76:79]\n"
        "v_mfma_f32_16x16x32_bf16 v[36:39], v[244:247], v[244:247], v[36:39]\n"
        "s_waitcnt lgkmcnt(3)\n"
        "v_mfma_f32_16x16x32_bf16 v[80:83], v[240:243], v[48:51], v[80:83]\n"
        "v_mfma_f32_16x16x32_bf16 v[24:27], v[248:251], v[40:43], v[24:27]\n"
        "v_mfma_f32_16x16x32_bf16 v[84:87], v[244:247], v[48:51], v[84:87]\n"
        "v_mfma_f32_16x16x32_bf16 v[28:31], v[248:251], v[248:251], v[28:31]\n"
        "s_waitcnt lgkmcnt(2)\n"
        "v_mfma_f32_16x16x32_bf16 v[64:67], v[248:251], v[52:55], v[64:67]\n"
        "v_mfma_f32_16x16x32_bf16 v[32:35], v[252:255], v[40:43], v[32:35]\n"
        "v_mfma_f32_16x16x32_bf16 v[68:71], v[252:255], v[52:55], v[68:71]\n"
        "v_mfma_f32_16x16x32_bf16 v[36:39], v[252:255], v[252:255], v[36:39]\n"
        "s_waitcnt lgkmcnt(1)\n"
        "v_mfma_f32_16x16x32_bf16 v[72:75], v[248:251], v[56:59], v[72:75]\n"
        "v_mfma_f32_16x16x32_bf16 v[76:79], v[252:255], v[56:59], v[76:79]\n"
        "s_waitcnt lgkmcnt(0)\n"
        "v_mfma_f32_16x16x32_bf16 v[80:83], v[248:251], v[60:63], v[80:83]\n"
        "v_mfma_f32_16x16x32_bf16 v[84:87], v[252:255], v[60:63], v[84:87]\n"
        "s_nop 7\n"
        "s_waitcnt lgkmcnt(0)\n"
        "s_barrier\n"
        : "=&{v[96:127]}"(X0), "=&{v[128:159]}"(X1), "=&{v[160:191]}"(X2), "=&{v[192:223]}"(X3), "=&{v[224:255]}"(X4), "=&{v[64:95]}"(AC), "=&{v[24:39]}"(SQ), "+{v[14:15]}"(non)
        : [vdr] "v"(LD + (l & 15) * 1280 + (((l >> 4) ^ (l & 15)) << 4)),
          [vbr] "v"(LRED + w * 12288 + (l & 15) * 128 + (((l >> 4) ^ ((l & 15) >> 1)) << 4)),
          [vxe] "v"((l >> 3) * 1280 + (((l & 7) ^ (l >> 4)) << 4)),
          [vce] "v"((l >> 3) * 4096 + (((l & 7) ^ (l >> 4)) << 4)),
          [vcw] "v"(T_CS + w * 320 + (l & 15) * 4),
          [sring] "s"(LRED + w * 12288),
          [scw] "s"(w * 131072),
          [srdc] "s"(srdc),
          [sjo] "s"(sjo),
          [srdx] "s"(srdx)
        : "scc", "vcc", "memory", "s58", "s59", "s60", "s61", "s62", "s63", "s64", "s65", "s66", "s67", "s68", "s69", "s70", "s71", "v11", "v12", "v13", "v16", "v17", "v18", "v19", "v22", "v23", "v40", "v41", "v42", "v43", "v44", "v45", "v46", "v47", "v48", "v49", "v50", "v51", "v52", "v53", "v54", "v55", "v56", "v57", "v58", "v59", "v60", "v61", "v62", "v63", "a0", "a1", "a2", "a3", "a4", "a5", "a6", "a7", "a8", "a9", "a10", "a11", "a12", "a13", "a14", "a15", "a16", "a17", "a18", "a19", "a20", "a21", "a22", "a23", "a24", "a25", "a26", "a27", "a28", "a29", "a30", "a31", "a32", "a33", "a34", "a35", "a36", "a37", "a38", "a39", "a40", "a41", "a42", "a43", "a44", "a45", "a46", "a47", "a48", "a49", "a50", "a51", "a52", "a53", "a54", "a55", "a56", "a57", "a58", "a59", "a60", "a61", "a62", "a63", "a64", "a65", "a66", "a67", "a68", "a69", "a70", "a71", "a72", "a73", "a74", "a75", "a76", "a77", "a78", "a79", "a80", "a81", "a82", "a83", "a84", "a85", "a86", "a87", "a88", "a89", "a90", "a91", "a92", "a93", "a94", "a95", "a96", "a97", "a98", "a99", "a100", "a101", "a102", "a103", "a104", "a105", "a106", "a107", "a108", "a109", "a110", "a111", "a112", "a113", "a114", "a115", "a116", "a117", "a118", "a119", "a120", "a121", "a122", "a123", "a124", "a125", "a126", "a127", "a128", "a129", "a130", "a131", "a132", "a133", "a134", "a135", "a136", "a137", "a138", "a139", "a140", "a141", "a142", "a143", "a144", "a145", "a146", "a147", "a148", "a149", "a150", "a151", "a152", "a153", "a154", "a155", "a156", "a157", "a158", "a159", "a160", "a161", "a162", "a163", "a164", "a165", "a166", "a167", "a168", "a169", "a170", "a171", "a172", "a173", "a174", "a175", "a176", "a177", "a178", "a179", "a180", "a181", "a182", "a183", "a184", "a185", "a186", "a187", "a188", "a189", "a190", "a191", "a192", "a193", "a194", "a195", "a196", "a197", "a198", "a199", "a200", "a201", "a202", "a203", "a204", "a205", "a206", "a207", "a208", "a209", "a210", "a211", "a212", "a213", "a214", "a215", "a216", "a217", "a218", "a219", "a220", "a221", "a222", "a223", "a224", "a225", "a226", "a227", "a228", "a229", "a230", "a231", "a232", "a233", "a234", "a235", "a236", "a237", "a238", "a239", "a240", "a241", "a242", "a243", "a244", "a245", "a246", "a247", "a248", "a249", "a250", "a251", "a252", "a253", "a254", "a255");
        ASTAMP_SA;
        LANE();
#pragma unroll
        for (int rt = 0; rt < 2; ++rt) {
            const float mu = diag(SQ[8 * rt], SQ[8 * rt + 1], SQ[8 * rt + 2], SQ[8 * rt + 3], l) * (1.f / 640);
            float m, rs;
            ln_params(mu, diag(SQ[8 * rt + 4], SQ[8 * rt + 5], SQ[8 * rt + 6], SQ[8 * rt + 7], l) * (1.f / 640) - mu * mu, m, rs);
            const unsigned o = w * 32 + rt * 16 + (l & 15);
            ldsw<float>(ondiag(l) ? T_AL + o * 4 : T_G, bep >= 8 ? -rs : rs); ldsw<u32x2>(ondiag(l) ? T_MH + o * 8 : T_C, u32x2{pk(-m, -m), 0u});
        }
        if (tid < 80) {
            const float cs = (ldsr<float>(T_CS + tid * 4) + ldsr<float>(T_CS + 320 + tid * 4)) + (ldsr<float>(T_CS + 640 + tid * 4) + ldsr<float>(T_CS + 960 + tid * 4));
            const unsigned p = pk(cs, cs - rbf(cs));
            ldsw<u32x2>(T_CSF + tid * 8, u32x2{p, p});
        }
    }
    const u64 tag = ((((u64)(unsigned)__builtin_amdgcn_readfirstlane((unsigned)(non >> 32)) << 32) | (unsigned)__builtin_amdgcn_readfirstlane((unsigned)non)) + 1) << 4;
    LANE();
    STAMP(1);
    {
        u64 fl = tag;
        asm volatile(
        "s_nop 4\n"
        "s_bfe_u32 s59, %[sjo], 262144\n"
        "s_mul_i32 s60, s59, 20480\n"
        "s_bfe_u32 s59, %[sjo], 262148\n"
        "s_mul_i32 s61, s59, 20480\n"
        "s_bfe_u32 s59, %[sjo], 262152\n"
        "s_mul_i32 s62, s59, 20480\n"
        "s_bfe_u32 s59, %[sjo], 262156\n"
        "s_mul_i32 s63, s59, 20480\n"
        "s_bfe_u32 s59, %[sjo], 262160\n"
        "s_mul_i32 s64, s59, 20480\n"
        "v_xor_b32 v11, 64, %[vbr]\n"
        "v_xor_b32 v13, 64, %[vce]\n"
        "v_mov_b32 v16, %[vdr]\n"
        "v_xor_b32 v17, 64, %[vdr]\n"
        "v_xor_b32 v18, 0x80, %[vdr]\n"
        "v_xor_b32 v19, 0xc0, %[vdr]\n"
        "s_waitcnt lgkmcnt(0)\n"
        "s_barrier\n"
        "v_add_u32 v22, s63, v16\n"
        "ds_read_b128 v[44:47], v22 offset:0\n"
        "v_add_u32 v23, s63, v17\n"
        "ds_read_b128 v[48:51], v23 offset:0\n"
        "v_add_u32 v22, s63, v18\n"
        "ds_read_b128 v[52:55], v22 offset:0\n"
        "v_add_u32 v23, s63, v19\n"
        "ds_read_b128 v[56:59], v23 offset:0\n"
        "ds_read_b128 v[28:31], %[vtab]\n"
        "ds_read_b128 v[32:35], %[vtab] offset:64\n"
        "ds_read_b64 v[36:37], %[vmh]\n"
        "ds_read_b64 v[38:39], %[vmh] offset:128\n"
        "s_waitcnt vmcnt(10)\n"
        "ds_read_b128 a[0:3], %[vbr] offset:4096\n"
        "ds_read_b128 a[32:35], v11 offset:4096\n"
        "v_add_u32 v22, s63, v16\n"
        "ds_read_b128 v[60:63], v22 offset:256\n"
        "s_waitcnt lgkmcnt(10)\n"
        "v_mfma_f32_16x16x32_bf16 v[88:91], v[96:99], v[44:47], 0\n"
        "s_bfe_u32 s59, %[sjo], 262144\n"
        "v_lshl_add_u32 v21, s59, 7, %[vcs]\n"
        "ds_read_b64 v[24:25], v21\n"
        "v_mfma_f32_16x16x32_bf16 v[92:95], v[100:103], v[44:47], 0\n"
        "v_add_u32 v23, s63, v17\n"
        "ds_read_b128 v[44:47], v23 offset:256\n"
        "s_waitcnt lgkmcnt(11)\n"
        "v_mfma_f32_16x16x32_bf16 v[88:91], v[104:107], v[48:51], v[88:91]\n"
        "s_waitcnt lgkmcnt(1)\n"
        "v_mfma_f32_16x16x16_bf16 v[64:67], v[36:37], v[24:25], v[64:67]\n"
        "v_mfma_f32_16x16x16_bf16 v[68:71], v[38:39], v[24:25], v[68:71]\n"
        "v_mfma_f32_16x16x32_bf16 v[92:95], v[108:111], v[48:51], v[92:95]\n"
        "v_add_u32 v22, s63, v18\n"
        "ds_read_b128 v[48:51], v22 offset:256\n"
        "v_mfma_f32_16x16x32_bf16 v[88:91], v[112:115], v[52:55], v[88:91]\n"
        "v_mfma_f32_16x16x32_bf16 v[92:95], v[116:119], v[52:55], v[92:95]\n"
        "v_add_u32 v23, s63, v19\n"
        "ds_read_b128 v[52:55], v23 offset:256\n"
        "v_mfma_f32_16x16x32_bf16 v[88:91], v[120:123], v[56:59], v[88:91]\n"
        "v_mfma_f32_16x16x32_bf16 v[92:95], v[124:127], v[56:59], v[92:95]\n"
        "v_mul_f32 v64, v28, v64\n"
        "v_mul_f32 v65, v29, v65\n"
        "v_mul_f32 v66, v30, v66\n"
        "v_mul_f32 v67, v31, v67\n"
        "s_waitcnt vmcnt(8)\n"
        "ds_read_b128 a[4:7], %[vbr] offset:6144\n"
        "ds_read_b128 a[36:39], v11 offset:6144\n"
        "s_add_u32 s58, %[scw], 393216\n"
        "s_add_u32 m0, %[sring], 4096\n"
        "s_and_b32 s58, s58, 0x7ffff\n"
        "buffer_load_dwordx4 %[vce], %[srdc], s58 offen offset:0 lds\n"
        "s_add_u32 s58, %[scw], 425984\n"
        "s_add_u32 m0, %[sring], 5120\n"
        "s_and_b32 s58, s58, 0x7ffff\n"
        "buffer_load_dwordx4 v13, %[srdc], s58 offen offset:0 lds\n"
        "v_add_u32 v22, s63, v16\n"
        "ds_read_b128 v[56:59], v22 offset:512\n"
        "v_mfma_f32_16x16x32_bf16 v[88:91], v[128:131], v[60:63], v[88:91]\n"
        "v_mul_f32 v68, v32, v68\n"
        "v_mul_f32 v69, v33, v69\n"
        "v_mul_f32 v70, v34, v70\n"
        "v_mul_f32 v71, v35, v71\n"
        "v_mfma_f32_16x16x32_bf16 v[92:95], v[132:135], v[60:63], v[92:95]\n"
        "v_cvt_pk_bf16_f32 v64, v64, v65\n"
        "v_cvt_pk_bf16_f32 v65, v66, v67\n"
        "v_cvt_pk_bf16_f32 v66, v68, v69\n"
        "v_cvt_pk_bf16_f32 v67, v70, v71\n"
        "v_add_u32 v23, s63, v17\n"
        "ds_read_b128 v[60:63], v23 offset:512\n"
        "s_waitcnt lgkmcnt(6)\n"
        "v_mfma_f32_16x16x32_bf16 v[88:91], v[136:139], v[44:47], v[88:91]\n"
        "s_nop 1\n"
        "v_permlane32_swap_b32 v64, v66\n"
        "v_permlane32_swap_b32 v65, v67\n"
        "v_mfma_f32_16x16x32_bf16 v[92:95], v[140:143], v[44:47], v[92:95]\n"
        "s_nop 1\n"
        "v_permlane16_swap_b32 v64, v66\n"
        "v_permlane16_swap_b32 v65, v67\n"
        "v_add_u32 v22, s63, v18\n"
        "ds_read_b128 v[44:47], v22 offset:512\n"
        "s_waitcnt lgkmcnt(6)\n"
        "v_mfma_f32_16x16x32_bf16 v[88:91], v[144:147], v[48:51], v[88:91]\n"
        "v_mov_b32 v40, v64\n"
        "v_mov_b32 v41, v65\n"
        "v_mov_b32 v42, v66\n"
        "v_mov_b32 v43, v67\n"
        "s_bfe_u32 s59, %[sjo], 262148\n"
        "v_lshl_add_u32 v21, s59, 7, %[vcs]\n"
        "ds_read_b64 v[26:27], v21\n"
        "v_mfma_f32_16x16x32_bf16 v[92:95], v[148:151], v[48:51], v[92:95]\n"
        "v_add_u32 v23, s63, v19\n"
        "ds_read_b128 v[48:51], v23 offset:512\n"
        "s_waitcnt lgkmcnt(7)\n"
        "v_mfma_f32_16x16x32_bf16 v[88:91], v[152:155], v[52:55], v[88:91]\n"
        "s_waitcnt lgkmcnt(1)\n"
        "v_mfma_f32_16x16x16_bf16 v[72:75], v[36:37], v[26:27], v[72:75]\n"
        "v_mfma_f32_16x16x32_bf16 v[92:95], v[156:159], v[52:55], v[92:95]\n"
        "v_mfma_f32_16x16x16_bf16 v[76:79], v[38:39], v[26:27], v[76:79]\n"
        "s_waitcnt vmcnt(8)\n"
        "ds_read_b128 a[8:11], %[vbr] offset:8192\n"
        "ds_read_b128 a[40:43], v11 offset:8192\n"
        "s_add_u32 s58, %[scw], 458752\n"
        "s_add_u32 m0, %[sring], 6144\n"
        "s_and_b32 s58, s58, 0x7ffff\n"
        "buffer_load_dwordx4 %[vce], %[srdc], s58 offen offset:0 lds\n"
        "s_add_u32 s58, %[scw], 491520\n"
        "s_add_u32 m0, %[sring], 7168\n"
        "s_and_b32 s58, s58, 0x7ffff\n"
        "buffer_load_dwordx4 v13, %[srdc], s58 offen offset:0 lds\n"
        "v_add_u32 v22, s63, v16\n"
        "ds_read_b128 v[52:55], v22 offset:768\n"
        "v_mfma_f32_16x16x32_bf16 v[88:91], v[160:163], v[56:59], v[88:91]\n"
        "v_mul_f32 v72, v28, v72\n"
        "v_mul_f32 v73, v29, v73\n"
        "v_mul_f32 v74, v30, v74\n"
        "v_mul_f32 v75, v31, v75\n"
        "v_mfma_f32_16x16x32_bf16 v[92:95], v[164:167], v[56:59], v[92:95]\n"
        "v_mul_f32 v76, v32, v76\n"
        "v_mul_f32 v77, v33, v77\n"
        "v_mul_f32 v78, v34, v78\n"
        "v_mul_f32 v79, v35, v79\n"
        "v_add_u32 v23, s63, v17\n"
        "ds_read_b128 v[56:59], v23 offset:768\n"
        "v_mfma_f32_16x16x32_bf16 v[88:91], v[168:171], v[60:63], v[88:91]\n"
        "v_cvt_pk_bf16_f32 v72, v72, v73\n"
        "v_cvt_pk_bf16_f32 v73, v74, v75\n"
        "v_cvt_pk_bf16_f32 v74, v76, v77\n"
        "v_cvt_pk_bf16_f32 v75, v78, v79\n"
        "v_mfma_f32_16x16x32_bf16 v[92:95], v[172:175], v[60:63], v[92:95]\n"
        "s_nop 1\n"
        "v_permlane32_swap_b32 v72, v74\n"
        "v_permlane32_swap_b32 v73, v75\n"
        "v_add_u32 v22, s63, v18\n"
        "ds_read_b128 v[60:63], v22 offset:768\n"
        "v_mfma_f32_16x16x32_bf16 v[88:91], v[176:179], v[44:47], v[88:91]\n"
        "s_nop 1\n"
        "v_permlane16_swap_b32 v72, v74\n"
        "v_permlane16_swap_b32 v73, v75\n"
        "v_mfma_f32_16x16x32_bf16 v[92:95], v[180:183], v[44:47], v[92:95]\n"
        "s_bfe_u32 s59, %[sjo], 262152\n"
        "v_lshl_add_u32 v21, s59, 7, %[vcs]\n"
        "ds_read_b64 v[24:25], v21\n"
        "v_add_u32 v23, s63, v19\n"
        "ds_read_b128 v[44:47], v23 offset:768\n"
        "s_waitcnt lgkmcnt(7)\n"
        "v_mfma_f32_16x16x32_bf16 v[88:91], v[184:187], v[48:51], v[88:91]\n"
        "v_mfma_f32_16x16x32_bf16 v[92:95], v[188:191], v[48:51], v[92:95]\n"
        "s_waitcnt lgkmcnt(1)\n"
        "v_mfma_f32_16x16x16_bf16 v[80:83], v[36:37], v[24:25], v[80:83]\n"
        "v_mfma_f32_16x16x16_bf16 v[84:87], v[38:39], v[24:25], v[84:87]\n"
        "s_waitcnt vmcnt(8)\n"
        "ds_read_b128 a[12:15], %[vbr] offset:10240\n"
        "ds_read_b128 a[44:47], v11 offset:10240\n"
        "s_add_u32 s58, %[scw], 0\n"
        "s_add_u32 m0, %[sring], 8064\n"
        "s_and_b32 s58, s58, 0x7ffff\n"
        "buffer_load_dwordx4 %[vce], %[srdc], s58 offen offset:128 lds\n"
        "s_add_u32 s58, %[scw], 32768\n"
        "s_add_u32 m0, %[sring], 9088\n"
        "s_and_b32 s58, s58, 0x7ffff\n"
        "buffer_load_dwordx4 v13, %[srdc], s58 offen offset:128 lds\n"
        "v_add_u32 v22, s63, v16\n"
        "ds_read_b128 v[48:51], v22 offset:1024\n"
        "v_mfma_f32_16x16x32_bf16 v[88:91], v[192:195], v[52:55], v[88:91]\n"
        "v_mfma_f32_16x16x32_bf16 v[92:95], v[196:199], v[52:55], v[92:95]\n"
        "v_add_u32 v23, s63, v17\n"
        "ds_read_b128 v[52:55], v23 offset:1024\n"
        "v_mfma_f32_16x16x32_bf16 v[88:91], v[200:203], v[56:59], v[88:91]\n"
        "v_mfma_f32_16x16x32_bf16 v[92:95], v[204:207], v[56:59], v[92:95]\n"
        "v_add_u32 v22, s63, v18\n"
        "ds_read_b128 v[56:59], v22 offset:1024\n"
        "v_mfma_f32_16x16x32_bf16 v[88:91], v[208:211], v[60:63], v[88:91]\n"
        "v_mul_f32 v80, v28, v80\n"
        "v_mul_f32 v81, v29, v81\n"
        "v_mul_f32 v82, v30, v82\n"
        "v_mul_f32 v83, v31, v83\n"
        "v_mfma_f32_16x16x32_bf16 v[92:95], v[212:215], v[60:63], v[92:95]\n"
        "v_mul_f32 v84, v32, v84\n"
        "v_mul_f32 v85, v33, v85\n"
        "v_mul_f32 v86, v34, v86\n"
        "v_mul_f32 v87, v35, v87\n"
        "v_add_u32 v23, s63, v19\n"
        "ds_read_b128 v[60:63], v23 offset:1024\n"
        "s_waitcnt lgkmcnt(6)\n"
        "v_mfma_f32_16x16x32_bf16 v[88:91], v[216:219], v[44:47], v[88:91]\n"
        "v_cvt_pk_bf16_f32 v80, v80, v81\n"
        "v_cvt_pk_bf16_f32 v81, v82, v83\n"
        "v_cvt_pk_bf16_f32 v82, v84, v85\n"
        "v_cvt_pk_bf16_f32 v83, v86, v87\n"
        "v_mfma_f32_16x16x32_bf16 v[92:95], v[220:223], v[44:47], v[92:95]\n"
        "s_nop 1\n"
        "v_permlane32_swap_b32 v80, v82\n"
        "v_permlane32_swap_b32 v81, v83\n"
        "s_waitcnt vmcnt(8)\n"
        "ds_read_b128 a[16:19], %[vbr] offset:0\n"
        "ds_read_b128 a[48:51], v11 offset:0\n"
        "s_waitcnt lgkmcnt(6)\n"
        "s_add_u32 s58, %[scw], 65536\n"
        "s_add_u32 m0, %[sring], 10112\n"
        "s_and_b32 s58, s58, 0x7ffff\n"
        "buffer_load_dwordx4 %[vce], %[srdc], s58 offen offset:128 lds\n"
        "s_add_u32 s58, %[scw], 98304\n"
        "s_add_u32 m0, %[sring], 11136\n"
        "s_and_b32 s58, s58, 0x7ffff\n"
        "buffer_load_dwordx4 v13, %[srdc], s58 offen offset:128 lds\n"
        "v_add_u32 v22, s64, v16\n"
        "ds_read_b128 v[44:47], v22 offset:0\n"
        "s_waitcnt lgkmcnt(6)\n"
        "v_mfma_f32_16x16x32_bf16 v[88:91], v[224:227], v[48:51], v[88:91]\n"
        "s_nop 1\n"
        "v_permlane16_swap_b32 v80, v82\n"
        "v_permlane16_swap_b32 v81, v83\n"
        "v_mfma_f32_16x16x32_bf16 v[92:95], v[228:231], v[48:51], v[92:95]\n"
        "v_add_u32 v23, s64, v17\n"
        "ds_read_b128 v[48:51], v23 offset:0\n"
        "s_waitcnt lgkmcnt(6)\n"
        "v_mfma_f32_16x16x32_bf16 v[88:91], v[232:235], v[52:55], v[88:91]\n"
        "v_mfma_f32_16x16x32_bf16 v[92:95], v[236:239], v[52:55], v[92:95]\n"
        "v_add_u32 v22, s64, v18\n"
        "ds_read_b128 v[52:55], v22 offset:0\n"
        "s_waitcnt lgkmcnt(6)\n"
        "v_mfma_f32_16x16x32_bf16 v[88:91], v[240:243], v[56:59], v[88:91]\n"
        "v_mfma_f32_16x16x32_bf16 v[92:95], v[244:247], v[56:59], v[92:95]\n"
        "v_add_u32 v23, s64, v19\n"
        "ds_read_b128 v[56:59], v23 offset:0\n"
        "s_waitcnt lgkmcnt(6)\n"
        "v_mfma_f32_16x16x32_bf16 v[88:91], v[248:251], v[60:63], v[88:91]\n"
        "v_mfma_f32_16x16x32_bf16 v[92:95], v[252:255], v[60:63], v[92:95]\n"
        "s_waitcnt vmcnt(8)\n"
        "ds_read_b128 a[20:23], %[vbr] offset:2048\n"
        "ds_read_b128 a[52:55], v11 offset:2048\n"
        "s_waitcnt lgkmcnt(6)\n"
        "s_add_u32 s58, %[scw], 131072\n"
        "s_sub_u32 m0, %[sring], 128\n"
        "s_and_b32 s58, s58, 0x7ffff\n"
        "buffer_load_dwordx4 %[vce], %[srdc], s58 offen offset:128 lds\n"
        "s_add_u32 s58, %[scw], 163840\n"
        "s_add_u32 m0, %[sring], 896\n"
        "s_and_b32 s58, s58, 0x7ffff\n"
        "buffer_load_dwordx4 v13, %[srdc], s58 offen offset:128 lds\n"
        "v_add_u32 v22, s64, v16\n"
        "ds_read_b128 v[60:63], v22 offset:256\n"
        "s_waitcnt lgkmcnt(6)\n"
        "v_mfma_f32_16x16x32_bf16 v[64:67], v[96:99], v[44:47], 0\n"
        "v_mfma_f32_16x16x32_bf16 v[68:71], v[100:103], v[44:47], 0\n"
        "v_add_u32 v23, s64, v17\n"
        "ds_read_b128 v[44:47], v23 offset:256\n"
        "s_waitcnt lgkmcnt(6)\n"
        "v_mfma_f32_16x16x32_bf16 v[64:67], v[104:107], v[48:51], v[64:67]\n"
        "s_bfe_u32 s59, %[sjo], 262156\n"
        "v_lshl_add_u32 v21, s59, 7, %[vcs]\n"
        "ds_read_b64 v[26:27], v21\n"
        "v_mfma_f32_16x16x32_bf16 v[68:71], v[108:111], v[48:51], v[68:71]\n"
        "v_add_u32 v22, s64, v18\n"
        "ds_read_b128 v[48:51], v22 offset:256\n"
        "s_waitcnt lgkmcnt(7)\n"
        "v_mfma_f32_16x16x32_bf16 v[64:67], v[112:115], v[52:55], v[64:67]\n"
        "v_mfma_f32_16x16x32_bf16 v[68:71], v[116:119], v[52:55], v[68:71]\n"
        "v_add_u32 v23, s64, v19\n"
        "ds_read_b128 v[52:55], v23 offset:256\n"
        "s_waitcnt lgkmcnt(7)\n"
        "v_mfma_f32_16x16x32_bf16 v[64:67], v[120:123], v[56:59], v[64:67]\n"
        "s_waitcnt lgkmcnt(2)\n"
        "v_mfma_f32_16x16x16_bf16 v[88:91], v[36:37], v[26:27], v[88:91]\n"
        "v_mfma_f32_16x16x32_bf16 v[68:71], v[124:127], v[56:59], v[68:71]\n"
        "v_mfma_f32_16x16x16_bf16 v[92:95], v[38:39], v[26:27], v[92:95]\n"
        "s_waitcnt vmcnt(8)\n"
        "ds_read_b128 a[24:27], %[vbr] offset:4096\n"
        "ds_read_b128 a[56:59], v11 offset:4096\n"
        "s_add_u32 s58, %[scw], 196608\n"
        "s_add_u32 m0, %[sring], 1920\n"
        "s_and_b32 s58, s58, 0x7ffff\n"
        "buffer_load_dwordx4 %[vce], %[srdc], s58 offen offset:128 lds\n"
        "s_add_u32 s58, %[scw], 229376\n"
        "s_add_u32 m0, %[sring], 2944\n"
        "s_and_b32 s58, s58, 0x7ffff\n"
        "buffer_load_dwordx4 v13, %[srdc], s58 offen offset:128 lds\n"
        "v_add_u32 v22, s64, v16\n"
        "ds_read_b128 v[56:59], v22 offset:512\n"
        "v_mfma_f32_16x16x32_bf16 v[64:67], v[128:131], v[60:63], v[64:67]\n"
        "v_mfma_f32_16x16x32_bf16 v[68:71], v[132:135], v[60:63], v[68:71]\n"
        "v_add_u32 v23, s64, v17\n"
        "ds_read_b128 v[60:63], v23 offset:512\n"
        "v_mfma_f32_16x16x32_bf16 v[64:67], v[136:139], v[44:47], v[64:67]\n"
        "v_mfma_f32_16x16x32_bf16 v[68:71], v[140:143], v[44:47], v[68:71]\n"
        "v_add_u32 v22, s64, v18\n"
        "ds_read_b128 v[44:47], v22 offset:512\n"
        "s_waitcnt lgkmcnt(6)\n"
        "v_mfma_f32_16x16x32_bf16 v[64:67], v[144:147], v[48:51], v[64:67]\n"
        "v_mul_f32 v88, v28, v88\n"
        "v_mul_f32 v89, v29, v89\n"
        "v_mfma_f32_16x16x32_bf16 v[68:71], v[148:151], v[48:51], v[68:71]\n"
        "v_mul_f32 v90, v30, v90\n"
        "v_mul_f32 v91, v31, v91\n"
        "v_add_u32 v23, s64, v19\n"
        "ds_read_b128 v[48:51], v23 offset:512\n"
        "s_waitcnt lgkmcnt(6)\n"
        "v_mfma_f32_16x16x32_bf16 v[64:67], v[152:155], v[52:55], v[64:67]\n"
        "v_mul_f32 v92, v32, v92\n"
        "v_mul_f32 v93, v33, v93\n"
        "v_mfma_f32_16x16x32_bf16 v[68:71], v[156:159], v[52:55], v[68:71]\n"
        "v_mul_f32 v94, v34, v94\n"
        "v_mul_f32 v95, v35, v95\n"
        "s_waitcnt vmcnt(8)\n"
        "ds_read_b128 a[28:31], %[vbr] offset:6144\n"
        "ds_read_b128 a[60:63], v11 offset:6144\n"
        "s_waitcnt lgkmcnt(6)\n"
        "s_add_u32 s58, %[scw], 262144\n"
        "s_add_u32 m0, %[sring], 3968\n"
        "s_and_b32 s58, s58, 0x7ffff\n"
        "buffer_load_dwordx4 %[vce], %[srdc], s58 offen offset:128 lds\n"
        "s_add_u32 s58, %[scw], 294912\n"
        "s_add_u32 m0, %[sring], 4992\n"
        "s_and_b32 s58, s58, 0x7ffff\n"
        "buffer_load_dwordx4 v13, %[srdc], s58 offen offset:128 lds\n"
        "v_add_u32 v22, s64, v16\n"
        "ds_read_b128 v[52:55], v22 offset:768\n"
        "s_waitcnt lgkmcnt(6)\n"
        "v_mfma_f32_16x16x32_bf16 v[64:67], v[160:163], v[56:59], v[64:67]\n"
        "v_cvt_pk_bf16_f32 v88, v88, v89\n"
        "v_cvt_pk_bf16_f32 v89, v90, v91\n"
        "v_mfma_f32_16x16x32_bf16 v[68:71], v[164:167], v[56:59], v[68:71]\n"
        "v_cvt_pk_bf16_f32 v90, v92, v93\n"
        "v_cvt_pk_bf16_f32 v91, v94, v95\n"
        "v_add_u32 v23, s64, v17\n"
        "ds_read_b128 v[56:59], v23 offset:768\n"
        "s_waitcnt lgkmcnt(6)\n"
        "v_mfma_f32_16x16x32_bf16 v[64:67], v[168:171], v[60:63], v[64:67]\n"
        "s_nop 1\n"
        "v_mfma_f32_16x16x32_bf16 v[68:71], v[172:175], v[60:63], v[68:71]\n"
        "v_permlane32_swap_b32 v88, v90\n"
        "v_permlane32_swap_b32 v89, v91\n"
        "v_add_u32 v22, s64, v18\n"
        "ds_read_b128 v[60:63], v22 offset:768\n"
        "s_waitcnt lgkmcnt(6)\n"
        "v_mfma_f32_16x16x32_bf16 v[64:67], v[176:179], v[44:47], v[64:67]\n"
        "s_nop 1\n"
        "v_mfma_f32_16x16x32_bf16 v[68:71], v[180:183], v[44:47], v[68:71]\n"
        "v_permlane16_swap_b32 v88, v90\n"
        "v_permlane16_swap_b32 v89, v91\n"
        "v_add_u32 v23, s64, v19\n"
        "ds_read_b128 v[44:47], v23 offset:768\n"
        "s_waitcnt lgkmcnt(6)\n"
        "v_mfma_f32_16x16x32_bf16 v[64:67], v[184:187], v[48:51], v[64:67]\n"
        "v_mfma_f32_16x16x32_bf16 v[68:71], v[188:191], v[48:51], v[68:71]\n"
        "s_waitcnt vmcnt(8)\n"
        "ds_read_b128 a[64:67], %[vbr] offset:8192\n"
        "ds_read_b128 a[96:99], v11 offset:8192\n"
        "s_waitcnt lgkmcnt(6)\n"
        "s_add_u32 s58, %[scw], 327680\n"
        "s_add_u32 m0, %[sring], 6016\n"
        "s_and_b32 s58, s58, 0x7ffff\n"
        "buffer_load_dwordx4 %[vce], %[srdc], s58 offen offset:128 lds\n"
        "s_add_u32 s58, %[scw], 360448\n"
        "s_add_u32 m0, %[sring], 7040\n"
        "s_and_b32 s58, s58, 0x7ffff\n"
        "buffer_load_dwordx4 v13, %[srdc], s58 offen offset:128 lds\n"
        "v_add_u32 v22, s64, v16\n"
        "ds_read_b128 v[48:51], v22 offset:1024\n"
        "s_waitcnt lgkmcnt(6)\n"
        "v_mfma_f32_16x16x32_bf16 v[64:67], v[192:195], v[52:55], v[64:67]\n"
        "v_mfma_f32_16x16x32_bf16 v[68:71], v[196:199], v[52:55], v[68:71]\n"
        "v_add_u32 v23, s64, v17\n"
        "ds_read_b128 v[52:55], v23 offset:1024\n"
        "s_waitcnt lgkmcnt(6)\n"
        "v_mfma_f32_16x16x32_bf16 v[64:67], v[200:203], v[56:59], v[64:67]\n"
        "v_mfma_f32_16x16x32_bf16 v[68:71], v[204:207], v[56:59], v[68:71]\n"
        "v_add_u32 v22, s64, v18\n"
        "ds_read_b128 v[56:59], v22 offset:1024\n"
        "s_waitcnt lgkmcnt(6)\n"
        "v_mfma_f32_16x16x32_bf16 v[64:67], v[208:211], v[60:63], v[64:67]\n"
        "v_mfma_f32_16x16x32_bf16 v[68:71], v[212:215], v[60:63], v[68:71]\n"
        "v_add_u32 v23, s64, v19\n"
        "ds_read_b128 v[60:63], v23 offset:1024\n"
        "s_waitcnt lgkmcnt(6)\n"
        "v_mfma_f32_16x16x32_bf16 v[64:67], v[216:219], v[44:47], v[64:67]\n"
        "v_mfma_f32_16x16x32_bf16 v[68:71], v[220:223], v[44:47], v[68:71]\n"
        "s_waitcnt vmcnt(8)\n"
        "ds_read_b128 a[68:71], %[vbr] offset:10240\n"
        "ds_read_b128 a[100:103], v11 offset:10240\n"
        "s_waitcnt lgkmcnt(6)\n"
        "s_add_u32 s58, %[scw], 393216\n"
        "s_add_u32 m0, %[sring], 8064\n"
        "s_and_b32 s58, s58, 0x7ffff\n"
        "buffer_load_dwordx4 %[vce], %[srdc], s58 offen offset:128 lds\n"
        "s_add_u32 s58, %[scw], 425984\n"
        "s_add_u32 m0, %[sring], 9088\n"
        "s_and_b32 s58, s58, 0x7ffff\n"
        "buffer_load_dwordx4 v13, %[srdc], s58 offen offset:128 lds\n"
        "s_waitcnt lgkmcnt(5)\n"
        "v_mfma_f32_16x16x32_bf16 v[64:67], v[224:227], v[48:51], v[64:67]\n"
        "v_mfma_f32_16x16x32_bf16 v[68:71], v[228:231], v[48:51], v[68:71]\n"
        "s_bfe_u32 s59, %[sjo], 262160\n"
        "v_lshl_add_u32 v21, s59, 7, %[vcs]\n"
        "ds_read_b64 v[24:25], v21\n"
        "s_waitcnt lgkmcnt(5)\n"
        "v_mfma_f32_16x16x32_bf16 v[64:67], v[232:235], v[52:55], v[64:67]\n"
        "v_mfma_f32_16x16x32_bf16 v[68:71], v[236:239], v[52:55], v[68:71]\n"
        "s_waitcnt lgkmcnt(4)\n"
        "v_mfma_f32_16x16x32_bf16 v[64:67], v[240:243], v[56:59], v[64:67]\n"
        "v_mfma_f32_16x16x32_bf16 v[68:71], v[244:247], v[56:59], v[68:71]\n"
        "s_waitcnt lgkmcnt(3)\n"
        "v_mfma_f32_16x16x32_bf16 v[64:67], v[248:251], v[60:63], v[64:67]\n"
        "v_mfma_f32_16x16x32_bf16 v[68:71], v[252:255], v[60:63], v[68:71]\n"
        "s_waitcnt lgkmcnt(0)\n"
        "v_mfma_f32_16x16x16_bf16 v[64:67], v[36:37], v[24:25], v[64:67]\n"
        "v_mfma_f32_16x16x16_bf16 v[68:71], v[38:39], v[24:25], v[68:71]\n"
        "s_nop 7\n"
        "v_mul_f32 v64, v28, v64\n"
        "v_mul_f32 v65, v29, v65\n"
        "v_mul_f32 v66, v30, v66\n"
        "v_mul_f32 v67, v31, v67\n"
        "v_mul_f32 v68, v32, v68\n"
        "v_mul_f32 v69, v33, v69\n"
        "v_mul_f32 v70, v34, v70\n"
        "v_mul_f32 v71, v35, v71\n"
        "v_cvt_pk_bf16_f32 v64, v64, v65\n"
        "v_cvt_pk_bf16_f32 v65, v66, v67\n"
        "v_cvt_pk_bf16_f32 v66, v68, v69\n"
        "v_cvt_pk_bf16_f32 v67, v70, v71\n"
        "s_nop 1\n"
        "v_permlane32_swap_b32 v64, v66\n"
        "v_permlane32_swap_b32 v65, v67\n"
        "s_nop 1\n"
        "v_permlane16_swap_b32 v64, v66\n"
        "v_permlane16_swap_b32 v65, v67\n"
        "s_bfe_u32 s59, %[sjo], 262144\n"
        "v_lshl_add_u32 v24, s59, 13, %[vab]\n"
        "global_store_dwordx4 v24, v[40:43], %[sws] sc1\n"
        "s_bfe_u32 s59, %[sjo], 262148\n"
        "v_lshl_add_u32 v25, s59, 13, %[vab]\n"
        "global_store_dwordx4 v25, v[72:75], %[sws] sc1\n"
        "s_bfe_u32 s59, %[sjo], 262152\n"
        "v_lshl_add_u32 v24, s59, 13, %[vab]\n"
        "global_store_dwordx4 v24, v[80:83], %[sws] sc1\n"
        "s_bfe_u32 s59, %[sjo], 262156\n"
        "v_lshl_add_u32 v25, s59, 13, %[vab]\n"
        "global_store_dwordx4 v25, v[88:91], %[sws] sc1\n"
        "s_bfe_u32 s59, %[sjo], 262160\n"
        "v_lshl_add_u32 v24, s59, 13, %[vab]\n"
        "global_store_dwordx4 v24, v[64:67], %[sws] sc1\n"
        "s_waitcnt vmcnt(13)\n"
        "ds_read_b128 a[72:75], %[vbr] offset:0\n"
        "ds_read_b128 a[104:107], v11 offset:0\n"
        "s_add_u32 s58, %[scw], 458752\n"
        "s_add_u32 m0, %[sring], 10112\n"
        "s_and_b32 s58, s58, 0x7ffff\n"
        "buffer_load_dwordx4 %[vce], %[srdc], s58 offen offset:128 lds\n"
        "s_add_u32 s58, %[scw], 491520\n"
        "s_add_u32 m0, %[sring], 11136\n"
        "s_and_b32 s58, s58, 0x7ffff\n"
        "buffer_load_dwordx4 v13, %[srdc], s58 offen offset:128 lds\n"
        "s_waitcnt vmcnt(13)\n"
        "ds_read_b128 a[76:79], %[vbr] offset:2048\n"
        "ds_read_b128 a[108:111], v11 offset:2048\n"
        "s_waitcnt lgkmcnt(2)\n"
        "s_add_u32 s58, %[scw], 0\n"
        "s_sub_u32 m0, %[sring], 256\n"
        "s_and_b32 s58, s58, 0x7ffff\n"
        "buffer_load_dwordx4 %[vce], %[srdc], s58 offen offset:256 lds\n"
        "s_add_u32 s58, %[scw], 32768\n"
        "s_add_u32 m0, %[sring], 768\n"
        "s_and_b32 s58, s58, 0x7ffff\n"
        "buffer_load_dwordx4 v13, %[srdc], s58 offen offset:256 lds\n"
        "s_waitcnt vmcnt(13)\n"
        "ds_read_b128 a[80:83], %[vbr] offset:4096\n"
        "ds_read_b128 a[112:115], v11 offset:4096\n"
        "s_waitcnt lgkmcnt(2)\n"
        "s_add_u32 s58, %[scw], 65536\n"
        "s_add_u32 m0, %[sring], 1792\n"
        "s_and_b32 s58, s58, 0x7ffff\n"
        "buffer_load_dwordx4 %[vce], %[srdc], s58 offen offset:256 lds\n"
        "s_add_u32 s58, %[scw], 98304\n"
        "s_add_u32 m0, %[sring], 2816\n"
        "s_and_b32 s58, s58, 0x7ffff\n"
        "buffer_load_dwordx4 v13, %[srdc], s58 offen offset:256 lds\n"
        "s_waitcnt vmcnt(13)\n"
        "ds_read_b128 a[84:87], %[vbr] offset:6144\n"
        "ds_read_b128 a[116:119], v11 offset:6144\n"
        "s_waitcnt lgkmcnt(2)\n"
        "s_add_u32 s58, %[scw], 131072\n"
        "s_add_u32 m0, %[sring], 3840\n"
        "s_and_b32 s58, s58, 0x7ffff\n"
        "buffer_load_dwordx4 %[vce], %[srdc], s58 offen offset:256 lds\n"
        "s_add_u32 s58, %[scw], 163840\n"
        "s_add_u32 m0, %[sring], 4864\n"
        "s_and_b32 s58, s58, 0x7ffff\n"
        "buffer_load_dwordx4 v13, %[srdc], s58 offen offset:256 lds\n"
        "s_waitcnt vmcnt(13)\n"
        "ds_read_b128 a[88:91], %[vbr] offset:8192\n"
        "ds_read_b128 a[120:123], v11 offset:8192\n"
        "s_waitcnt lgkmcnt(2)\n"
        "s_add_u32 s58, %[scw], 196608\n"
        "s_add_u32 m0, %[sring], 5888\n"
        "s_and_b32 s58, s58, 0x7ffff\n"
        "buffer_load_dwordx4 %[vce], %[srdc], s58 offen offset:256 lds\n"
        "s_add_u32 s58, %[scw], 229376\n"
        "s_add_u32 m0, %[sring], 6912\n"
        "s_and_b32 s58, s58, 0x7ffff\n"
        "buffer_load_dwordx4 v13, %[srdc], s58 offen offset:256 lds\n"
        "s_waitcnt vmcnt(8)\n"
        "ds_read_b128 a[92:95], %[vbr] offset:10240\n"
        "ds_read_b128 a[124:127], v11 offset:10240\n"
        "s_waitcnt lgkmcnt(2)\n"
        "s_add_u32 s58, %[scw], 262144\n"
        "s_add_u32 m0, %[sring], 7936\n"
        "s_and_b32 s58, s58, 0x7ffff\n"
        "buffer_load_dwordx4 %[vce], %[srdc], s58 offen offset:256 lds\n"
        "s_add_u32 s58, %[scw], 294912\n"
        "s_add_u32 m0, %[sring], 8960\n"
        "s_and_b32 s58, s58, 0x7ffff\n"
        "buffer_load_dwordx4 v13, %[srdc], s58 offen offset:256 lds\n"
        "s_waitcnt vmcnt(8)\n"
        "ds_read_b128 a[128:131], %[vbr] offset:0\n"
        "ds_read_b128 a[160:163], v11 offset:0\n"
        "s_waitcnt lgkmcnt(2)\n"
        "s_add_u32 s58, %[scw], 327680\n"
        "s_add_u32 m0, %[sring], 9984\n"
        "s_and_b32 s58, s58, 0x7ffff\n"
        "buffer_load_dwordx4 %[vce], %[srdc], s58 offen offset:256 lds\n"
        "s_add_u32 s58, %[scw], 360448\n"
        "s_add_u32 m0, %[sring], 11008\n"
        "s_and_b32 s58, s58, 0x7ffff\n"
        "buffer_load_dwordx4 v13, %[srdc], s58 offen offset:256 lds\n"
        "s_waitcnt vmcnt(8)\n"
        "ds_read_b128 a[132:135], %[vbr] offset:2048\n"
        "ds_read_b128 a[164:167], v11 offset:2048\n"
        "s_waitcnt lgkmcnt(2)\n"
        "s_add_u32 s58, %[scw], 393216\n"
        "s_sub_u32 m0, %[sring], 256\n"
        "s_and_b32 s58, s58, 0x7ffff\n"
        "buffer_load_dwordx4 %[vce], %[srdc], s58 offen offset:256 lds\n"
        "s_add_u32 s58, %[scw], 425984\n"
        "s_add_u32 m0, %[sring], 768\n"
        "s_and_b32 s58, s58, 0x7ffff\n"
        "buffer_load_dwordx4 v13, %[srdc], s58 offen offset:256 lds\n"
        "s_waitcnt vmcnt(8)\n"
        "ds_read_b128 a[136:139], %[vbr] offset:4096\n"
        "ds_read_b128 a[168:171], v11 offset:4096\n"
        "s_waitcnt lgkmcnt(2)\n"
        "s_add_u32 s58, %[scw], 458752\n"
        "s_add_u32 m0, %[sring], 1792\n"
        "s_and_b32 s58, s58, 0x7ffff\n"
        "buffer_load_dwordx4 %[vce], %[srdc], s58 offen offset:256 lds\n"
        "s_add_u32 s58, %[scw], 491520\n"
        "s_add_u32 m0, %[sring], 2816\n"
        "s_and_b32 s58, s58, 0x7ffff\n"
        "buffer_load_dwordx4 v13, %[srdc], s58 offen offset:256 lds\n"
        "v_add_u32 v14, 4, v14\n"
        "v_add_u32 v14, 1, v14\n"
        "s_mov_b64 exec, 1\n"
        "v_mov_b32 v20, %[spf]\n"
        "global_store_dwordx2 v20, v[14:15], %[sws] sc1\n"
        "s_mov_b64 exec, -1\n"
        "s_waitcnt vmcnt(9)\n"
        "ds_read_b128 a[140:143], %[vbr] offset:6144\n"
        "ds_read_b128 a[172:175], v11 offset:6144\n"
        "s_waitcnt lgkmcnt(2)\n"
        "s_add_u32 s58, %[scw], 0\n"
        "s_add_u32 m0, %[sring], 3712\n"
        "s_and_b32 s58, s58, 0x7ffff\n"
        "buffer_load_dwordx4 %[vce], %[srdc], s58 offen offset:384 lds\n"
        "s_add_u32 s58, %[scw], 32768\n"
        "s_add_u32 m0, %[sring], 4736\n"
        "s_and_b32 s58, s58, 0x7ffff\n"
        "buffer_load_dwordx4 v13, %[srdc], s58 offen offset:384 lds\n"
        "s_waitcnt vmcnt(9)\n"
        "ds_read_b128 a[144:147], %[vbr] offset:8192\n"
        "ds_read_b128 a[176:179], v11 offset:8192\n"
        "s_waitcnt lgkmcnt(2)\n"
        "s_add_u32 s58, %[scw], 65536\n"
        "s_add_u32 m0, %[sring], 5760\n"
        "s_and_b32 s58, s58, 0x7ffff\n"
        "buffer_load_dwordx4 %[vce], %[srdc], s58 offen offset:384 lds\n"
        "s_add_u32 s58, %[scw], 98304\n"
        "s_add_u32 m0, %[sring], 6784\n"
        "s_and_b32 s58, s58, 0x7ffff\n"
        "buffer_load_dwordx4 v13, %[srdc], s58 offen offset:384 lds\n"
        "s_waitcnt vmcnt(9)\n"
        "ds_read_b128 a[148:151], %[vbr] offset:10240\n"
        "ds_read_b128 a[180:183], v11 offset:10240\n"
        "s_waitcnt lgkmcnt(2)\n"
        "s_add_u32 s58, %[scw], 131072\n"
        "s_add_u32 m0, %[sring], 7808\n"
        "s_and_b32 s58, s58, 0x7ffff\n"
        "buffer_load_dwordx4 %[vce], %[srdc], s58 offen offset:384 lds\n"
        "s_add_u32 s58, %[scw], 163840\n"
        "s_add_u32 m0, %[sring], 8832\n"
        "s_and_b32 s58, s58, 0x7ffff\n"
        "buffer_load_dwordx4 v13, %[srdc], s58 offen offset:384 lds\n"
        "s_waitcnt vmcnt(9)\n"
        "ds_read_b128 a[152:155], %[vbr] offset:0\n"
        "ds_read_b128 a[184:187], v11 offset:0\n"
        "s_waitcnt lgkmcnt(2)\n"
        "s_add_u32 s58, %[scw], 196608\n"
        "s_add_u32 m0, %[sring], 9856\n"
        "s_and_b32 s58, s58, 0x7ffff\n"
        "buffer_load_dwordx4 %[vce], %[srdc], s58 offen offset:384 lds\n"
        "s_add_u32 s58, %[scw], 229376\n"
        "s_add_u32 m0, %[sring], 10880\n"
        "s_and_b32 s58, s58, 0x7ffff\n"
        "buffer_load_dwordx4 v13, %[srdc], s58 offen offset:384 lds\n"
        "s_waitcnt vmcnt(9)\n"
        "ds_read_b128 a[156:159], %[vbr] offset:2048\n"
        "ds_read_b128 a[188:191], v11 offset:2048\n"
        "s_waitcnt lgkmcnt(2)\n"
        "s_add_u32 s58, %[scw], 262144\n"
        "s_sub_u32 m0, %[sring], 384\n"
        "s_and_b32 s58, s58, 0x7ffff\n"
        "buffer_load_dwordx4 %[vce], %[srdc], s58 offen offset:384 lds\n"
        "s_add_u32 s58, %[scw], 294912\n"
        "s_add_u32 m0, %[sring], 640\n"
        "s_and_b32 s58, s58, 0x7ffff\n"
        "buffer_load_dwordx4 v13, %[srdc], s58 offen offset:384 lds\n"
        "s_waitcnt vmcnt(8)\n"
        "ds_read_b128 a[192:195], %[vbr] offset:4096\n"
        "ds_read_b128 a[224:227], v11 offset:4096\n"
        "s_waitcnt lgkmcnt(2)\n"
        "s_add_u32 s58, %[scw], 327680\n"
        "s_add_u32 m0, %[sring], 1664\n"
        "s_and_b32 s58, s58, 0x7ffff\n"
        "buffer_load_dwordx4 %[vce], %[srdc], s58 offen offset:384 lds\n"
        "s_add_u32 s58, %[scw], 360448\n"
        "s_add_u32 m0, %[sring], 2688\n"
        "s_and_b32 s58, s58, 0x7ffff\n"
        "buffer_load_dwordx4 v13, %[srdc], s58 offen offset:384 lds\n"
        "s_waitcnt vmcnt(8)\n"
        "ds_read_b128 a[196:199], %[vbr] offset:6144\n"
        "ds_read_b128 a[228:231], v11 offset:6144\n"
        "s_waitcnt lgkmcnt(2)\n"
        "s_add_u32 s58, %[scw], 393216\n"
        "s_add_u32 m0, %[sring], 3712\n"
        "s_and_b32 s58, s58, 0x7ffff\n"
        "buffer_load_dwordx4 %[vce], %[srdc], s58 offen offset:384 lds\n"
        "s_add_u32 s58, %[scw], 425984\n"
        "s_add_u32 m0, %[sring], 4736\n"
        "s_and_b32 s58, s58, 0x7ffff\n"
        "buffer_load_dwordx4 v13, %[srdc], s58 offen offset:384 lds\n"
        "s_waitcnt vmcnt(8)\n"
        "ds_read_b128 a[200:203], %[vbr] offset:8192\n"
        "ds_read_b128 a[232:235], v11 offset:8192\n"
        "s_waitcnt lgkmcnt(2)\n"
        "s_add_u32 s58, %[scw], 458752\n"
        "s_add_u32 m0, %[sring], 5760\n"
        "s_and_b32 s58, s58, 0x7ffff\n"
        "buffer_load_dwordx4 %[vce], %[srdc], s58 offen offset:384 lds\n"
        "s_add_u32 s58, %[scw], 491520\n"
        "s_add_u32 m0, %[sring], 6784\n"
        "s_and_b32 s58, s58, 0x7ffff\n"
        "buffer_load_dwordx4 v13, %[srdc], s58 offen offset:384 lds\n"
        "s_waitcnt vmcnt(8)\n"
        "ds_read_b128 a[204:207], %[vbr] offset:10240\n"
        "ds_read_b128 a[236:239], v11 offset:10240\n"
        "s_waitcnt lgkmcnt(2)\n"
        "s_waitcnt vmcnt(6)\n"
        "ds_read_b128 a[208:211], %[vbr] offset:0\n"
        "ds_read_b128 a[240:243], v11 offset:0\n"
        "s_waitcnt lgkmcnt(2)\n"
        "s_waitcnt vmcnt(4)\n"
        "ds_read_b128 a[212:215], %[vbr] offset:2048\n"
        "ds_read_b128 a[244:247], v11 offset:2048\n"
        "s_waitcnt lgkmcnt(2)\n"
        "s_waitcnt vmcnt(2)\n"
        "ds_read_b128 a[216:219], %[vbr] offset:4096\n"
        "ds_read_b128 a[248:251], v11 offset:4096\n"
        "s_waitcnt lgkmcnt(2)\n"
        "s_waitcnt vmcnt(0)\n"
        "ds_read_b128 a[220:223], %[vbr] offset:6144\n"
        "ds_read_b128 a[252:255], v11 offset:6144\n"
        "s_waitcnt lgkmcnt(2)\n"
        "s_waitcnt lgkmcnt(0)\n"
        "s_barrier\n"
        : "+{v[64:95]}"(AC), "+{v[14:15]}"(fl), "+{v[96:127]}"(X0), "+{v[128:159]}"(X1), "+{v[160:191]}"(X2), "+{v[192:223]}"(X3), "+{v[224:255]}"(X4)
        : [vdr] "v"(LD + (l & 15) * 1280 + (((l >> 4) ^ (l & 15)) << 4)),
          [vtab] "v"(T_AL + (w * 32 + (l >> 4) * 4) * 4),
          [vcs] "v"(T_CSF + (l & 15) * 8),
          [vmh] "v"(l < 16 ? T_MH + (w * 32 + l) * 8 : T_ZERO),
          [vbr] "v"(LRED + w * 12288 + (l & 15) * 128 + (((l >> 4) ^ ((l & 15) >> 1)) << 4)),
          [vce] "v"((l >> 3) * 4096 + (((l & 7) ^ (l >> 4)) << 4)),
          [sring] "s"(LRED + w * 12288),
          [scw] "s"(w * 131072),
          [srdc] "s"(srdc),
          [sjo] "s"(sjo),
          [sws] "s"(ws),
          [vab] "v"(abo + l * 16),
          [spf] "s"(O_PF + ((0 * 256 + b) * 4 + w) * 128)
        : "scc", "vcc", "memory", "s58", "s59", "s60", "s61", "s62", "s63", "s64", "v11", "v13", "v16", "v17", "v18", "v19", "v20", "v21", "v22", "v23", "v24", "v25", "v26", "v27", "v28", "v29", "v30", "v31", "v32", "v33", "v34", "v35", "v36", "v37", "v38", "v39", "v40", "v41", "v42", "v43", "v44", "v45", "v46", "v47", "v48", "v49", "v50", "v51", "v52", "v53", "v54", "v55", "v56", "v57", "v58", "v59", "v60", "v61", "v62", "v63", "a0", "a1", "a2", "a3", "a4", "a5", "a6", "a7", "a8", "a9", "a10", "a11", "a12", "a13", "a14", "a15", "a16", "a17", "a18", "a19", "a20", "a21", "a22", "a23", "a24", "a25", "a26", "a27", "a28", "a29", "a30", "a31", "a32", "a33", "a34", "a35", "a36", "a37", "a38", "a39", "a40", "a41", "a42", "a43", "a44", "a45", "a46", "a47", "a48", "a49", "a50", "a51", "a52", "a53", "a54", "a55", "a56", "a57", "a58", "a59", "a60", "a61", "a62", "a63", "a64", "a65", "a66", "a67", "a68", "a69", "a70", "a71", "a72", "a73", "a74", "a75", "a76", "a77", "a78", "a79", "a80", "a81", "a82", "a83", "a84", "a85", "a86", "a87", "a88", "a89", "a90", "a91", "a92", "a93", "a94", "a95", "a96", "a97", "a98", "a99", "a100", "a101", "a102", "a103", "a104", "a105", "a106", "a107", "a108", "a109", "a110", "a111", "a112", "a113", "a114", "a115", "a116", "a117", "a118", "a119", "a120", "a121", "a122", "a123", "a124", "a125", "a126", "a127", "a128", "a129", "a130", "a131", "a132", "a133", "a134", "a135", "a136", "a137", "a138", "a139", "a140", "a141", "a142", "a143", "a144", "a145", "a146", "a147", "a148", "a149", "a150", "a151", "a152", "a153", "a154", "a155", "a156", "a157", "a158", "a159", "a160", "a161", "a162", "a163", "a164", "a165", "a166", "a167", "a168", "a169", "a170", "a171", "a172", "a173", "a174", "a175", "a176", "a177", "a178", "a179", "a180", "a181", "a182", "a183", "a184", "a185", "a186", "a187", "a188", "a189", "a190", "a191", "a192", "a193", "a194", "a195", "a196", "a197", "a198", "a199", "a200", "a201", "a202", "a203", "a204", "a205", "a206", "a207", "a208", "a209", "a210", "a211", "a212", "a213", "a214", "a215", "a216", "a217", "a218", "a219", "a220", "a221", "a222", "a223", "a224", "a225", "a226", "a227", "a228", "a229", "a230", "a231", "a232", "a233", "a234", "a235", "a236", "a237", "a238", "a239", "a240", "a241", "a242", "a243", "a244", "a245", "a246", "a247", "a248", "a249", "a250", "a251", "a252", "a253", "a254", "a255");
        ASTAMP_SB;
    }
    LANE();
    STAMP(2);
    u32x16 XO;
    {
        const u16* sxr = ARG(0) + (size_t)(bt * 128 + w * 32) * 640;
        asm volatile(
        "s_nop 4\n"
        "s_mov_b32 s81, 0x40000\n"
        "v_add_u32 v58, s54, %[vl16]\n"
        "v_lshrrev_b32 v59, 1, %[vl16]\n"
        "ds_read_b128 v[32:35], v58\n"
        "v_add_u32 v59, 156224, v59\n"
        "ds_read_b64 v[40:41], v59\n"
        "ds_read_b64 v[44:45], v59\n"
        "ds_read_b64 v[46:47], v59\n"
        "v_add_u32 v58, 161408, %[vl16]\n"
        "ds_read_b128 v[36:39], v58\n"
        "v_mov_b32 v42, 0\n"
        "v_mov_b32 v43, 0\n"
        "v_add_u32 v48, s40, %[vl16]\n"
        "v_add_u32 v49, s41, %[vl16]\n"
        "v_add_u32 v50, s42, %[vl16]\n"
        "v_add_u32 v51, s43, %[vl16]\n"
        "v_mov_b32 v63, s57\n"
        "s_waitcnt lgkmcnt(0)\n"
        "v_add_u32 v31, s27, v37\n"
        "global_load_dwordx2 v[16:17], v31, %[sxr] offset:0\n"
        "global_load_dwordx2 v[18:19], v31, %[sxr] offset:32\n"
        "v_add_u32 v31, 20480, v31\n"
        "global_load_dwordx2 v[20:21], v31, %[sxr] offset:0\n"
        "global_load_dwordx2 v[22:23], v31, %[sxr] offset:32\n"
        "v_add_u32 v31, s28, v37\n"
        "global_load_dwordx2 v[24:25], v31, %[sxr] offset:0\n"
        "v_lshl_add_u32 v31, v38, 1, s29\n"
        "global_load_dwordx4 v[12:15], v31, %[swr] offset:0\n"
        "v_add_u32 v31, s30, v38\n"
        "global_load_dwordx2 v[26:27], v31, %[swr] offset:0\n"
        "global_load_dwordx2 v[60:61], v32, %[sws] sc1\n"
        "s_waitcnt vmcnt(0)\n"
        "v_bfe_u32 v59, v33, 0, 4\n"
        "v_subrev_u32 v60, s56, v60\n"
        "v_sub_u32 v60, v60, v59\n"
        "v_cmp_lt_u32 vcc, 7, v60\n"
        "v_cmp_ne_u32 s[82:83], s57, v61\n"
        "s_or_b64 vcc, vcc, s[82:83]\n"
        "s_cbranch_vccnz Rpf0_%=\n"
        "Cpf0_%=:\n"
        "s_bfe_u32 s59, s48, 262144\n"
        "s_lshl_b32 s59, s59, 14\n"
        "s_add_u32 s59, s59, s24\n"
        "v_add_u32 v56, s59, %[vl16]\n"
        "v_add_u32 v57, 8192, v56\n"
        "global_load_dwordx4 v[96:99], v56, %[sws] offset:-4096 sc1\n"
        "global_load_dwordx4 v[100:103], v57, %[sws] offset:-4096 sc1\n"
        "global_load_dwordx4 v[104:107], v56, %[sws] offset:-3072 sc1\n"
        "global_load_dwordx4 v[108:111], v57, %[sws] offset:-3072 sc1\n"
        "global_load_dwordx4 v[112:115], v56, %[sws] offset:-2048 sc1\n"
        "global_load_dwordx4 v[116:119], v57, %[sws] offset:-2048 sc1\n"
        "global_load_dwordx4 v[120:123], v56, %[sws] offset:-1024 sc1\n"
        "global_load_dwordx4 v[124:127], v57, %[sws] offset:-1024 sc1\n"
        "global_load_dwordx2 v[60:61], v32, %[sws] sc1\n"
        "s_waitcnt vmcnt(8)\n"
        "v_mfma_f32_16x16x32_bf16 v[128:131], v[96:99], a[0:3], 0\n"
        "s_waitcnt vmcnt(7)\n"
        "v_mfma_f32_16x16x32_bf16 v[132:135], v[100:103], a[0:3], 0\n"
        "v_mov_b32 v52, -1\n"
        "v_mov_b32 v53, -1\n"
        "v_mov_b32 v54, -1\n"
        "v_mov_b32 v55, -1\n"
        "v_mfma_f32_16x16x32_bf16 v[136:139], v[96:99], a[4:7], 0\n"
        "v_mfma_f32_16x16x32_bf16 v[140:143], v[100:103], a[4:7], 0\n"
        "v_mfma_f32_16x16x32_bf16 v[144:147], v[96:99], a[8:11], 0\n"
        "v_mfma_f32_16x16x32_bf16 v[148:151], v[100:103], a[8:11], 0\n"
        "v_mfma_f32_16x16x32_bf16 v[152:155], v[96:99], a[12:15], 0\n"
        "v_mfma_f32_16x16x32_bf16 v[156:159], v[100:103], a[12:15], 0\n"
        "v_mfma_f32_16x16x32_bf16 v[160:163], v[96:99], a[16:19], 0\n"
        "v_mfma_f32_16x16x32_bf16 v[164:167], v[100:103], a[16:19], 0\n"
        "v_mfma_f32_16x16x32_bf16 v[168:171], v[96:99], a[20:23], 0\n"
        "v_mfma_f32_16x16x32_bf16 v[172:175], v[100:103], a[20:23], 0\n"
        "v_mfma_f32_16x16x32_bf16 v[176:179], v[96:99], a[24:27], 0\n"
        "v_mfma_f32_16x16x32_bf16 v[180:183], v[100:103], a[24:27], 0\n"
        "v_mfma_f32_16x16x32_bf16 v[184:187], v[96:99], a[28:31], 0\n"
        "global_load_dwordx4 v[64:67], v56, %[sws] offset:0 sc1\n"
        "v_mfma_f32_16x16x32_bf16 v[188:191], v[100:103], a[28:31], 0\n"
        "global_load_dwordx4 v[68:71], v57, %[sws] offset:0 sc1\n"
        "s_waitcnt vmcnt(8)\n"
        "v_mfma_f32_16x16x32_bf16 v[128:131], v[104:107], a[32:35], v[128:131]\n"
        "s_waitcnt vmcnt(7)\n"
        "v_mfma_f32_16x16x32_bf16 v[132:135], v[108:111], a[32:35], v[132:135]\n"
        "v_mfma_f32_16x16x32_bf16 v[136:139], v[104:107], a[36:39], v[136:139]\n"
        "v_mfma_f32_16x16x32_bf16 v[140:143], v[108:111], a[36:39], v[140:143]\n"
        "v_mfma_f32_16x16x32_bf16 v[144:147], v[104:107], a[40:43], v[144:147]\n"
        "v_mfma_f32_16x16x32_bf16 v[148:151], v[108:111], a[40:43], v[148:151]\n"
        "v_mfma_f32_16x16x32_bf16 v[152:155], v[104:107], a[44:47], v[152:155]\n"
        "v_mfma_f32_16x16x32_bf16 v[156:159], v[108:111], a[44:47], v[156:159]\n"
        "v_mfma_f32_16x16x32_bf16 v[160:163], v[104:107], a[48:51], v[160:163]\n"
        "v_mfma_f32_16x16x32_bf16 v[164:167], v[108:111], a[48:51], v[164:167]\n"
        "v_mfma_f32_16x16x32_bf16 v[168:171], v[104:107], a[52:55], v[168:171]\n"
        "v_mfma_f32_16x16x32_bf16 v[172:175], v[108:111], a[52:55], v[172:175]\n"
        "v_mfma_f32_16x16x32_bf16 v[176:179], v[104:107], a[56:59], v[176:179]\n"
        "global_load_dwordx4 v[72:75], v56, %[sws] offset:1024 sc1\n"
        "v_mfma_f32_16x16x32_bf16 v[180:183], v[108:111], a[56:59], v[180:183]\n"
        "global_load_dwordx4 v[76:79], v57, %[sws] offset:1024 sc1\n"
        "v_mfma_f32_16x16x32_bf16 v[184:187], v[104:107], a[60:63], v[184:187]\n"
        "global_load_dwordx4 v[80:83], v56, %[sws] offset:2048 sc1\n"
        "v_mfma_f32_16x16x32_bf16 v[188:191], v[108:111], a[60:63], v[188:191]\n"
        "global_load_dwordx4 v[84:87], v57, %[sws] offset:2048 sc1\n"
        "s_waitcnt vmcnt(10)\n"
        "v_mfma_f32_16x16x32_bf16 v[128:131], v[112:115], a[64:67], v[128:131]\n"
        "s_waitcnt vmcnt(9)\n"
        "v_mfma_f32_16x16x32_bf16 v[132:135], v[116:119], a[64:67], v[132:135]\n"
        "v_mfma_f32_16x16x32_bf16 v[136:139], v[112:115], a[68:71], v[136:139]\n"
        "v_mfma_f32_16x16x32_bf16 v[140:143], v[116:119], a[68:71], v[140:143]\n"
        "v_mfma_f32_16x16x32_bf16 v[144:147], v[112:115], a[72:75], v[144:147]\n"
        "v_mfma_f32_16x16x32_bf16 v[148:151], v[116:119], a[72:75], v[148:151]\n"
        "v_mfma_f32_16x16x32_bf16 v[152:155], v[112:115], a[76:79], v[152:155]\n"
        "v_mfma_f32_16x16x32_bf16 v[156:159], v[116:119], a[76:79], v[156:159]\n"
        "v_mfma_f32_16x16x32_bf16 v[160:163], v[112:115], a[80:83], v[160:163]\n"
        "v_mfma_f32_16x16x32_bf16 v[164:167], v[116:119], a[80:83], v[164:167]\n"
        "v_mfma_f32_16x16x32_bf16 v[168:171], v[112:115], a[84:87], v[168:171]\n"
        "v_mfma_f32_16x16x32_bf16 v[172:175], v[116:119], a[84:87], v[172:175]\n"
        "v_mfma_f32_16x16x32_bf16 v[176:179], v[112:115], a[88:91], v[176:179]\n"
        "v_mfma_f32_16x16x32_bf16 v[180:183], v[116:119], a[88:91], v[180:183]\n"
        "v_mfma_f32_16x16x32_bf16 v[184:187], v[112:115], a[92:95], v[184:187]\n"
        "global_load_dwordx4 v[88:91], v56, %[sws] offset:3072 sc1\n"
        "v_mfma_f32_16x16x32_bf16 v[188:191], v[116:119], a[92:95], v[188:191]\n"
        "global_load_dwordx4 v[92:95], v57, %[sws] offset:3072 sc1\n"
        "s_waitcnt vmcnt(10)\n"
        "v_mfma_f32_16x16x32_bf16 v[128:131], v[120:123], a[96:99], v[128:131]\n"
        "s_waitcnt vmcnt(9)\n"
        "v_mfma_f32_16x16x32_bf16 v[132:135], v[124:127], a[96:99], v[132:135]\n"
        "v_mfma_f32_16x16x32_bf16 v[136:139], v[120:123], a[100:103], v[136:139]\n"
        "s_waitcnt vmcnt(8)\n"
        "v_bfe_u32 v59, v33, 4, 4\n"
        "v_subrev_u32 v60, s56, v60\n"
        "v_sub_u32 v60, v60, v59\n"
        "v_cmp_lt_u32 vcc, 7, v60\n"
        "v_cmp_ne_u32 s[82:83], s57, v61\n"
        "s_or_b64 vcc, vcc, s[82:83]\n"
        "s_cbranch_vccnz Rpf1_%=\n"
        "Cpf1_%=:\n"
        "v_mfma_f32_16x16x32_bf16 v[140:143], v[124:127], a[100:103], v[140:143]\n"
        "v_mfma_f32_16x16x32_bf16 v[144:147], v[120:123], a[104:107], v[144:147]\n"
        "v_add_u32 v62, s47, %[vl16]\n"
        "global_store_dwordx4 v62, v[52:55], %[sws] offset:0 sc1\n"
        "v_mfma_f32_16x16x32_bf16 v[148:151], v[124:127], a[104:107], v[148:151]\n"
        "v_mfma_f32_16x16x32_bf16 v[152:155], v[120:123], a[108:111], v[152:155]\n"
        "v_mfma_f32_16x16x32_bf16 v[156:159], v[124:127], a[108:111], v[156:159]\n"
        "v_mfma_f32_16x16x32_bf16 v[160:163], v[120:123], a[112:115], v[160:163]\n"
        "v_mfma_f32_16x16x32_bf16 v[164:167], v[124:127], a[112:115], v[164:167]\n"
        "v_mfma_f32_16x16x32_bf16 v[168:171], v[120:123], a[116:119], v[168:171]\n"
        "v_mfma_f32_16x16x32_bf16 v[172:175], v[124:127], a[116:119], v[172:175]\n"
        "v_mfma_f32_16x16x32_bf16 v[176:179], v[120:123], a[120:123], v[176:179]\n"
        "s_bfe_u32 s59, s48, 262148\n"
        "s_lshl_b32 s59, s59, 14\n"
        "s_add_u32 s59, s59, s24\n"
        "v_add_u32 v56, s59, %[vl16]\n"
        "v_add_u32 v57, 8192, v56\n"
        "global_load_dwordx4 v[96:99], v56, %[sws] offset:-4096 sc1\n"
        "v_mfma_f32_16x16x32_bf16 v[180:183], v[124:127], a[120:123], v[180:183]\n"
        "global_load_dwordx4 v[100:103], v57, %[sws] offset:-4096 sc1\n"
        "v_mfma_f32_16x16x32_bf16 v[184:187], v[120:123], a[124:127], v[184:187]\n"
        "global_load_dwordx4 v[104:107], v56, %[sws] offset:-3072 sc1\n"
        "v_mfma_f32_16x16x32_bf16 v[188:191], v[124:127], a[124:127], v[188:191]\n"
        "global_load_dwordx4 v[108:111], v57, %[sws] offset:-3072 sc1\n"
        "s_waitcnt vmcnt(12)\n"
        "v_mfma_f32_16x16x32_bf16 v[128:131], v[64:67], a[128:131], v[128:131]\n"
        "s_waitcnt vmcnt(11)\n"
        "v_mfma_f32_16x16x32_bf16 v[132:135], v[68:71], a[128:131], v[132:135]\n"
        "v_mfma_f32_16x16x32_bf16 v[136:139], v[64:67], a[132:135], v[136:139]\n"
        "v_mfma_f32_16x16x32_bf16 v[140:143], v[68:71], a[132:135], v[140:143]\n"
        "v_mfma_f32_16x16x32_bf16 v[144:147], v[64:67], a[136:139], v[144:147]\n"
        "v_mfma_f32_16x16x32_bf16 v[148:151], v[68:71], a[136:139], v[148:151]\n"
        "v_mfma_f32_16x16x32_bf16 v[152:155], v[64:67], a[140:143], v[152:155]\n"
        "v_mfma_f32_16x16x32_bf16 v[156:159], v[68:71], a[140:143], v[156:159]\n"
        "v_mfma_f32_16x16x32_bf16 v[160:163], v[64:67], a[144:147], v[160:163]\n"
        "v_mfma_f32_16x16x32_bf16 v[164:167], v[68:71], a[144:147], v[164:167]\n"
        "v_mfma_f32_16x16x32_bf16 v[168:171], v[64:67], a[148:151], v[168:171]\n"
        "v_mfma_f32_16x16x32_bf16 v[172:175], v[68:71], a[148:151], v[172:175]\n"
        "v_mfma_f32_16x16x32_bf16 v[176:179], v[64:67], a[152:155], v[176:179]\n"
        "v_mfma_f32_16x16x32_bf16 v[180:183], v[68:71], a[152:155], v[180:183]\n"
        "v_mfma_f32_16x16x32_bf16 v[184:187], v[64:67], a[156:159], v[184:187]\n"
        "global_load_dwordx4 v[112:115], v56, %[sws] offset:-2048 sc1\n"
        "v_mfma_f32_16x16x32_bf16 v[188:191], v[68:71], a[156:159], v[188:191]\n"
        "global_load_dwordx4 v[116:119], v57, %[sws] offset:-2048 sc1\n"
        "s_waitcnt vmcnt(12)\n"
        "v_mfma_f32_16x16x32_bf16 v[128:131], v[72:75], a[160:163], v[128:131]\n"
        "s_waitcnt vmcnt(11)\n"
        "v_mfma_f32_16x16x32_bf16 v[132:135], v[76:79], a[160:163], v[132:135]\n"
        "v_mfma_f32_16x16x32_bf16 v[136:139], v[72:75], a[164:167], v[136:139]\n"
        "v_mfma_f32_16x16x32_bf16 v[140:143], v[76:79], a[164:167], v[140:143]\n"
        "global_load_dwordx2 v[60:61], v32, %[sws] sc1\n"
        "v_mfma_f32_16x16x32_bf16 v[144:147], v[72:75], a[168:171], v[144:147]\n"
        "v_add_u32 v62, s47, %[vl16]\n"
        "global_store_dwordx4 v62, v[52:55], %[sws] offset:1024 sc1\n"
        "v_mfma_f32_16x16x32_bf16 v[148:151], v[76:79], a[168:171], v[148:151]\n"
        "v_mfma_f32_16x16x32_bf16 v[152:155], v[72:75], a[172:175], v[152:155]\n"
        "v_mfma_f32_16x16x32_bf16 v[156:159], v[76:79], a[172:175], v[156:159]\n"
        "v_mfma_f32_16x16x32_bf16 v[160:163], v[72:75], a[176:179], v[160:163]\n"
        "v_mfma_f32_16x16x32_bf16 v[164:167], v[76:79], a[176:179], v[164:167]\n"
        "v_mfma_f32_16x16x32_bf16 v[168:171], v[72:75], a[180:183], v[168:171]\n"
        "v_mfma_f32_16x16x32_bf16 v[172:175], v[76:79], a[180:183], v[172:175]\n"
        "v_mfma_f32_16x16x32_bf16 v[176:179], v[72:75], a[184:187], v[176:179]\n"
        "global_load_dwordx4 v[120:123], v56, %[sws] offset:-1024 sc1\n"
        "v_mfma_f32_16x16x32_bf16 v[180:183], v[76:79], a[184:187], v[180:183]\n"
        "global_load_dwordx4 v[124:127], v57, %[sws] offset:-1024 sc1\n"
        "v_mfma_f32_16x16x32_bf16 v[184:187], v[72:75], a[188:191], v[184:187]\n"
        "global_load_dwordx4 v[64:67], v56, %[sws] offset:0 sc1\n"
        "v_mfma_f32_16x16x32_bf16 v[188:191], v[76:79], a[188:191], v[188:191]\n"
        "global_load_dwordx4 v[68:71], v57, %[sws] offset:0 sc1\n"
        "s_waitcnt vmcnt(16)\n"
        "v_mfma_f32_16x16x32_bf16 v[128:131], v[80:83], a[192:195], v[128:131]\n"
        "s_waitcnt vmcnt(15)\n"
        "v_mfma_f32_16x16x32_bf16 v[132:135], v[84:87], a[192:195], v[132:135]\n"
        "v_mfma_f32_16x16x32_bf16 v[136:139], v[80:83], a[196:199], v[136:139]\n"
        "v_mfma_f32_16x16x32_bf16 v[140:143], v[84:87], a[196:199], v[140:143]\n"
        "v_mfma_f32_16x16x32_bf16 v[144:147], v[80:83], a[200:203], v[144:147]\n"
        "v_mfma_f32_16x16x32_bf16 v[148:151], v[84:87], a[200:203], v[148:151]\n"
        "v_mfma_f32_16x16x32_bf16 v[152:155], v[80:83], a[204:207], v[152:155]\n"
        "v_mfma_f32_16x16x32_bf16 v[156:159], v[84:87], a[204:207], v[156:159]\n"
        "v_mfma_f32_16x16x32_bf16 v[160:163], v[80:83], a[208:211], v[160:163]\n"
        "v_mfma_f32_16x16x32_bf16 v[164:167], v[84:87], a[208:211], v[164:167]\n"
        "v_mfma_f32_16x16x32_bf16 v[168:171], v[80:83], a[212:215], v[168:171]\n"
        "v_mfma_f32_16x16x32_bf16 v[172:175], v[84:87], a[212:215], v[172:175]\n"
        "v_mfma_f32_16x16x32_bf16 v[176:179], v[80:83], a[216:219], v[176:179]\n"
        "v_mfma_f32_16x16x32_bf16 v[180:183], v[84:87], a[216:219], v[180:183]\n"
        "v_mfma_f32_16x16x32_bf16 v[184:187], v[80:83], a[220:223], v[184:187]\n"
        "global_load_dwordx4 v[72:75], v56, %[sws] offset:1024 sc1\n"
        "v_mfma_f32_16x16x32_bf16 v[188:191], v[84:87], a[220:223], v[188:191]\n"
        "global_load_dwordx4 v[76:79], v57, %[sws] offset:1024 sc1\n"
        "s_waitcnt vmcnt(16)\n"
        "v_mfma_f32_16x16x32_bf16 v[128:131], v[88:91], a[224:227], v[128:131]\n"
        "s_waitcnt vmcnt(15)\n"
        "v_mfma_f32_16x16x32_bf16 v[132:135], v[92:95], a[224:227], v[132:135]\n"
        "v_mfma_f32_16x16x32_bf16 v[136:139], v[88:91], a[228:231], v[136:139]\n"
        "v_mfma_f32_16x16x32_bf16 v[140:143], v[92:95], a[228:231], v[140:143]\n"
        "v_mfma_f32_16x16x32_bf16 v[144:147], v[88:91], a[232:235], v[144:147]\n"
        "v_add_u32 v62, s31, %[vl16]\n"
        "global_store_dwordx4 v62, v[52:55], %[sws] offset:0 sc1\n"
        "v_mfma_f32_16x16x32_bf16 v[148:151], v[92:95], a[232:235], v[148:151]\n"
        "v_mfma_f32_16x16x32_bf16 v[152:155], v[88:91], a[236:239], v[152:155]\n"
        "v_mfma_f32_16x16x32_bf16 v[156:159], v[92:95], a[236:239], v[156:159]\n"
        "v_mfma_f32_16x16x32_bf16 v[160:163], v[88:91], a[240:243], v[160:163]\n"
        "v_mfma_f32_16x16x32_bf16 v[164:167], v[92:95], a[240:243], v[164:167]\n"
        "v_mfma_f32_16x16x32_bf16 v[168:171], v[88:91], a[244:247], v[168:171]\n"
        "v_mfma_f32_16x16x32_bf16 v[172:175], v[92:95], a[244:247], v[172:175]\n"
        "v_mfma_f32_16x16x32_bf16 v[176:179], v[88:91], a[248:251], v[176:179]\n"
        "v_mfma_f32_16x16x32_bf16 v[180:183], v[92:95], a[248:251], v[180:183]\n"
        "v_mfma_f32_16x16x32_bf16 v[184:187], v[88:91], a[252:255], v[184:187]\n"
        "global_load_dwordx4 v[80:83], v56, %[sws] offset:2048 sc1\n"
        "v_mfma_f32_16x16x32_bf16 v[188:191], v[92:95], a[252:255], v[188:191]\n"
        "global_load_dwordx4 v[84:87], v57, %[sws] offset:2048 sc1\n"
        "s_waitcnt vmcnt(16)\n"
        "v_mfma_f32_16x16x32_bf16 v[192:195], v[96:99], a[0:3], 0\n"
        "s_waitcnt vmcnt(15)\n"
        "v_mfma_f32_16x16x32_bf16 v[196:199], v[100:103], a[0:3], 0\n"
        "v_mfma_f32_16x16x32_bf16 v[200:203], v[96:99], a[4:7], 0\n"
        "v_cvt_pk_bf16_f32 v144, v144, v145\n"
        "v_cvt_pk_bf16_f32 v145, v146, v147\n"
        "v_mfma_f32_16x16x32_bf16 v[204:207], v[100:103], a[4:7], 0\n"
        "v_cvt_pk_bf16_f32 v146, v148, v149\n"
        "v_cvt_pk_bf16_f32 v147, v150, v151\n"
        "v_mfma_f32_16x16x32_bf16 v[208:211], v[96:99], a[8:11], 0\n"
        "ds_write_b128 v48, v[144:147] offset:0\n"
        "v_mfma_f32_16x16x32_bf16 v[212:215], v[100:103], a[8:11], 0\n"
        "v_cvt_pk_bf16_f32 v152, v152, v153\n"
        "v_cvt_pk_bf16_f32 v153, v154, v155\n"
        "v_mfma_f32_16x16x32_bf16 v[216:219], v[96:99], a[12:15], 0\n"
        "v_cvt_pk_bf16_f32 v154, v156, v157\n"
        "v_cvt_pk_bf16_f32 v155, v158, v159\n"
        "v_mfma_f32_16x16x32_bf16 v[220:223], v[100:103], a[12:15], 0\n"
        "ds_write_b128 v48, v[152:155] offset:1024\n"
        "v_mfma_f32_16x16x32_bf16 v[224:227], v[96:99], a[16:19], 0\n"
        "v_cvt_pk_bf16_f32 v160, v160, v161\n"
        "v_cvt_pk_bf16_f32 v161, v162, v163\n"
        "v_mfma_f32_16x16x32_bf16 v[228:231], v[100:103], a[16:19], 0\n"
        "v_cvt_pk_bf16_f32 v162, v164, v165\n"
        "v_cvt_pk_bf16_f32 v163, v166, v167\n"
        "v_mfma_f32_16x16x32_bf16 v[232:235], v[96:99], a[20:23], 0\n"
        "ds_write_b128 v49, v[160:163] offset:0\n"
        "v_mfma_f32_16x16x32_bf16 v[236:239], v[100:103], a[20:23], 0\n"
        "v_cvt_pk_bf16_f32 v168, v168, v169\n"
        "v_cvt_pk_bf16_f32 v169, v170, v171\n"
        "v_mfma_f32_16x16x32_bf16 v[240:243], v[96:99], a[24:27], 0\n"
        "v_cvt_pk_bf16_f32 v170, v172, v173\n"
        "v_cvt_pk_bf16_f32 v171, v174, v175\n"
        "v_mfma_f32_16x16x32_bf16 v[244:247], v[100:103], a[24:27], 0\n"
        "ds_write_b128 v49, v[168:171] offset:1024\n"
        "v_mfma_f32_16x16x32_bf16 v[248:251], v[96:99], a[28:31], 0\n"
        "v_cvt_pk_bf16_f32 v176, v176, v177\n"
        "v_cvt_pk_bf16_f32 v177, v178, v179\n"
        "global_load_dwordx4 v[88:91], v56, %[sws] offset:3072 sc1\n"
        "v_mfma_f32_16x16x32_bf16 v[252:255], v[100:103], a[28:31], 0\n"
        "v_cvt_pk_bf16_f32 v178, v180, v181\n"
        "v_cvt_pk_bf16_f32 v179, v182, v183\n"
        "global_load_dwordx4 v[92:95], v57, %[sws] offset:3072 sc1\n"
        "s_waitcnt vmcnt(16)\n"
        "v_mfma_f32_16x16x32_bf16 v[192:195], v[104:107], a[32:35], v[192:195]\n"
        "ds_write_b128 v50, v[176:179] offset:0\n"
        "s_waitcnt vmcnt(15)\n"
        "v_mfma_f32_16x16x32_bf16 v[196:199], v[108:111], a[32:35], v[196:199]\n"
        "v_cvt_pk_bf16_f32 v184, v184, v185\n"
        "v_cvt_pk_bf16_f32 v185, v186, v187\n"
        "v_mfma_f32_16x16x32_bf16 v[200:203], v[104:107], a[36:39], v[200:203]\n"
        "s_waitcnt vmcnt(12)\n"
        "v_bfe_u32 v59, v33, 16, 4\n"
        "v_subrev_u32 v60, s56, v60\n"
        "v_sub_u32 v60, v60, v59\n"
        "v_cmp_lt_u32 vcc, 7, v60\n"
        "v_cmp_ne_u32 s[82:83], s57, v61\n"
        "s_or_b64 vcc, vcc, s[82:83]\n"
        "s_cbranch_vccnz Rpf4_%=\n"
        "Cpf4_%=:\n"
        "v_cvt_pk_bf16_f32 v186, v188, v189\n"
        "v_cvt_pk_bf16_f32 v187, v190, v191\n"
        "v_mfma_f32_16x16x32_bf16 v[204:207], v[108:111], a[36:39], v[204:207]\n"
        "ds_write_b128 v50, v[184:187] offset:1024\n"
        "v_mfma_f32_16x16x32_bf16 v[208:211], v[104:107], a[40:43], v[208:211]\n"
        "v_mfma_f32_16x16x32_bf16 v[212:215], v[108:111], a[40:43], v[212:215]\n"
        "v_mfma_f32_16x16x32_bf16 v[216:219], v[104:107], a[44:47], v[216:219]\n"
        "s_waitcnt lgkmcnt(0)\n"
        "s_barrier\n"
        "v_mfma_f32_16x16x32_bf16 v[220:223], v[108:111], a[44:47], v[220:223]\n"
        "ds_read_b128 v[144:147], v51 offset:0\n"
        "v_mfma_f32_16x16x32_bf16 v[224:227], v[104:107], a[48:51], v[224:227]\n"
        "ds_read_b128 v[148:151], v51 offset:1024\n"
        "v_mfma_f32_16x16x32_bf16 v[228:231], v[108:111], a[48:51], v[228:231]\n"
        "ds_read_b128 v[152:155], v51 offset:2048\n"
        "v_mfma_f32_16x16x32_bf16 v[232:235], v[104:107], a[52:55], v[232:235]\n"
        "ds_read_b128 v[156:159], v51 offset:3072\n"
        "v_mfma_f32_16x16x32_bf16 v[236:239], v[108:111], a[52:55], v[236:239]\n"
        "ds_read_b128 v[160:163], v51 offset:4096\n"
        "v_mfma_f32_16x16x32_bf16 v[240:243], v[104:107], a[56:59], v[240:243]\n"
        "ds_read_b128 v[164:167], v51 offset:5120\n"
        "v_mfma_f32_16x16x32_bf16 v[244:247], v[108:111], a[56:59], v[244:247]\n"
        "v_mfma_f32_16x16x32_bf16 v[248:251], v[104:107], a[60:63], v[248:251]\n"
        "s_bfe_u32 s59, s48, 262152\n"
        "s_lshl_b32 s59, s59, 14\n"
        "s_add_u32 s59, s59, s24\n"
        "v_add_u32 v56, s59, %[vl16]\n"
        "v_add_u32 v57, 8192, v56\n"
        "global_load_dwordx4 v[96:99], v56, %[sws] offset:-4096 sc1\n"
        "v_mfma_f32_16x16x32_bf16 v[252:255], v[108:111], a[60:63], v[252:255]\n"
        "global_load_dwordx4 v[100:103], v57, %[sws] offset:-4096 sc1\n"
        "v_mfma_f32_16x16x32_bf16 v[192:195], v[112:115], a[64:67], v[192:195]\n"
        "s_waitcnt lgkmcnt(5)\n"
        "v_mfma_f32_16x16x32_bf16 v[128:131], v[40:43], v[144:147], v[128:131]\n"
        "v_mfma_f32_16x16x32_bf16 v[196:199], v[116:119], a[64:67], v[196:199]\n"
        "v_mfma_f32_16x16x32_bf16 v[132:135], v[42:45], v[144:147], v[132:135]\n"
        "v_mfma_f32_16x16x32_bf16 v[200:203], v[112:115], a[68:71], v[200:203]\n"
        "s_waitcnt lgkmcnt(4)\n"
        "v_mfma_f32_16x16x32_bf16 v[136:139], v[40:43], v[148:151], v[136:139]\n"
        "v_mfma_f32_16x16x32_bf16 v[204:207], v[116:119], a[68:71], v[204:207]\n"
        "v_mfma_f32_16x16x32_bf16 v[140:143], v[42:45], v[148:151], v[140:143]\n"
        "v_mfma_f32_16x16x32_bf16 v[208:211], v[112:115], a[72:75], v[208:211]\n"
        "s_waitcnt lgkmcnt(3)\n"
        "v_mfma_f32_16x16x32_bf16 v[128:131], v[40:43], v[152:155], v[128:131]\n"
        "v_mfma_f32_16x16x32_bf16 v[212:215], v[116:119], a[72:75], v[212:215]\n"
        "v_mfma_f32_16x16x32_bf16 v[132:135], v[42:45], v[152:155], v[132:135]\n"
        "v_mfma_f32_16x16x32_bf16 v[216:219], v[112:115], a[76:79], v[216:219]\n"
        "s_waitcnt lgkmcnt(2)\n"
        "v_mfma_f32_16x16x32_bf16 v[136:139], v[40:43], v[156:159], v[136:139]\n"
        "v_mfma_f32_16x16x32_bf16 v[220:223], v[116:119], a[76:79], v[220:223]\n"
        "v_mfma_f32_16x16x32_bf16 v[140:143], v[42:45], v[156:159], v[140:143]\n"
        "v_mfma_f32_16x16x32_bf16 v[224:227], v[112:115], a[80:83], v[224:227]\n"
        "s_waitcnt lgkmcnt(1)\n"
        "v_mfma_f32_16x16x32_bf16 v[128:131], v[40:43], v[160:163], v[128:131]\n"
        "v_mfma_f32_16x16x32_bf16 v[228:231], v[116:119], a[80:83], v[228:231]\n"
        "v_mfma_f32_16x16x32_bf16 v[132:135], v[42:45], v[160:163], v[132:135]\n"
        "v_mfma_f32_16x16x32_bf16 v[232:235], v[112:115], a[84:87], v[232:235]\n"
        "s_waitcnt lgkmcnt(0)\n"
        "v_mfma_f32_16x16x32_bf16 v[136:139], v[40:43], v[164:167], v[136:139]\n"
        "v_mfma_f32_16x16x32_bf16 v[236:239], v[116:119], a[84:87], v[236:239]\n"
        "v_mfma_f32_16x16x32_bf16 v[140:143], v[42:45], v[164:167], v[140:143]\n"
        "v_mfma_f32_16x16x32_bf16 v[240:243], v[112:115], a[88:91], v[240:243]\n"
        "s_barrier\n"
        "v_mfma_f32_16x16x32_bf16 v[244:247], v[116:119], a[88:91], v[244:247]\n"
        "v_mfma_f32_16x16x32_bf16 v[248:251], v[112:115], a[92:95], v[248:251]\n"
        "global_load_dwordx4 v[104:107], v56, %[sws] offset:-3072 sc1\n"
        "v_mfma_f32_16x16x32_bf16 v[252:255], v[116:119], a[92:95], v[252:255]\n"
        "global_load_dwordx4 v[108:111], v57, %[sws] offset:-3072 sc1\n"
        "s_waitcnt vmcnt(14)\n"
        "v_mfma_f32_16x16x32_bf16 v[192:195], v[120:123], a[96:99], v[192:195]\n"
        "s_waitcnt vmcnt(13)\n"
        "v_mfma_f32_16x16x32_bf16 v[196:199], v[124:127], a[96:99], v[196:199]\n"
        "v_mfma_f32_16x16x32_bf16 v[200:203], v[120:123], a[100:103], v[200:203]\n"
        "v_cvt_pk_bf16_f32 v144, v128, v129\n"
        "v_cvt_pk_bf16_f32 v145, v130, v131\n"
        "v_mfma_f32_16x16x32_bf16 v[204:207], v[124:127], a[100:103], v[204:207]\n"
        "v_cvt_pk_bf16_f32 v146, v132, v133\n"
        "v_cvt_pk_bf16_f32 v147, v134, v135\n"
        "v_mfma_f32_16x16x32_bf16 v[208:211], v[120:123], a[104:107], v[208:211]\n"
        "v_cvt_pk_bf16_f32 v148, v136, v137\n"
        "v_cvt_pk_bf16_f32 v149, v138, v139\n"
        "v_mfma_f32_16x16x32_bf16 v[212:215], v[124:127], a[104:107], v[212:215]\n"
        "v_cvt_pk_bf16_f32 v150, v140, v141\n"
        "v_cvt_pk_bf16_f32 v151, v142, v143\n"
        "v_lshrrev_b32 v58, 1, %[vl16]\n"
        "v_mfma_f32_16x16x32_bf16 v[216:219], v[120:123], a[108:111], v[216:219]\n"
        "v_add_u32 v58, s44, v58\n"
        "v_mfma_f32_16x16x32_bf16 v[220:223], v[124:127], a[108:111], v[220:223]\n"
        "global_store_dwordx2 v58, v[144:145], %[sws] sc1\n"
        "v_mfma_f32_16x16x32_bf16 v[224:227], v[120:123], a[112:115], v[224:227]\n"
        "v_add_u32 v58, 32768, v58\n"
        "v_mfma_f32_16x16x32_bf16 v[228:231], v[124:127], a[112:115], v[228:231]\n"
        "global_store_dwordx2 v58, v[146:147], %[sws] sc1\n"
        "v_mfma_f32_16x16x32_bf16 v[232:235], v[120:123], a[116:119], v[232:235]\n"
        "v_add_u32 v58, 32768, v58\n"
        "v_mfma_f32_16x16x32_bf16 v[236:239], v[124:127], a[116:119], v[236:239]\n"
        "global_store_dwordx2 v58, v[148:149], %[sws] sc1\n"
        "v_mfma_f32_16x16x32_bf16 v[240:243], v[120:123], a[120:123], v[240:243]\n"
        "v_add_u32 v58, 32768, v58\n"
        "v_mfma_f32_16x16x32_bf16 v[244:247], v[124:127], a[120:123], v[244:247]\n"
        "global_store_dwordx2 v58, v[150:151], %[sws] sc1\n"
        "v_mfma_f32_16x16x32_bf16 v[248:251], v[120:123], a[124:127], v[248:251]\n"
        "global_load_dwordx4 v[112:115], v56, %[sws] offset:-2048 sc1\n"
        "v_mfma_f32_16x16x32_bf16 v[252:255], v[124:127], a[124:127], v[252:255]\n"
        "global_load_dwordx4 v[116:119], v57, %[sws] offset:-2048 sc1\n"
        "s_waitcnt vmcnt(18)\n"
        "v_mfma_f32_16x16x32_bf16 v[192:195], v[64:67], a[128:131], v[192:195]\n"
        "s_waitcnt vmcnt(17)\n"
        "v_mfma_f32_16x16x32_bf16 v[196:199], v[68:71], a[128:131], v[196:199]\n"
        "v_mfma_f32_16x16x32_bf16 v[200:203], v[64:67], a[132:135], v[200:203]\n"
        "v_mfma_f32_16x16x32_bf16 v[204:207], v[68:71], a[132:135], v[204:207]\n"
        "v_mfma_f32_16x16x32_bf16 v[208:211], v[64:67], a[136:139], v[208:211]\n"
        "v_add_u32 v62, s31, %[vl16]\n"
        "v_add_u32 v62, 20480, v62\n"
        "global_store_dwordx4 v62, v[52:55], %[sws] offset:0 sc1\n"
        "v_mfma_f32_16x16x32_bf16 v[212:215], v[68:71], a[136:139], v[212:215]\n"
        "v_mfma_f32_16x16x32_bf16 v[216:219], v[64:67], a[140:143], v[216:219]\n"
        "v_mfma_f32_16x16x32_bf16 v[220:223], v[68:71], a[140:143], v[220:223]\n"
        "v_mfma_f32_16x16x32_bf16 v[224:227], v[64:67], a[144:147], v[224:227]\n"
        "v_mfma_f32_16x16x32_bf16 v[228:231], v[68:71], a[144:147], v[228:231]\n"
        "v_mfma_f32_16x16x32_bf16 v[232:235], v[64:67], a[148:151], v[232:235]\n"
        "v_mfma_f32_16x16x32_bf16 v[236:239], v[68:71], a[148:151], v[236:239]\n"
        "v_mfma_f32_16x16x32_bf16 v[240:243], v[64:67], a[152:155], v[240:243]\n"
        "v_mfma_f32_16x16x32_bf16 v[244:247], v[68:71], a[152:155], v[244:247]\n"
        "v_mfma_f32_16x16x32_bf16 v[248:251], v[64:67], a[156:159], v[248:251]\n"
        "global_load_dwordx4 v[120:123], v56, %[sws] offset:-1024 sc1\n"
        "v_mfma_f32_16x16x32_bf16 v[252:255], v[68:71], a[156:159], v[252:255]\n"
        "global_load_dwordx4 v[124:127], v57, %[sws] offset:-1024 sc1\n"
        "s_waitcnt vmcnt(19)\n"
        "v_mfma_f32_16x16x32_bf16 v[192:195], v[72:75], a[160:163], v[192:195]\n"
        "s_waitcnt vmcnt(18)\n"
        "v_mfma_f32_16x16x32_bf16 v[196:199], v[76:79], a[160:163], v[196:199]\n"
        "v_mfma_f32_16x16x32_bf16 v[200:203], v[72:75], a[164:167], v[200:203]\n"
        "v_mfma_f32_16x16x32_bf16 v[204:207], v[76:79], a[164:167], v[204:207]\n"
        "v_mfma_f32_16x16x32_bf16 v[208:211], v[72:75], a[168:171], v[208:211]\n"
        "v_mfma_f32_16x16x32_bf16 v[212:215], v[76:79], a[168:171], v[212:215]\n"
        "v_mfma_f32_16x16x32_bf16 v[216:219], v[72:75], a[172:175], v[216:219]\n"
        "v_mfma_f32_16x16x32_bf16 v[220:223], v[76:79], a[172:175], v[220:223]\n"
        "v_mfma_f32_16x16x32_bf16 v[224:227], v[72:75], a[176:179], v[224:227]\n"
        "v_add_u32 v62, s32, v36\n"
        "global_store_dwordx2 v62, v[52:53], %[sws] offset:0 sc1\n"
        "v_mfma_f32_16x16x32_bf16 v[228:231], v[76:79], a[176:179], v[228:231]\n"
        "v_mfma_f32_16x16x32_bf16 v[232:235], v[72:75], a[180:183], v[232:235]\n"
        "v_mfma_f32_16x16x32_bf16 v[236:239], v[76:79], a[180:183], v[236:239]\n"
        "v_mfma_f32_16x16x32_bf16 v[240:243], v[72:75], a[184:187], v[240:243]\n"
        "v_mfma_f32_16x16x32_bf16 v[244:247], v[76:79], a[184:187], v[244:247]\n"
        "v_mfma_f32_16x16x32_bf16 v[248:251], v[72:75], a[188:191], v[248:251]\n"
        "global_load_dwordx4 v[64:67], v56, %[sws] offset:0 sc1\n"
        "v_mfma_f32_16x16x32_bf16 v[252:255], v[76:79], a[188:191], v[252:255]\n"
        "global_load_dwordx4 v[68:71], v57, %[sws] offset:0 sc1\n"
        "s_waitcnt vmcnt(19)\n"
        "v_mfma_f32_16x16x32_bf16 v[192:195], v[80:83], a[192:195], v[192:195]\n"
        "s_waitcnt vmcnt(18)\n"
        "v_mfma_f32_16x16x32_bf16 v[196:199], v[84:87], a[192:195], v[196:199]\n"
        "v_mfma_f32_16x16x32_bf16 v[200:203], v[80:83], a[196:199], v[200:203]\n"
        "v_mfma_f32_16x16x32_bf16 v[204:207], v[84:87], a[196:199], v[204:207]\n"
        "v_mfma_f32_16x16x32_bf16 v[208:211], v[80:83], a[200:203], v[208:211]\n"
        "v_mfma_f32_16x16x32_bf16 v[212:215], v[84:87], a[200:203], v[212:215]\n"
        "v_mfma_f32_16x16x32_bf16 v[216:219], v[80:83], a[204:207], v[216:219]\n"
        "v_mfma_f32_16x16x32_bf16 v[220:223], v[84:87], a[204:207], v[220:223]\n"
        "v_mfma_f32_16x16x32_bf16 v[224:227], v[80:83], a[208:211], v[224:227]\n"
        "v_mfma_f32_16x16x32_bf16 v[228:231], v[84:87], a[208:211], v[228:231]\n"
        "v_mfma_f32_16x16x32_bf16 v[232:235], v[80:83], a[212:215], v[232:235]\n"
        "v_mfma_f32_16x16x32_bf16 v[236:239], v[84:87], a[212:215], v[236:239]\n"
        "v_mfma_f32_16x16x32_bf16 v[240:243], v[80:83], a[216:219], v[240:243]\n"
        "v_lshrrev_b32 v62, 1, %[vl16]\n"
        "v_add_u32 v62, s33, v62\n"
        "global_store_dwordx2 v62, v[52:53], %[sws] offset:0 sc1\n"
        "v_mfma_f32_16x16x32_bf16 v[244:247], v[84:87], a[216:219], v[244:247]\n"
        "v_mfma_f32_16x16x32_bf16 v[248:251], v[80:83], a[220:223], v[248:251]\n"
        "global_load_dwordx4 v[72:75], v56, %[sws] offset:1024 sc1\n"
        "v_mfma_f32_16x16x32_bf16 v[252:255], v[84:87], a[220:223], v[252:255]\n"
        "global_load_dwordx4 v[76:79], v57, %[sws] offset:1024 sc1\n"
        "s_waitcnt vmcnt(20)\n"
        "v_mfma_f32_16x16x32_bf16 v[192:195], v[88:91], a[224:227], v[192:195]\n"
        "s_waitcnt vmcnt(19)\n"
        "v_mfma_f32_16x16x32_bf16 v[196:199], v[92:95], a[224:227], v[196:199]\n"
        "v_mfma_f32_16x16x32_bf16 v[200:203], v[88:91], a[228:231], v[200:203]\n"
        "v_mfma_f32_16x16x32_bf16 v[204:207], v[92:95], a[228:231], v[204:207]\n"
        "v_mfma_f32_16x16x32_bf16 v[208:211], v[88:91], a[232:235], v[208:211]\n"
        "v_mfma_f32_16x16x32_bf16 v[212:215], v[92:95], a[232:235], v[212:215]\n"
        "v_mfma_f32_16x16x32_bf16 v[216:219], v[88:91], a[236:239], v[216:219]\n"
        "v_mfma_f32_16x16x32_bf16 v[220:223], v[92:95], a[236:239], v[220:223]\n"
        "v_mfma_f32_16x16x32_bf16 v[224:227], v[88:91], a[240:243], v[224:227]\n"
        "v_mfma_f32_16x16x32_bf16 v[228:231], v[92:95], a[240:243], v[228:231]\n"
        "v_mfma_f32_16x16x32_bf16 v[232:235], v[88:91], a[244:247], v[232:235]\n"
        "v_mfma_f32_16x16x32_bf16 v[236:239], v[92:95], a[244:247], v[236:239]\n"
        "v_mfma_f32_16x16x32_bf16 v[240:243], v[88:91], a[248:251], v[240:243]\n"
        "v_mfma_f32_16x16x32_bf16 v[244:247], v[92:95], a[248:251], v[244:247]\n"
        "v_mfma_f32_16x16x32_bf16 v[248:251], v[88:91], a[252:255], v[248:251]\n"
        "global_load_dwordx4 v[80:83], v56, %[sws] offset:2048 sc1\n"
        "v_mfma_f32_16x16x32_bf16 v[252:255], v[92:95], a[252:255], v[252:255]\n"
        "global_load_dwordx4 v[84:87], v57, %[sws] offset:2048 sc1\n"
        "s_waitcnt vmcnt(20)\n"
        "v_mfma_f32_16x16x32_bf16 v[128:131], v[96:99], a[0:3], 0\n"
        "s_waitcnt vmcnt(19)\n"
        "v_mfma_f32_16x16x32_bf16 v[132:135], v[100:103], a[0:3], 0\n"
        "v_mfma_f32_16x16x32_bf16 v[136:139], v[96:99], a[4:7], 0\n"
        "v_cvt_pk_bf16_f32 v208, v208, v209\n"
        "v_cvt_pk_bf16_f32 v209, v210, v211\n"
        "v_mfma_f32_16x16x32_bf16 v[140:143], v[100:103], a[4:7], 0\n"
        "v_cvt_pk_bf16_f32 v210, v212, v213\n"
        "v_cvt_pk_bf16_f32 v211, v214, v215\n"
        "v_mfma_f32_16x16x32_bf16 v[144:147], v[96:99], a[8:11], 0\n"
        "ds_write_b128 v48, v[208:211] offset:0\n"
        "v_mfma_f32_16x16x32_bf16 v[148:151], v[100:103], a[8:11], 0\n"
        "v_cvt_pk_bf16_f32 v216, v216, v217\n"
        "v_cvt_pk_bf16_f32 v217, v218, v219\n"
        "v_mfma_f32_16x16x32_bf16 v[152:155], v[96:99], a[12:15], 0\n"
        "v_cvt_pk_bf16_f32 v218, v220, v221\n"
        "v_cvt_pk_bf16_f32 v219, v222, v223\n"
        "v_mfma_f32_16x16x32_bf16 v[156:159], v[100:103], a[12:15], 0\n"
        "ds_write_b128 v48, v[216:219] offset:1024\n"
        "v_mfma_f32_16x16x32_bf16 v[160:163], v[96:99], a[16:19], 0\n"
        "v_cvt_pk_bf16_f32 v224, v224, v225\n"
        "v_cvt_pk_bf16_f32 v225, v226, v227\n"
        "v_mfma_f32_16x16x32_bf16 v[164:167], v[100:103], a[16:19], 0\n"
        "v_cvt_pk_bf16_f32 v226, v228, v229\n"
        "v_cvt_pk_bf16_f32 v227, v230, v231\n"
        "v_mfma_f32_16x16x32_bf16 v[168:171], v[96:99], a[20:23], 0\n"
        "ds_write_b128 v49, v[224:227] offset:0\n"
        "v_mfma_f32_16x16x32_bf16 v[172:175], v[100:103], a[20:23], 0\n"
        "v_cvt_pk_bf16_f32 v232, v232, v233\n"
        "v_cvt_pk_bf16_f32 v233, v234, v235\n"
        "v_mfma_f32_16x16x32_bf16 v[176:179], v[96:99], a[24:27], 0\n"
        "v_cvt_pk_bf16_f32 v234, v236, v237\n"
        "v_cvt_pk_bf16_f32 v235, v238, v239\n"
        "v_mfma_f32_16x16x32_bf16 v[180:183], v[100:103], a[24:27], 0\n"
        "ds_write_b128 v49, v[232:235] offset:1024\n"
        "v_mfma_f32_16x16x32_bf16 v[184:187], v[96:99], a[28:31], 0\n"
        "v_cvt_pk_bf16_f32 v240, v240, v241\n"
        "v_cvt_pk_bf16_f32 v241, v242, v243\n"
        "global_load_dwordx4 v[88:91], v56, %[sws] offset:3072 sc1\n"
        "v_mfma_f32_16x16x32_bf16 v[188:191], v[100:103], a[28:31], 0\n"
        "v_cvt_pk_bf16_f32 v242, v244, v245\n"
        "v_cvt_pk_bf16_f32 v243, v246, v247\n"
        "global_load_dwordx4 v[92:95], v57, %[sws] offset:3072 sc1\n"
        "s_waitcnt vmcnt(20)\n"
        "v_mfma_f32_16x16x32_bf16 v[128:131], v[104:107], a[32:35], v[128:131]\n"
        "ds_write_b128 v50, v[240:243] offset:0\n"
        "s_waitcnt vmcnt(19)\n"
        "v_mfma_f32_16x16x32_bf16 v[132:135], v[108:111], a[32:35], v[132:135]\n"
        "v_cvt_pk_bf16_f32 v248, v248, v249\n"
        "v_cvt_pk_bf16_f32 v249, v250, v251\n"
        "v_mfma_f32_16x16x32_bf16 v[136:139], v[104:107], a[36:39], v[136:139]\n"
        "v_cvt_pk_bf16_f32 v250, v252, v253\n"
        "v_cvt_pk_bf16_f32 v251, v254, v255\n"
        "v_mfma_f32_16x16x32_bf16 v[140:143], v[108:111], a[36:39], v[140:143]\n"
        "ds_write_b128 v50, v[248:251] offset:1024\n"
        "v_mfma_f32_16x16x32_bf16 v[144:147], v[104:107], a[40:43], v[144:147]\n"
        "v_mfma_f32_16x16x32_bf16 v[148:151], v[108:111], a[40:43], v[148:151]\n"
        "v_mfma_f32_16x16x32_bf16 v[152:155], v[104:107], a[44:47], v[152:155]\n"
        "s_waitcnt lgkmcnt(0)\n"
        "s_barrier\n"
        "v_mfma_f32_16x16x32_bf16 v[156:159], v[108:111], a[44:47], v[156:159]\n"
        "ds_read_b128 v[208:211], v51 offset:0\n"
        "v_mfma_f32_16x16x32_bf16 v[160:163], v[104:107], a[48:51], v[160:163]\n"
        "ds_read_b128 v[212:215], v51 offset:1024\n"
        "v_mfma_f32_16x16x32_bf16 v[164:167], v[108:111], a[48:51], v[164:167]\n"
        "ds_read_b128 v[216:219], v51 offset:2048\n"
        "v_mfma_f32_16x16x32_bf16 v[168:171], v[104:107], a[52:55], v[168:171]\n"
        "ds_read_b128 v[220:223], v51 offset:3072\n"
        "v_mfma_f32_16x16x32_bf16 v[172:175], v[108:111], a[52:55], v[172:175]\n"
        "ds_read_b128 v[224:227], v51 offset:4096\n"
        "v_mfma_f32_16x16x32_bf16 v[176:179], v[104:107], a[56:59], v[176:179]\n"
        "ds_read_b128 v[228:231], v51 offset:5120\n"
        "v_mfma_f32_16x16x32_bf16 v[180:183], v[108:111], a[56:59], v[180:183]\n"
        "v_mfma_f32_16x16x32_bf16 v[184:187], v[104:107], a[60:63], v[184:187]\n"
        "s_waitcnt vmcnt(15)\n"
        "v_mov_b32 v62, s56\n"
        "v_add_u32 v62, 1, v62\n"
        "s_mov_b64 exec, 1\n"
        "v_mov_b32 v31, s34\n"
        "global_store_dwordx2 v31, v[62:63], %[sws] sc1\n"
        "s_mov_b64 exec, -1\n"
        "s_bfe_u32 s59, s48, 262156\n"
        "s_lshl_b32 s59, s59, 14\n"
        "s_add_u32 s59, s59, s24\n"
        "v_add_u32 v56, s59, %[vl16]\n"
        "v_add_u32 v57, 8192, v56\n"
        "global_load_dwordx4 v[96:99], v56, %[sws] offset:-4096 sc1\n"
        "v_mfma_f32_16x16x32_bf16 v[188:191], v[108:111], a[60:63], v[188:191]\n"
        "global_load_dwordx4 v[100:103], v57, %[sws] offset:-4096 sc1\n"
        "s_waitcnt vmcnt(17)\n"
        "v_mfma_f32_16x16x32_bf16 v[128:131], v[112:115], a[64:67], v[128:131]\n"
        "s_waitcnt lgkmcnt(5)\n"
        "v_mfma_f32_16x16x32_bf16 v[192:195], v[40:43], v[208:211], v[192:195]\n"
        "s_waitcnt vmcnt(16)\n"
        "v_mfma_f32_16x16x32_bf16 v[132:135], v[116:119], a[64:67], v[132:135]\n"
        "v_mfma_f32_16x16x32_bf16 v[196:199], v[42:45], v[208:211], v[196:199]\n"
        "v_mfma_f32_16x16x32_bf16 v[136:139], v[112:115], a[68:71], v[136:139]\n"
        "s_waitcnt lgkmcnt(4)\n"
        "v_mfma_f32_16x16x32_bf16 v[200:203], v[40:43], v[212:215], v[200:203]\n"
        "v_mfma_f32_16x16x32_bf16 v[140:143], v[116:119], a[68:71], v[140:143]\n"
        "v_mfma_f32_16x16x32_bf16 v[204:207], v[42:45], v[212:215], v[204:207]\n"
        "v_mfma_f32_16x16x32_bf16 v[144:147], v[112:115], a[72:75], v[144:147]\n"
        "s_waitcnt lgkmcnt(3)\n"
        "v_mfma_f32_16x16x32_bf16 v[192:195], v[40:43], v[216:219], v[192:195]\n"
        "v_mfma_f32_16x16x32_bf16 v[148:151], v[116:119], a[72:75], v[148:151]\n"
        "v_mfma_f32_16x16x32_bf16 v[196:199], v[42:45], v[216:219], v[196:199]\n"
        "v_mfma_f32_16x16x32_bf16 v[152:155], v[112:115], a[76:79], v[152:155]\n"
        "s_waitcnt lgkmcnt(2)\n"
        "v_mfma_f32_16x16x32_bf16 v[200:203], v[40:43], v[220:223], v[200:203]\n"
        "v_mfma_f32_16x16x32_bf16 v[156:159], v[116:119], a[76:79], v[156:159]\n"
        "v_mfma_f32_16x16x32_bf16 v[204:207], v[42:45], v[220:223], v[204:207]\n"
        "v_mfma_f32_16x16x32_bf16 v[160:163], v[112:115], a[80:83], v[160:163]\n"
        "s_waitcnt lgkmcnt(1)\n"
        "v_mfma_f32_16x16x32_bf16 v[192:195], v[40:43], v[224:227], v[192:195]\n"
        "v_mfma_f32_16x16x32_bf16 v[164:167], v[116:119], a[80:83], v[164:167]\n"
        "v_mfma_f32_16x16x32_bf16 v[196:199], v[42:45], v[224:227], v[196:199]\n"
        "v_mfma_f32_16x16x32_bf16 v[168:171], v[112:115], a[84:87], v[168:171]\n"
        "s_waitcnt lgkmcnt(0)\n"
        "v_mfma_f32_16x16x32_bf16 v[200:203], v[40:43], v[228:231], v[200:203]\n"
        "v_mfma_f32_16x16x32_bf16 v[172:175], v[116:119], a[84:87], v[172:175]\n"
        "v_mfma_f32_16x16x32_bf16 v[204:207], v[42:45], v[228:231], v[204:207]\n"
        "v_mfma_f32_16x16x32_bf16 v[176:179], v[112:115], a[88:91], v[176:179]\n"
        "s_barrier\n"
        "v_mfma_f32_16x16x32_bf16 v[180:183], v[116:119], a[88:91], v[180:183]\n"
        "v_mfma_f32_16x16x32_bf16 v[184:187], v[112:115], a[92:95], v[184:187]\n"
        "global_load_dwordx4 v[104:107], v56, %[sws] offset:-3072 sc1\n"
        "v_mfma_f32_16x16x32_bf16 v[188:191], v[116:119], a[92:95], v[188:191]\n"
        "global_load_dwordx4 v[108:111], v57, %[sws] offset:-3072 sc1\n"
        "s_waitcnt vmcnt(16)\n"
        "v_mfma_f32_16x16x32_bf16 v[128:131], v[120:123], a[96:99], v[128:131]\n"
        "s_waitcnt vmcnt(15)\n"
        "v_mfma_f32_16x16x32_bf16 v[132:135], v[124:127], a[96:99], v[132:135]\n"
        "v_mfma_f32_16x16x32_bf16 v[136:139], v[120:123], a[100:103], v[136:139]\n"
        "v_cvt_pk_bf16_f32 v208, v192, v193\n"
        "v_cvt_pk_bf16_f32 v209, v194, v195\n"
        "v_mfma_f32_16x16x32_bf16 v[140:143], v[124:127], a[100:103], v[140:143]\n"
        "v_cvt_pk_bf16_f32 v210, v196, v197\n"
        "v_cvt_pk_bf16_f32 v211, v198, v199\n"
        "v_mfma_f32_16x16x32_bf16 v[144:147], v[120:123], a[104:107], v[144:147]\n"
        "v_cvt_pk_bf16_f32 v212, v200, v201\n"
        "v_cvt_pk_bf16_f32 v213, v202, v203\n"
        "v_mfma_f32_16x16x32_bf16 v[148:151], v[124:127], a[104:107], v[148:151]\n"
        "v_cvt_pk_bf16_f32 v214, v204, v205\n"
        "v_cvt_pk_bf16_f32 v215, v206, v207\n"
        "v_add_u32 v58, s45, %[vl16]\n"
        "v_mfma_f32_16x16x32_bf16 v[152:155], v[120:123], a[108:111], v[152:155]\n"
        "v_mfma_f32_16x16x32_bf16 v[156:159], v[124:127], a[108:111], v[156:159]\n"
        "global_store_dwordx4 v58, v[208:211], %[sws] sc1\n"
        "v_mfma_f32_16x16x32_bf16 v[160:163], v[120:123], a[112:115], v[160:163]\n"
        "v_mfma_f32_16x16x32_bf16 v[164:167], v[124:127], a[112:115], v[164:167]\n"
        "global_store_dwordx4 v58, v[212:215], %[sws] offset:1024 sc1\n"
        "v_mfma_f32_16x16x32_bf16 v[168:171], v[120:123], a[116:119], v[168:171]\n"
        "v_mfma_f32_16x16x32_bf16 v[172:175], v[124:127], a[116:119], v[172:175]\n"
        "v_mfma_f32_16x16x32_bf16 v[176:179], v[120:123], a[120:123], v[176:179]\n"
        "v_mfma_f32_16x16x32_bf16 v[180:183], v[124:127], a[120:123], v[180:183]\n"
        "v_mfma_f32_16x16x32_bf16 v[184:187], v[120:123], a[124:127], v[184:187]\n"
        "global_load_dwordx4 v[112:115], v56, %[sws] offset:-2048 sc1\n"
        "v_mfma_f32_16x16x32_bf16 v[188:191], v[124:127], a[124:127], v[188:191]\n"
        "global_load_dwordx4 v[116:119], v57, %[sws] offset:-2048 sc1\n"
        "s_waitcnt vmcnt(17)\n"
        "v_mfma_f32_16x16x32_bf16 v[128:131], v[64:67], a[128:131], v[128:131]\n"
        "s_waitcnt vmcnt(16)\n"
        "v_mfma_f32_16x16x32_bf16 v[132:135], v[68:71], a[128:131], v[132:135]\n"
        "v_mfma_f32_16x16x32_bf16 v[136:139], v[64:67], a[132:135], v[136:139]\n"
        "v_mfma_f32_16x16x32_bf16 v[140:143], v[68:71], a[132:135], v[140:143]\n"
        "v_mfma_f32_16x16x32_bf16 v[144:147], v[64:67], a[136:139], v[144:147]\n"
        "v_add_u32 v62, s53, %[vl16]\n"
        "v_add_u32 v62, 0, v62\n"
        "global_store_dwordx4 v62, v[52:55], %[sws] offset:0 sc1\n"
        "v_mfma_f32_16x16x32_bf16 v[148:151], v[68:71], a[136:139], v[148:151]\n"
        "v_mfma_f32_16x16x32_bf16 v[152:155], v[64:67], a[140:143], v[152:155]\n"
        "v_mfma_f32_16x16x32_bf16 v[156:159], v[68:71], a[140:143], v[156:159]\n"
        "v_mfma_f32_16x16x32_bf16 v[160:163], v[64:67], a[144:147], v[160:163]\n"
        "v_mfma_f32_16x16x32_bf16 v[164:167], v[68:71], a[144:147], v[164:167]\n"
        "v_mfma_f32_16x16x32_bf16 v[168:171], v[64:67], a[148:151], v[168:171]\n"
        "v_mfma_f32_16x16x32_bf16 v[172:175], v[68:71], a[148:151], v[172:175]\n"
        "v_mfma_f32_16x16x32_bf16 v[176:179], v[64:67], a[152:155], v[176:179]\n"
        "v_mfma_f32_16x16x32_bf16 v[180:183], v[68:71], a[152:155], v[180:183]\n"
        "v_mfma_f32_16x16x32_bf16 v[184:187], v[64:67], a[156:159], v[184:187]\n"
        "global_load_dwordx4 v[120:123], v56, %[sws] offset:-1024 sc1\n"
        "v_mfma_f32_16x16x32_bf16 v[188:191], v[68:71], a[156:159], v[188:191]\n"
        "global_load_dwordx4 v[124:127], v57, %[sws] offset:-1024 sc1\n"
        "s_waitcnt vmcnt(17)\n"
        "v_mfma_f32_16x16x32_bf16 v[128:131], v[72:75], a[160:163], v[128:131]\n"
        "s_waitcnt vmcnt(16)\n"
        "v_mfma_f32_16x16x32_bf16 v[132:135], v[76:79], a[160:163], v[132:135]\n"
        "v_mfma_f32_16x16x32_bf16 v[136:139], v[72:75], a[164:167], v[136:139]\n"
        "v_mfma_f32_16x16x32_bf16 v[140:143], v[76:79], a[164:167], v[140:143]\n"
        "v_mfma_f32_16x16x32_bf16 v[144:147], v[72:75], a[168:171], v[144:147]\n"
        "v_mfma_f32_16x16x32_bf16 v[148:151], v[76:79], a[168:171], v[148:151]\n"
        "v_mfma_f32_16x16x32_bf16 v[152:155], v[72:75], a[172:175], v[152:155]\n"
        "v_mfma_f32_16x16x32_bf16 v[156:159], v[76:79], a[172:175], v[156:159]\n"
        "v_mfma_f32_16x16x32_bf16 v[160:163], v[72:75], a[176:179], v[160:163]\n"
        "v_add_u32 v62, s53, %[vl16]\n"
        "v_add_u32 v62, 8192, v62\n"
        "global_store_dwordx4 v62, v[52:55], %[sws] offset:0 sc1\n"
        "v_mfma_f32_16x16x32_bf16 v[164:167], v[76:79], a[176:179], v[164:167]\n"
        "v_mfma_f32_16x16x32_bf16 v[168:171], v[72:75], a[180:183], v[168:171]\n"
        "v_mfma_f32_16x16x32_bf16 v[172:175], v[76:79], a[180:183], v[172:175]\n"
        "v_mfma_f32_16x16x32_bf16 v[176:179], v[72:75], a[184:187], v[176:179]\n"
        "v_mfma_f32_16x16x32_bf16 v[180:183], v[76:79], a[184:187], v[180:183]\n"
        "v_mfma_f32_16x16x32_bf16 v[184:187], v[72:75], a[188:191], v[184:187]\n"
        "global_load_dwordx4 v[64:67], v56, %[sws] offset:0 sc1\n"
        "v_mfma_f32_16x16x32_bf16 v[188:191], v[76:79], a[188:191], v[188:191]\n"
        "global_load_dwordx4 v[68:71], v57, %[sws] offset:0 sc1\n"
        "s_waitcnt vmcnt(18)\n"
        "v_mfma_f32_16x16x32_bf16 v[128:131], v[80:83], a[192:195], v[128:131]\n"
        "s_waitcnt vmcnt(17)\n"
        "v_mfma_f32_16x16x32_bf16 v[132:135], v[84:87], a[192:195], v[132:135]\n"
        "v_mfma_f32_16x16x32_bf16 v[136:139], v[80:83], a[196:199], v[136:139]\n"
        "v_mfma_f32_16x16x32_bf16 v[140:143], v[84:87], a[196:199], v[140:143]\n"
        "v_mfma_f32_16x16x32_bf16 v[144:147], v[80:83], a[200:203], v[144:147]\n"
        "v_mfma_f32_16x16x32_bf16 v[148:151], v[84:87], a[200:203], v[148:151]\n"
        "v_mfma_f32_16x16x32_bf16 v[152:155], v[80:83], a[204:207], v[152:155]\n"
        "v_mfma_f32_16x16x32_bf16 v[156:159], v[84:87], a[204:207], v[156:159]\n"
        "v_mfma_f32_16x16x32_bf16 v[160:163], v[80:83], a[208:211], v[160:163]\n"
        "v_mfma_f32_16x16x32_bf16 v[164:167], v[84:87], a[208:211], v[164:167]\n"
        "v_mfma_f32_16x16x32_bf16 v[168:171], v[80:83], a[212:215], v[168:171]\n"
        "v_mfma_f32_16x16x32_bf16 v[172:175], v[84:87], a[212:215], v[172:175]\n"
        "v_mfma_f32_16x16x32_bf16 v[176:179], v[80:83], a[216:219], v[176:179]\n"
        "v_add_u32 v62, s53, %[vl16]\n"
        "v_add_u32 v62, 16384, v62\n"
        "global_store_dwordx4 v62, v[52:55], %[sws] offset:0 sc1\n"
        "v_mfma_f32_16x16x32_bf16 v[180:183], v[84:87], a[216:219], v[180:183]\n"
        "v_mfma_f32_16x16x32_bf16 v[184:187], v[80:83], a[220:223], v[184:187]\n"
        "global_load_dwordx4 v[72:75], v56, %[sws] offset:1024 sc1\n"
        "v_mfma_f32_16x16x32_bf16 v[188:191], v[84:87], a[220:223], v[188:191]\n"
        "global_load_dwordx4 v[76:79], v57, %[sws] offset:1024 sc1\n"
        "s_waitcnt vmcnt(19)\n"
        "v_mfma_f32_16x16x32_bf16 v[128:131], v[88:91], a[224:227], v[128:131]\n"
        "s_waitcnt vmcnt(18)\n"
        "v_mfma_f32_16x16x32_bf16 v[132:135], v[92:95], a[224:227], v[132:135]\n"
        "v_mfma_f32_16x16x32_bf16 v[136:139], v[88:91], a[228:231], v[136:139]\n"
        "v_mfma_f32_16x16x32_bf16 v[140:143], v[92:95], a[228:231], v[140:143]\n"
        "v_mfma_f32_16x16x32_bf16 v[144:147], v[88:91], a[232:235], v[144:147]\n"
        "v_mfma_f32_16x16x32_bf16 v[148:151], v[92:95], a[232:235], v[148:151]\n"
        "v_mfma_f32_16x16x32_bf16 v[152:155], v[88:91], a[236:239], v[152:155]\n"
        "v_mfma_f32_16x16x32_bf16 v[156:159], v[92:95], a[236:239], v[156:159]\n"
        "v_mfma_f32_16x16x32_bf16 v[160:163], v[88:91], a[240:243], v[160:163]\n"
        "v_mfma_f32_16x16x32_bf16 v[164:167], v[92:95], a[240:243], v[164:167]\n"
        "v_mfma_f32_16x16x32_bf16 v[168:171], v[88:91], a[244:247], v[168:171]\n"
        "v_mfma_f32_16x16x32_bf16 v[172:175], v[92:95], a[244:247], v[172:175]\n"
        "v_mfma_f32_16x16x32_bf16 v[176:179], v[88:91], a[248:251], v[176:179]\n"
        "v_mfma_f32_16x16x32_bf16 v[180:183], v[92:95], a[248:251], v[180:183]\n"
        "v_mfma_f32_16x16x32_bf16 v[184:187], v[88:91], a[252:255], v[184:187]\n"
        "global_load_dwordx4 v[80:83], v56, %[sws] offset:2048 sc1\n"
        "v_mfma_f32_16x16x32_bf16 v[188:191], v[92:95], a[252:255], v[188:191]\n"
        "global_load_dwordx4 v[84:87], v57, %[sws] offset:2048 sc1\n"
        "s_waitcnt vmcnt(18)\n"
        "v_mfma_f32_16x16x32_bf16 v[192:195], v[96:99], a[0:3], 0\n"
        "s_waitcnt vmcnt(17)\n"
        "v_mfma_f32_16x16x32_bf16 v[196:199], v[100:103], a[0:3], 0\n"
        "v_cvt_pk_bf16_f32 v144, v144, v145\n"
        "v_cvt_pk_bf16_f32 v145, v146, v147\n"
        "v_mfma_f32_16x16x32_bf16 v[200:203], v[96:99], a[4:7], 0\n"
        "v_cvt_pk_bf16_f32 v146, v148, v149\n"
        "v_cvt_pk_bf16_f32 v147, v150, v151\n"
        "v_mfma_f32_16x16x32_bf16 v[204:207], v[100:103], a[4:7], 0\n"
        "ds_write_b128 v48, v[144:147] offset:0\n"
        "v_cvt_pk_bf16_f32 v152, v152, v153\n"
        "v_cvt_pk_bf16_f32 v153, v154, v155\n"
        "v_mfma_f32_16x16x32_bf16 v[208:211], v[96:99], a[8:11], 0\n"
        "v_cvt_pk_bf16_f32 v154, v156, v157\n"
        "v_cvt_pk_bf16_f32 v155, v158, v159\n"
        "v_add_u32 v62, s53, %[vl16]\n"
        "v_add_u32 v62, 24576, v62\n"
        "global_store_dwordx4 v62, v[52:55], %[sws] offset:0 sc1\n"
        "v_mfma_f32_16x16x32_bf16 v[212:215], v[100:103], a[8:11], 0\n"
        "ds_write_b128 v48, v[152:155] offset:1024\n"
        "v_cvt_pk_bf16_f32 v160, v160, v161\n"
        "v_cvt_pk_bf16_f32 v161, v162, v163\n"
        "v_mfma_f32_16x16x32_bf16 v[216:219], v[96:99], a[12:15], 0\n"
        "v_cvt_pk_bf16_f32 v162, v164, v165\n"
        "v_cvt_pk_bf16_f32 v163, v166, v167\n"
        "v_mfma_f32_16x16x32_bf16 v[220:223], v[100:103], a[12:15], 0\n"
        "ds_write_b128 v49, v[160:163] offset:0\n"
        "v_cvt_pk_bf16_f32 v168, v168, v169\n"
        "v_cvt_pk_bf16_f32 v169, v170, v171\n"
        "v_mfma_f32_16x16x32_bf16 v[224:227], v[96:99], a[16:19], 0\n"
        "v_cvt_pk_bf16_f32 v170, v172, v173\n"
        "v_cvt_pk_bf16_f32 v171, v174, v175\n"
        "v_mfma_f32_16x16x32_bf16 v[228:231], v[100:103], a[16:19], 0\n"
        "ds_write_b128 v49, v[168:171] offset:1024\n"
        "v_cvt_pk_bf16_f32 v176, v176, v177\n"
        "v_cvt_pk_bf16_f32 v177, v178, v179\n"
        "v_mfma_f32_16x16x32_bf16 v[232:235], v[96:99], a[20:23], 0\n"
        "v_cvt_pk_bf16_f32 v178, v180, v181\n"
        "v_cvt_pk_bf16_f32 v179, v182, v183\n"
        "v_mfma_f32_16x16x32_bf16 v[236:239], v[100:103], a[20:23], 0\n"
        "ds_write_b128 v50, v[176:179] offset:0\n"
        "v_cvt_pk_bf16_f32 v184, v184, v185\n"
        "v_cvt_pk_bf16_f32 v185, v186, v187\n"
        "v_mfma_f32_16x16x32_bf16 v[240:243], v[96:99], a[24:27], 0\n"
        "v_cvt_pk_bf16_f32 v186, v188, v189\n"
        "v_cvt_pk_bf16_f32 v187, v190, v191\n"
        "v_mfma_f32_16x16x32_bf16 v[244:247], v[100:103], a[24:27], 0\n"
        "ds_write_b128 v50, v[184:187] offset:1024\n"
        "v_mfma_f32_16x16x32_bf16 v[248:251], v[96:99], a[28:31], 0\n"
        "global_load_dwordx4 v[88:91], v56, %[sws] offset:3072 sc1\n"
        "v_mfma_f32_16x16x32_bf16 v[252:255], v[100:103], a[28:31], 0\n"
        "s_waitcnt lgkmcnt(0)\n"
        "s_barrier\n"
        "global_load_dwordx4 v[92:95], v57, %[sws] offset:3072 sc1\n"
        "s_waitcnt vmcnt(19)\n"
        "v_mfma_f32_16x16x32_bf16 v[192:195], v[104:107], a[32:35], v[192:195]\n"
        "ds_read_b128 v[144:147], v51 offset:0\n"
        "s_waitcnt vmcnt(18)\n"
        "v_mfma_f32_16x16x32_bf16 v[196:199], v[108:111], a[32:35], v[196:199]\n"
        "ds_read_b128 v[148:151], v51 offset:1024\n"
        "v_mfma_f32_16x16x32_bf16 v[200:203], v[104:107], a[36:39], v[200:203]\n"
        "ds_read_b128 v[152:155], v51 offset:2048\n"
        "v_mfma_f32_16x16x32_bf16 v[204:207], v[108:111], a[36:39], v[204:207]\n"
        "ds_read_b128 v[156:159], v51 offset:3072\n"
        "v_mfma_f32_16x16x32_bf16 v[208:211], v[104:107], a[40:43], v[208:211]\n"
        "ds_read_b128 v[160:163], v51 offset:4096\n"
        "v_add_u32 v62, s53, %[vl16]\n"
        "v_add_u32 v62, 32768, v62\n"
        "global_store_dwordx4 v62, v[52:55], %[sws] offset:0 sc1\n"
        "v_mfma_f32_16x16x32_bf16 v[212:215], v[108:111], a[40:43], v[212:215]\n"
        "ds_read_b128 v[164:167], v51 offset:5120\n"
        "v_mfma_f32_16x16x32_bf16 v[216:219], v[104:107], a[44:47], v[216:219]\n"
        "v_mfma_f32_16x16x32_bf16 v[220:223], v[108:111], a[44:47], v[220:223]\n"
        "s_waitcnt lgkmcnt(5)\n"
        "v_mfma_f32_16x16x32_bf16 v[128:131], v[40:43], v[144:147], v[128:131]\n"
        "v_mfma_f32_16x16x32_bf16 v[224:227], v[104:107], a[48:51], v[224:227]\n"
        "v_mfma_f32_16x16x32_bf16 v[132:135], v[42:45], v[144:147], v[132:135]\n"
        "v_mfma_f32_16x16x32_bf16 v[228:231], v[108:111], a[48:51], v[228:231]\n"
        "s_waitcnt lgkmcnt(4)\n"
        "v_mfma_f32_16x16x32_bf16 v[136:139], v[40:43], v[148:151], v[136:139]\n"
        "v_mfma_f32_16x16x32_bf16 v[232:235], v[104:107], a[52:55], v[232:235]\n"
        "v_mfma_f32_16x16x32_bf16 v[140:143], v[42:45], v[148:151], v[140:143]\n"
        "v_mfma_f32_16x16x32_bf16 v[236:239], v[108:111], a[52:55], v[236:239]\n"
        "s_waitcnt lgkmcnt(3)\n"
        "v_mfma_f32_16x16x32_bf16 v[128:131], v[40:43], v[152:155], v[128:131]\n"
        "v_mfma_f32_16x16x32_bf16 v[240:243], v[104:107], a[56:59], v[240:243]\n"
        "v_mfma_f32_16x16x32_bf16 v[132:135], v[42:45], v[152:155], v[132:135]\n"
        "v_mfma_f32_16x16x32_bf16 v[244:247], v[108:111], a[56:59], v[244:247]\n"
        "s_waitcnt lgkmcnt(2)\n"
        "v_mfma_f32_16x16x32_bf16 v[136:139], v[40:43], v[156:159], v[136:139]\n"
        "v_mfma_f32_16x16x32_bf16 v[248:251], v[104:107], a[60:63], v[248:251]\n"
        "s_waitcnt vmcnt(17)\n"
        "v_mov_b32 v62, s56\n"
        "v_add_u32 v62, 2, v62\n"
        "s_mov_b64 exec, 1\n"
        "v_mov_b32 v31, s34\n"
        "global_store_dwordx2 v31, v[62:63], %[sws] sc1\n"
        "s_mov_b64 exec, -1\n"
        "v_mfma_f32_16x16x32_bf16 v[140:143], v[42:45], v[156:159], v[140:143]\n"
        "s_bfe_u32 s59, s48, 262160\n"
        "s_lshl_b32 s59, s59, 14\n"
        "s_add_u32 s59, s59, s24\n"
        "v_add_u32 v56, s59, %[vl16]\n"
        "v_add_u32 v57, 8192, v56\n"
        "global_load_dwordx4 v[96:99], v56, %[sws] offset:-4096 sc1\n"
        "v_mfma_f32_16x16x32_bf16 v[252:255], v[108:111], a[60:63], v[252:255]\n"
        "s_waitcnt lgkmcnt(1)\n"
        "v_mfma_f32_16x16x32_bf16 v[128:131], v[40:43], v[160:163], v[128:131]\n"
        "global_load_dwordx4 v[100:103], v57, %[sws] offset:-4096 sc1\n"
        "s_waitcnt vmcnt(19)\n"
        "v_mfma_f32_16x16x32_bf16 v[192:195], v[112:115], a[64:67], v[192:195]\n"
        "v_mfma_f32_16x16x32_bf16 v[132:135], v[42:45], v[160:163], v[132:135]\n"
        "s_waitcnt vmcnt(18)\n"
        "v_mfma_f32_16x16x32_bf16 v[196:199], v[116:119], a[64:67], v[196:199]\n"
        "s_waitcnt lgkmcnt(0)\n"
        "v_mfma_f32_16x16x32_bf16 v[136:139], v[40:43], v[164:167], v[136:139]\n"
        "v_mfma_f32_16x16x32_bf16 v[200:203], v[112:115], a[68:71], v[200:203]\n"
        "v_mfma_f32_16x16x32_bf16 v[140:143], v[42:45], v[164:167], v[140:143]\n"
        "v_mfma_f32_16x16x32_bf16 v[204:207], v[116:119], a[68:71], v[204:207]\n"
        "s_barrier\n"
        "v_mfma_f32_16x16x32_bf16 v[208:211], v[112:115], a[72:75], v[208:211]\n"
        "v_lshrrev_b32 v62, 1, %[vl16]\n"
        "v_add_u32 v62, s39, v62\n"
        "global_store_dwordx2 v62, v[52:53], %[sws] offset:0 sc1\n"
        "v_mfma_f32_16x16x32_bf16 v[212:215], v[116:119], a[72:75], v[212:215]\n"
        "v_mfma_f32_16x16x32_bf16 v[216:219], v[112:115], a[76:79], v[216:219]\n"
        "v_mfma_f32_16x16x32_bf16 v[220:223], v[116:119], a[76:79], v[220:223]\n"
        "v_mfma_f32_16x16x32_bf16 v[224:227], v[112:115], a[80:83], v[224:227]\n"
        "v_mfma_f32_16x16x32_bf16 v[228:231], v[116:119], a[80:83], v[228:231]\n"
        "v_cvt_pk_bf16_f32 v144, v128, v129\n"
        "v_cvt_pk_bf16_f32 v145, v130, v131\n"
        "v_mfma_f32_16x16x32_bf16 v[232:235], v[112:115], a[84:87], v[232:235]\n"
        "v_cvt_pk_bf16_f32 v146, v132, v133\n"
        "v_cvt_pk_bf16_f32 v147, v134, v135\n"
        "v_mfma_f32_16x16x32_bf16 v[236:239], v[116:119], a[84:87], v[236:239]\n"
        "v_cvt_pk_bf16_f32 v148, v136, v137\n"
        "v_cvt_pk_bf16_f32 v149, v138, v139\n"
        "v_mfma_f32_16x16x32_bf16 v[240:243], v[112:115], a[88:91], v[240:243]\n"
        "v_cvt_pk_bf16_f32 v150, v140, v141\n"
        "v_cvt_pk_bf16_f32 v151, v142, v143\n"
        "v_add_u32 v58, s46, %[vl16]\n"
        "v_mfma_f32_16x16x32_bf16 v[244:247], v[116:119], a[88:91], v[244:247]\n"
        "v_mfma_f32_16x16x32_bf16 v[248:251], v[112:115], a[92:95], v[248:251]\n"
        "global_store_dwordx4 v58, v[144:147], %[sws] sc1\n"
        "global_load_dwordx4 v[104:107], v56, %[sws] offset:-3072 sc1\n"
        "v_mfma_f32_16x16x32_bf16 v[252:255], v[116:119], a[92:95], v[252:255]\n"
        "global_load_dwordx4 v[108:111], v57, %[sws] offset:-3072 sc1\n"
        "s_waitcnt vmcnt(20)\n"
        "v_mfma_f32_16x16x32_bf16 v[192:195], v[120:123], a[96:99], v[192:195]\n"
        "global_store_dwordx4 v58, v[148:151], %[sws] offset:1024 sc1\n"
        "s_waitcnt vmcnt(20)\n"
        "v_mfma_f32_16x16x32_bf16 v[196:199], v[124:127], a[96:99], v[196:199]\n"
        "v_mfma_f32_16x16x32_bf16 v[200:203], v[120:123], a[100:103], v[200:203]\n"
        "v_mfma_f32_16x16x32_bf16 v[204:207], v[124:127], a[100:103], v[204:207]\n"
        "v_mfma_f32_16x16x32_bf16 v[208:211], v[120:123], a[104:107], v[208:211]\n"
        "v_mfma_f32_16x16x32_bf16 v[212:215], v[124:127], a[104:107], v[212:215]\n"
        "v_mfma_f32_16x16x32_bf16 v[216:219], v[120:123], a[108:111], v[216:219]\n"
        "v_mfma_f32_16x16x32_bf16 v[220:223], v[124:127], a[108:111], v[220:223]\n"
        "v_mfma_f32_16x16x32_bf16 v[224:227], v[120:123], a[112:115], v[224:227]\n"
        "v_mfma_f32_16x16x32_bf16 v[228:231], v[124:127], a[112:115], v[228:231]\n"
        "v_mfma_f32_16x16x32_bf16 v[232:235], v[120:123], a[116:119], v[232:235]\n"
        "v_mfma_f32_16x16x32_bf16 v[236:239], v[124:127], a[116:119], v[236:239]\n"
        "v_mfma_f32_16x16x32_bf16 v[240:243], v[120:123], a[120:123], v[240:243]\n"
        "v_mfma_f32_16x16x32_bf16 v[244:247], v[124:127], a[120:123], v[244:247]\n"
        "v_mfma_f32_16x16x32_bf16 v[248:251], v[120:123], a[124:127], v[248:251]\n"
        "global_load_dwordx4 v[112:115], v56, %[sws] offset:-2048 sc1\n"
        "v_mfma_f32_16x16x32_bf16 v[252:255], v[124:127], a[124:127], v[252:255]\n"
        "global_load_dwordx4 v[116:119], v57, %[sws] offset:-2048 sc1\n"
        "s_waitcnt vmcnt(20)\n"
        "v_mfma_f32_16x16x32_bf16 v[192:195], v[64:67], a[128:131], v[192:195]\n"
        "s_waitcnt vmcnt(19)\n"
        "v_mfma_f32_16x16x32_bf16 v[196:199], v[68:71], a[128:131], v[196:199]\n"
        "v_mfma_f32_16x16x32_bf16 v[200:203], v[64:67], a[132:135], v[200:203]\n"
        "v_mfma_f32_16x16x32_bf16 v[204:207], v[68:71], a[132:135], v[204:207]\n"
        "v_mfma_f32_16x16x32_bf16 v[208:211], v[64:67], a[136:139], v[208:211]\n"
        "v_mfma_f32_16x16x32_bf16 v[212:215], v[68:71], a[136:139], v[212:215]\n"
        "v_mfma_f32_16x16x32_bf16 v[216:219], v[64:67], a[140:143], v[216:219]\n"
        "v_mfma_f32_16x16x32_bf16 v[220:223], v[68:71], a[140:143], v[220:223]\n"
        "v_mfma_f32_16x16x32_bf16 v[224:227], v[64:67], a[144:147], v[224:227]\n"
        "v_mfma_f32_16x16x32_bf16 v[228:231], v[68:71], a[144:147], v[228:231]\n"
        "v_mfma_f32_16x16x32_bf16 v[232:235], v[64:67], a[148:151], v[232:235]\n"
        "v_mfma_f32_16x16x32_bf16 v[236:239], v[68:71], a[148:151], v[236:239]\n"
        "v_mfma_f32_16x16x32_bf16 v[240:243], v[64:67], a[152:155], v[240:243]\n"
        "v_mfma_f32_16x16x32_bf16 v[244:247], v[68:71], a[152:155], v[244:247]\n"
        "v_mfma_f32_16x16x32_bf16 v[248:251], v[64:67], a[156:159], v[248:251]\n"
        "global_load_dwordx4 v[120:123], v56, %[sws] offset:-1024 sc1\n"
        "v_mfma_f32_16x16x32_bf16 v[252:255], v[68:71], a[156:159], v[252:255]\n"
        "global_load_dwordx4 v[124:127], v57, %[sws] offset:-1024 sc1\n"
        "s_waitcnt vmcnt(19)\n"
        "v_mfma_f32_16x16x32_bf16 v[192:195], v[72:75], a[160:163], v[192:195]\n"
        "s_waitcnt vmcnt(18)\n"
        "v_mfma_f32_16x16x32_bf16 v[196:199], v[76:79], a[160:163], v[196:199]\n"
        "v_mfma_f32_16x16x32_bf16 v[200:203], v[72:75], a[164:167], v[200:203]\n"
        "v_mfma_f32_16x16x32_bf16 v[204:207], v[76:79], a[164:167], v[204:207]\n"
        "v_mfma_f32_16x16x32_bf16 v[208:211], v[72:75], a[168:171], v[208:211]\n"
        "v_mfma_f32_16x16x32_bf16 v[212:215], v[76:79], a[168:171], v[212:215]\n"
        "v_mfma_f32_16x16x32_bf16 v[216:219], v[72:75], a[172:175], v[216:219]\n"
        "v_mfma_f32_16x16x32_bf16 v[220:223], v[76:79], a[172:175], v[220:223]\n"
        "v_mfma_f32_16x16x32_bf16 v[224:227], v[72:75], a[176:179], v[224:227]\n"
        "global_load_dwordx2 v[60:61], v34, %[sws] sc1\n"
        "v_mfma_f32_16x16x32_bf16 v[228:231], v[76:79], a[176:179], v[228:231]\n"
        "v_mfma_f32_16x16x32_bf16 v[232:235], v[72:75], a[180:183], v[232:235]\n"
        "v_mfma_f32_16x16x32_bf16 v[236:239], v[76:79], a[180:183], v[236:239]\n"
        "v_mfma_f32_16x16x32_bf16 v[240:243], v[72:75], a[184:187], v[240:243]\n"
        "v_mfma_f32_16x16x32_bf16 v[244:247], v[76:79], a[184:187], v[244:247]\n"
        "v_mfma_f32_16x16x32_bf16 v[248:251], v[72:75], a[188:191], v[248:251]\n"
        "global_load_dwordx4 v[64:67], v56, %[sws] offset:0 sc1\n"
        "v_mfma_f32_16x16x32_bf16 v[252:255], v[76:79], a[188:191], v[252:255]\n"
        "global_load_dwordx4 v[68:71], v57, %[sws] offset:0 sc1\n"
        "s_waitcnt vmcnt(20)\n"
        "v_mfma_f32_16x16x32_bf16 v[192:195], v[80:83], a[192:195], v[192:195]\n"
        "s_waitcnt vmcnt(19)\n"
        "v_mfma_f32_16x16x32_bf16 v[196:199], v[84:87], a[192:195], v[196:199]\n"
        "v_mfma_f32_16x16x32_bf16 v[200:203], v[80:83], a[196:199], v[200:203]\n"
        "v_mfma_f32_16x16x32_bf16 v[204:207], v[84:87], a[196:199], v[204:207]\n"
        "v_mfma_f32_16x16x32_bf16 v[208:211], v[80:83], a[200:203], v[208:211]\n"
        "v_mfma_f32_16x16x32_bf16 v[212:215], v[84:87], a[200:203], v[212:215]\n"
        "v_mfma_f32_16x16x32_bf16 v[216:219], v[80:83], a[204:207], v[216:219]\n"
        "v_mfma_f32_16x16x32_bf16 v[220:223], v[84:87], a[204:207], v[220:223]\n"
        "v_mfma_f32_16x16x32_bf16 v[224:227], v[80:83], a[208:211], v[224:227]\n"
        "v_mfma_f32_16x16x32_bf16 v[228:231], v[84:87], a[208:211], v[228:231]\n"
        "v_mfma_f32_16x16x32_bf16 v[232:235], v[80:83], a[212:215], v[232:235]\n"
        "v_mfma_f32_16x16x32_bf16 v[236:239], v[84:87], a[212:215], v[236:239]\n"
        "v_mfma_f32_16x16x32_bf16 v[240:243], v[80:83], a[216:219], v[240:243]\n"
        "v_mfma_f32_16x16x32_bf16 v[244:247], v[84:87], a[216:219], v[244:247]\n"
        "v_mfma_f32_16x16x32_bf16 v[248:251], v[80:83], a[220:223], v[248:251]\n"
        "global_load_dwordx4 v[72:75], v56, %[sws] offset:1024 sc1\n"
        "v_mfma_f32_16x16x32_bf16 v[252:255], v[84:87], a[220:223], v[252:255]\n"
        "global_load_dwordx4 v[76:79], v57, %[sws] offset:1024 sc1\n"
        "s_waitcnt vmcnt(19)\n"
        "v_mfma_f32_16x16x32_bf16 v[192:195], v[88:91], a[224:227], v[192:195]\n"
        "s_waitcnt vmcnt(18)\n"
        "v_mfma_f32_16x16x32_bf16 v[196:199], v[92:95], a[224:227], v[196:199]\n"
        "v_mfma_f32_16x16x32_bf16 v[200:203], v[88:91], a[228:231], v[200:203]\n"
        "v_mfma_f32_16x16x32_bf16 v[204:207], v[92:95], a[228:231], v[204:207]\n"
        "v_mfma_f32_16x16x32_bf16 v[208:211], v[88:91], a[232:235], v[208:211]\n"
        "v_mfma_f32_16x16x32_bf16 v[212:215], v[92:95], a[232:235], v[212:215]\n"
        "v_mfma_f32_16x16x32_bf16 v[216:219], v[88:91], a[236:239], v[216:219]\n"
        "v_mfma_f32_16x16x32_bf16 v[220:223], v[92:95], a[236:239], v[220:223]\n"
        "v_mfma_f32_16x16x32_bf16 v[224:227], v[88:91], a[240:243], v[224:227]\n"
        "v_mfma_f32_16x16x32_bf16 v[228:231], v[92:95], a[240:243], v[228:231]\n"
        "v_mfma_f32_16x16x32_bf16 v[232:235], v[88:91], a[244:247], v[232:235]\n"
        "v_mfma_f32_16x16x32_bf16 v[236:239], v[92:95], a[244:247], v[236:239]\n"
        "v_mfma_f32_16x16x32_bf16 v[240:243], v[88:91], a[248:251], v[240:243]\n"
        "s_waitcnt vmcnt(9)\n"
        "v_mov_b32 v62, s56\n"
        "v_add_u32 v62, 3, v62\n"
        "s_mov_b64 exec, 1\n"
        "v_mov_b32 v31, s34\n"
        "global_store_dwordx2 v31, v[62:63], %[sws] sc1\n"
        "s_mov_b64 exec, -1\n"
        "v_mfma_f32_16x16x32_bf16 v[244:247], v[92:95], a[248:251], v[244:247]\n"
        "v_mfma_f32_16x16x32_bf16 v[248:251], v[88:91], a[252:255], v[248:251]\n"
        "global_load_dwordx4 v[80:83], v56, %[sws] offset:2048 sc1\n"
        "v_mfma_f32_16x16x32_bf16 v[252:255], v[92:95], a[252:255], v[252:255]\n"
        "global_load_dwordx4 v[84:87], v57, %[sws] offset:2048 sc1\n"
        "v_mfma_f32_16x16x32_bf16 v[144:147], v[96:99], a[8:11], 0\n"
        "v_mfma_f32_16x16x32_bf16 v[148:151], v[100:103], a[8:11], 0\n"
        "v_cvt_pk_bf16_f32 v208, v208, v209\n"
        "v_cvt_pk_bf16_f32 v209, v210, v211\n"
        "v_mfma_f32_16x16x32_bf16 v[152:155], v[96:99], a[12:15], 0\n"
        "v_cvt_pk_bf16_f32 v210, v212, v213\n"
        "v_cvt_pk_bf16_f32 v211, v214, v215\n"
        "v_mfma_f32_16x16x32_bf16 v[156:159], v[100:103], a[12:15], 0\n"
        "ds_write_b128 v48, v[208:211] offset:0\n"
        "v_cvt_pk_bf16_f32 v216, v216, v217\n"
        "v_cvt_pk_bf16_f32 v217, v218, v219\n"
        "v_mfma_f32_16x16x32_bf16 v[160:163], v[96:99], a[16:19], 0\n"
        "v_cvt_pk_bf16_f32 v218, v220, v221\n"
        "v_cvt_pk_bf16_f32 v219, v222, v223\n"
        "v_mfma_f32_16x16x32_bf16 v[164:167], v[100:103], a[16:19], 0\n"
        "ds_write_b128 v48, v[216:219] offset:1024\n"
        "v_cvt_pk_bf16_f32 v224, v224, v225\n"
        "v_cvt_pk_bf16_f32 v225, v226, v227\n"
        "v_mfma_f32_16x16x32_bf16 v[168:171], v[96:99], a[20:23], 0\n"
        "v_cvt_pk_bf16_f32 v226, v228, v229\n"
        "v_cvt_pk_bf16_f32 v227, v230, v231\n"
        "v_mfma_f32_16x16x32_bf16 v[172:175], v[100:103], a[20:23], 0\n"
        "ds_write_b128 v49, v[224:227] offset:0\n"
        "v_cvt_pk_bf16_f32 v232, v232, v233\n"
        "v_cvt_pk_bf16_f32 v233, v234, v235\n"
        "v_mfma_f32_16x16x32_bf16 v[176:179], v[96:99], a[24:27], 0\n"
        "v_cvt_pk_bf16_f32 v234, v236, v237\n"
        "v_cvt_pk_bf16_f32 v235, v238, v239\n"
        "v_mfma_f32_16x16x32_bf16 v[180:183], v[100:103], a[24:27], 0\n"
        "ds_write_b128 v49, v[232:235] offset:1024\n"
        "v_cvt_pk_bf16_f32 v240, v240, v241\n"
        "v_cvt_pk_bf16_f32 v241, v242, v243\n"
        "v_mfma_f32_16x16x32_bf16 v[184:187], v[96:99], a[28:31], 0\n"
        "v_cvt_pk_bf16_f32 v242, v244, v245\n"
        "v_cvt_pk_bf16_f32 v243, v246, v247\n"
        "v_mfma_f32_16x16x32_bf16 v[188:191], v[100:103], a[28:31], 0\n"
        "ds_write_b128 v50, v[240:243] offset:0\n"
        "v_cvt_pk_bf16_f32 v248, v248, v249\n"
        "v_cvt_pk_bf16_f32 v249, v250, v251\n"
        "global_load_dwordx4 v[88:91], v56, %[sws] offset:3072 sc1\n"
        "global_load_dwordx4 v[92:95], v57, %[sws] offset:3072 sc1\n"
        "v_mfma_f32_16x16x32_bf16 v[144:147], v[104:107], a[40:43], v[144:147]\n"
        "v_cvt_pk_bf16_f32 v250, v252, v253\n"
        "v_cvt_pk_bf16_f32 v251, v254, v255\n"
        "v_mfma_f32_16x16x32_bf16 v[148:151], v[108:111], a[40:43], v[148:151]\n"
        "ds_write_b128 v50, v[248:251] offset:1024\n"
        "v_mfma_f32_16x16x32_bf16 v[152:155], v[104:107], a[44:47], v[152:155]\n"
        "v_mfma_f32_16x16x32_bf16 v[156:159], v[108:111], a[44:47], v[156:159]\n"
        "s_waitcnt lgkmcnt(0)\n"
        "s_barrier\n"
        "v_mfma_f32_16x16x32_bf16 v[160:163], v[104:107], a[48:51], v[160:163]\n"
        "ds_read_b128 v[208:211], v51 offset:0\n"
        "v_mfma_f32_16x16x32_bf16 v[164:167], v[108:111], a[48:51], v[164:167]\n"
        "ds_read_b128 v[212:215], v51 offset:1024\n"
        "v_mfma_f32_16x16x32_bf16 v[168:171], v[104:107], a[52:55], v[168:171]\n"
        "ds_read_b128 v[216:219], v51 offset:2048\n"
        "v_mfma_f32_16x16x32_bf16 v[172:175], v[108:111], a[52:55], v[172:175]\n"
        "ds_read_b128 v[220:223], v51 offset:3072\n"
        "v_mfma_f32_16x16x32_bf16 v[176:179], v[104:107], a[56:59], v[176:179]\n"
        "ds_read_b128 v[224:227], v51 offset:4096\n"
        "v_mfma_f32_16x16x32_bf16 v[180:183], v[108:111], a[56:59], v[180:183]\n"
        "ds_read_b128 v[228:231], v51 offset:5120\n"
        "s_waitcnt vmcnt(9)\n"
        "v_bfe_u32 v59, v33, 20, 4\n"
        "v_subrev_u32 v60, s56, v60\n"
        "v_sub_u32 v60, v60, v59\n"
        "v_cmp_lt_u32 vcc, 7, v60\n"
        "v_cmp_ne_u32 s[82:83], s57, v61\n"
        "s_or_b64 vcc, vcc, s[82:83]\n"
        "s_cbranch_vccnz Rxfc_%=\n"
        "Cxfc_%=:\n"
        "v_mfma_f32_16x16x32_bf16 v[184:187], v[104:107], a[60:63], v[184:187]\n"
        "s_cmp_lg_u32 s51, 0\n"
        "s_cbranch_scc1 G2K_%=\n"
        "v_mov_b32 v62, s56\n"
        "s_mov_b64 exec, 1\n"
        "v_mov_b32 v31, s35\n"
        "global_store_dwordx2 v31, v[62:63], %[sws] sc1\n"
        "s_mov_b64 exec, -1\n"
        "G2K_%=:\n"
        "v_add_u32 v31, s25, %[vl16]\n"
        "global_load_dwordx4 v[248:251], v31, %[sws] offset:0 sc1\n"
        "v_mfma_f32_16x16x32_bf16 v[188:191], v[108:111], a[60:63], v[188:191]\n"
        "s_waitcnt lgkmcnt(5)\n"
        "v_mfma_f32_16x16x32_bf16 v[192:195], v[40:43], v[208:211], v[192:195]\n"
        "v_mfma_f32_16x16x32_bf16 v[144:147], v[112:115], a[72:75], v[144:147]\n"
        "v_mfma_f32_16x16x32_bf16 v[196:199], v[42:45], v[208:211], v[196:199]\n"
        "global_load_dwordx4 v[252:255], v31, %[sws] offset:1024 sc1\n"
        "v_mfma_f32_16x16x32_bf16 v[148:151], v[116:119], a[72:75], v[148:151]\n"
        "s_waitcnt lgkmcnt(4)\n"
        "v_mfma_f32_16x16x32_bf16 v[200:203], v[40:43], v[212:215], v[200:203]\n"
        "v_mfma_f32_16x16x32_bf16 v[152:155], v[112:115], a[76:79], v[152:155]\n"
        "v_mfma_f32_16x16x32_bf16 v[204:207], v[42:45], v[212:215], v[204:207]\n"
        "v_mfma_f32_16x16x32_bf16 v[156:159], v[116:119], a[76:79], v[156:159]\n"
        "s_waitcnt lgkmcnt(3)\n"
        "v_mfma_f32_16x16x32_bf16 v[192:195], v[40:43], v[216:219], v[192:195]\n"
        "v_mfma_f32_16x16x32_bf16 v[160:163], v[112:115], a[80:83], v[160:163]\n"
        "v_mfma_f32_16x16x32_bf16 v[196:199], v[42:45], v[216:219], v[196:199]\n"
        "v_mfma_f32_16x16x32_bf16 v[164:167], v[116:119], a[80:83], v[164:167]\n"
        "s_waitcnt lgkmcnt(2)\n"
        "v_mfma_f32_16x16x32_bf16 v[200:203], v[40:43], v[220:223], v[200:203]\n"
        "v_mfma_f32_16x16x32_bf16 v[168:171], v[112:115], a[84:87], v[168:171]\n"
        "v_mfma_f32_16x16x32_bf16 v[204:207], v[42:45], v[220:223], v[204:207]\n"
        "v_mfma_f32_16x16x32_bf16 v[172:175], v[116:119], a[84:87], v[172:175]\n"
        "s_waitcnt lgkmcnt(1)\n"
        "v_mfma_f32_16x16x32_bf16 v[192:195], v[40:43], v[224:227], v[192:195]\n"
        "v_mfma_f32_16x16x32_bf16 v[176:179], v[112:115], a[88:91], v[176:179]\n"
        "v_mfma_f32_16x16x32_bf16 v[196:199], v[42:45], v[224:227], v[196:199]\n"
        "v_mfma_f32_16x16x32_bf16 v[180:183], v[116:119], a[88:91], v[180:183]\n"
        "s_waitcnt lgkmcnt(0)\n"
        "v_mfma_f32_16x16x32_bf16 v[200:203], v[40:43], v[228:231], v[200:203]\n"
        "v_mfma_f32_16x16x32_bf16 v[184:187], v[112:115], a[92:95], v[184:187]\n"
        "v_mfma_f32_16x16x32_bf16 v[204:207], v[42:45], v[228:231], v[204:207]\n"
        "v_mfma_f32_16x16x32_bf16 v[188:191], v[116:119], a[92:95], v[188:191]\n"
        "s_barrier\n"
        "v_mfma_f32_16x16x32_bf16 v[144:147], v[120:123], a[104:107], v[144:147]\n"
        "v_mfma_f32_16x16x32_bf16 v[148:151], v[124:127], a[104:107], v[148:151]\n"
        "v_mfma_f32_16x16x32_bf16 v[152:155], v[120:123], a[108:111], v[152:155]\n"
        "v_mfma_f32_16x16x32_bf16 v[156:159], v[124:127], a[108:111], v[156:159]\n"
        "v_mfma_f32_16x16x32_bf16 v[160:163], v[120:123], a[112:115], v[160:163]\n"
        "v_lshrrev_b32 v31, 1, %[vl16]\n"
        "v_add_u32 v31, s26, v31\n"
        "global_load_dwordx2 v[216:217], v31, %[sws] offset:-4096 sc1\n"
        "v_mfma_f32_16x16x32_bf16 v[164:167], v[124:127], a[112:115], v[164:167]\n"
        "v_cvt_pk_bf16_f32 v208, v192, v193\n"
        "v_cvt_pk_bf16_f32 v209, v194, v195\n"
        "global_load_dwordx2 v[218:219], v31, %[sws] offset:-2048 sc1\n"
        "v_mfma_f32_16x16x32_bf16 v[168:171], v[120:123], a[116:119], v[168:171]\n"
        "v_cvt_pk_bf16_f32 v210, v196, v197\n"
        "v_cvt_pk_bf16_f32 v211, v198, v199\n"
        "global_load_dwordx2 v[220:221], v31, %[sws] offset:0 sc1\n"
        "v_mfma_f32_16x16x32_bf16 v[172:175], v[124:127], a[116:119], v[172:175]\n"
        "v_cvt_pk_bf16_f32 v212, v200, v201\n"
        "v_cvt_pk_bf16_f32 v213, v202, v203\n"
        "global_load_dwordx2 v[222:223], v31, %[sws] offset:2048 sc1\n"
        "v_mfma_f32_16x16x32_bf16 v[176:179], v[120:123], a[120:123], v[176:179]\n"
        "v_cvt_pk_bf16_f32 v214, v204, v205\n"
        "v_cvt_pk_bf16_f32 v215, v206, v207\n"
        "v_add_u32 v58, s47, %[vl16]\n"
        "global_load_dwordx2 v[60:61], v34, %[sws] sc1\n"
        "v_mfma_f32_16x16x32_bf16 v[180:183], v[124:127], a[120:123], v[180:183]\n"
        "v_min_u32 v208, -2, v208\n"
        "v_min_u32 v209, -2, v209\n"
        "v_min_u32 v210, -2, v210\n"
        "v_min_u32 v211, -2, v211\n"
        "v_mfma_f32_16x16x32_bf16 v[184:187], v[120:123], a[124:127], v[184:187]\n"
        "global_store_dwordx4 v58, v[208:211], %[sws] sc1\n"
        "v_mfma_f32_16x16x32_bf16 v[188:191], v[124:127], a[124:127], v[188:191]\n"
        "v_min_u32 v212, -2, v212\n"
        "v_min_u32 v213, -2, v213\n"
        "v_min_u32 v214, -2, v214\n"
        "v_min_u32 v215, -2, v215\n"
        "s_waitcnt vmcnt(16)\n"
        "v_mfma_f32_16x16x32_bf16 v[144:147], v[64:67], a[136:139], v[144:147]\n"
        "global_store_dwordx4 v58, v[212:215], %[sws] offset:1024 sc1\n"
        "s_waitcnt vmcnt(16)\n"
        "v_mfma_f32_16x16x32_bf16 v[148:151], v[68:71], a[136:139], v[148:151]\n"
        "v_mfma_f32_16x16x32_bf16 v[152:155], v[64:67], a[140:143], v[152:155]\n"
        "v_mfma_f32_16x16x32_bf16 v[156:159], v[68:71], a[140:143], v[156:159]\n"
        "v_mfma_f32_16x16x32_bf16 v[160:163], v[64:67], a[144:147], v[160:163]\n"
        "v_mfma_f32_16x16x32_bf16 v[164:167], v[68:71], a[144:147], v[164:167]\n"
        "v_mfma_f32_16x16x32_bf16 v[168:171], v[64:67], a[148:151], v[168:171]\n"
        "v_mfma_f32_16x16x32_bf16 v[172:175], v[68:71], a[148:151], v[172:175]\n"
        "v_mfma_f32_16x16x32_bf16 v[176:179], v[64:67], a[152:155], v[176:179]\n"
        "v_mfma_f32_16x16x32_bf16 v[180:183], v[68:71], a[152:155], v[180:183]\n"
        "v_mfma_f32_16x16x32_bf16 v[184:187], v[64:67], a[156:159], v[184:187]\n"
        "v_mfma_f32_16x16x32_bf16 v[188:191], v[68:71], a[156:159], v[188:191]\n"
        "s_waitcnt vmcnt(15)\n"
        "v_mfma_f32_16x16x32_bf16 v[144:147], v[72:75], a[168:171], v[144:147]\n"
        "s_waitcnt vmcnt(14)\n"
        "v_mfma_f32_16x16x32_bf16 v[148:151], v[76:79], a[168:171], v[148:151]\n"
        "v_mfma_f32_16x16x32_bf16 v[152:155], v[72:75], a[172:175], v[152:155]\n"
        "v_mfma_f32_16x16x32_bf16 v[156:159], v[76:79], a[172:175], v[156:159]\n"
        "v_mfma_f32_16x16x32_bf16 v[160:163], v[72:75], a[176:179], v[160:163]\n"
        "v_mfma_f32_16x16x32_bf16 v[164:167], v[76:79], a[176:179], v[164:167]\n"
        "v_mfma_f32_16x16x32_bf16 v[168:171], v[72:75], a[180:183], v[168:171]\n"
        "v_mfma_f32_16x16x32_bf16 v[172:175], v[76:79], a[180:183], v[172:175]\n"
        "v_mfma_f32_16x16x32_bf16 v[176:179], v[72:75], a[184:187], v[176:179]\n"
        "v_mfma_f32_16x16x32_bf16 v[180:183], v[76:79], a[184:187], v[180:183]\n"
        "v_mfma_f32_16x16x32_bf16 v[184:187], v[72:75], a[188:191], v[184:187]\n"
        "v_mfma_f32_16x16x32_bf16 v[188:191], v[76:79], a[188:191], v[188:191]\n"
        "s_waitcnt vmcnt(12)\n"
        "v_mfma_f32_16x16x32_bf16 v[144:147], v[80:83], a[200:203], v[144:147]\n"
        "s_waitcnt vmcnt(11)\n"
        "v_mfma_f32_16x16x32_bf16 v[148:151], v[84:87], a[200:203], v[148:151]\n"
        "v_mfma_f32_16x16x32_bf16 v[152:155], v[80:83], a[204:207], v[152:155]\n"
        "v_mfma_f32_16x16x32_bf16 v[156:159], v[84:87], a[204:207], v[156:159]\n"
        "v_mfma_f32_16x16x32_bf16 v[160:163], v[80:83], a[208:211], v[160:163]\n"
        "v_mfma_f32_16x16x32_bf16 v[164:167], v[84:87], a[208:211], v[164:167]\n"
        "v_mfma_f32_16x16x32_bf16 v[168:171], v[80:83], a[212:215], v[168:171]\n"
        "v_mfma_f32_16x16x32_bf16 v[172:175], v[84:87], a[212:215], v[172:175]\n"
        "v_mfma_f32_16x16x32_bf16 v[176:179], v[80:83], a[216:219], v[176:179]\n"
        "v_mfma_f32_16x16x32_bf16 v[180:183], v[84:87], a[216:219], v[180:183]\n"
        "v_mfma_f32_16x16x32_bf16 v[184:187], v[80:83], a[220:223], v[184:187]\n"
        "v_mfma_f32_16x16x32_bf16 v[188:191], v[84:87], a[220:223], v[188:191]\n"
        "s_waitcnt vmcnt(10)\n"
        "v_mfma_f32_16x16x32_bf16 v[144:147], v[88:91], a[232:235], v[144:147]\n"
        "s_waitcnt vmcnt(9)\n"
        "v_mfma_f32_16x16x32_bf16 v[148:151], v[92:95], a[232:235], v[148:151]\n"
        "v_mfma_f32_16x16x32_bf16 v[152:155], v[88:91], a[236:239], v[152:155]\n"
        "s_waitcnt vmcnt(2)\n"
        "v_bfe_u32 v59, v33, 24, 4\n"
        "v_subrev_u32 v60, s56, v60\n"
        "v_sub_u32 v60, v60, v59\n"
        "v_cmp_lt_u32 vcc, 7, v60\n"
        "v_cmp_ne_u32 s[82:83], s57, v61\n"
        "s_or_b64 vcc, vcc, s[82:83]\n"
        "s_cbranch_vccnz Rxfd_%=\n"
        "Cxfd_%=:\n"
        "v_add_u32 v31, s25, %[vl16]\n"
        "v_add_u32 v31, 8192, v31\n"
        "global_load_dwordx4 v[224:227], v31, %[sws] offset:0 sc1\n"
        "global_load_dwordx4 v[228:231], v31, %[sws] offset:1024 sc1\n"
        "v_mfma_f32_16x16x32_bf16 v[156:159], v[92:95], a[236:239], v[156:159]\n"
        "v_mfma_f32_16x16x32_bf16 v[160:163], v[88:91], a[240:243], v[160:163]\n"
        "v_mfma_f32_16x16x32_bf16 v[164:167], v[92:95], a[240:243], v[164:167]\n"
        "v_mfma_f32_16x16x32_bf16 v[168:171], v[88:91], a[244:247], v[168:171]\n"
        "v_mfma_f32_16x16x32_bf16 v[172:175], v[92:95], a[244:247], v[172:175]\n"
        "v_mfma_f32_16x16x32_bf16 v[176:179], v[88:91], a[248:251], v[176:179]\n"
        "v_mfma_f32_16x16x32_bf16 v[180:183], v[92:95], a[248:251], v[180:183]\n"
        "v_mfma_f32_16x16x32_bf16 v[184:187], v[88:91], a[252:255], v[184:187]\n"
        "v_mfma_f32_16x16x32_bf16 v[188:191], v[92:95], a[252:255], v[188:191]\n"
        "v_mfma_f32_16x16x32_bf16 v[128:131], v[96:99], a[0:3], 0\n"
        "v_mfma_f32_16x16x32_bf16 v[132:135], v[100:103], a[0:3], 0\n"
        "v_mfma_f32_16x16x32_bf16 v[136:139], v[96:99], a[4:7], 0\n"
        "v_mfma_f32_16x16x32_bf16 v[140:143], v[100:103], a[4:7], 0\n"
        "v_mfma_f32_16x16x32_bf16 v[128:131], v[104:107], a[32:35], v[128:131]\n"
        "v_cvt_pk_bf16_f32 v144, v144, v145\n"
        "v_cvt_pk_bf16_f32 v145, v146, v147\n"
        "v_mfma_f32_16x16x32_bf16 v[128:131], v[40:43], v[248:251], v[128:131]\n"
        "v_mfma_f32_16x16x32_bf16 v[132:135], v[108:111], a[32:35], v[132:135]\n"
        "v_cvt_pk_bf16_f32 v146, v148, v149\n"
        "v_cvt_pk_bf16_f32 v147, v150, v151\n"
        "v_mfma_f32_16x16x32_bf16 v[132:135], v[42:45], v[248:251], v[132:135]\n"
        "v_mfma_f32_16x16x32_bf16 v[136:139], v[104:107], a[36:39], v[136:139]\n"
        "ds_write_b128 v48, v[144:147] offset:0\n"
        "v_mfma_f32_16x16x32_bf16 v[136:139], v[40:43], v[252:255], v[136:139]\n"
        "v_mfma_f32_16x16x32_bf16 v[140:143], v[108:111], a[36:39], v[140:143]\n"
        "v_mfma_f32_16x16x32_bf16 v[140:143], v[42:45], v[252:255], v[140:143]\n"
        "v_mfma_f32_16x16x32_bf16 v[128:131], v[112:115], a[64:67], v[128:131]\n"
        "v_cvt_pk_bf16_f32 v152, v152, v153\n"
        "v_cvt_pk_bf16_f32 v153, v154, v155\n"
        "v_mfma_f32_16x16x32_bf16 v[128:131], v[40:43], v[16:19], v[128:131]\n"
        "v_mfma_f32_16x16x32_bf16 v[132:135], v[116:119], a[64:67], v[132:135]\n"
        "v_cvt_pk_bf16_f32 v154, v156, v157\n"
        "v_cvt_pk_bf16_f32 v155, v158, v159\n"
        "v_mfma_f32_16x16x32_bf16 v[132:135], v[42:45], v[16:19], v[132:135]\n"
        "v_mfma_f32_16x16x32_bf16 v[136:139], v[112:115], a[68:71], v[136:139]\n"
        "ds_write_b128 v48, v[152:155] offset:1024\n"
        "v_mfma_f32_16x16x32_bf16 v[136:139], v[40:43], v[20:23], v[136:139]\n"
        "v_mfma_f32_16x16x32_bf16 v[140:143], v[116:119], a[68:71], v[140:143]\n"
        "v_mfma_f32_16x16x32_bf16 v[140:143], v[42:45], v[20:23], v[140:143]\n"
        "v_mfma_f32_16x16x32_bf16 v[128:131], v[120:123], a[96:99], v[128:131]\n"
        "v_cvt_pk_bf16_f32 v160, v160, v161\n"
        "v_cvt_pk_bf16_f32 v161, v162, v163\n"
        "v_mfma_f32_16x16x32_bf16 v[132:135], v[124:127], a[96:99], v[132:135]\n"
        "v_cvt_pk_bf16_f32 v162, v164, v165\n"
        "v_cvt_pk_bf16_f32 v163, v166, v167\n"
        "v_mfma_f32_16x16x32_bf16 v[192:195], v[44:47], v[216:219], 0\n"
        "v_mfma_f32_16x16x32_bf16 v[136:139], v[120:123], a[100:103], v[136:139]\n"
        "ds_write_b128 v49, v[160:163] offset:0\n"
        "v_mfma_f32_16x16x32_bf16 v[192:195], v[44:47], v[220:223], v[192:195]\n"
        "v_mfma_f32_16x16x32_bf16 v[140:143], v[124:127], a[100:103], v[140:143]\n"
        "v_mfma_f32_16x16x32_bf16 v[128:131], v[64:67], a[128:131], v[128:131]\n"
        "v_cvt_pk_bf16_f32 v168, v168, v169\n"
        "v_cvt_pk_bf16_f32 v169, v170, v171\n"
        "v_mfma_f32_16x16x32_bf16 v[132:135], v[68:71], a[128:131], v[132:135]\n"
        "v_cvt_pk_bf16_f32 v170, v172, v173\n"
        "v_cvt_pk_bf16_f32 v171, v174, v175\n"
        "v_mfma_f32_16x16x32_bf16 v[136:139], v[64:67], a[132:135], v[136:139]\n"
        "ds_write_b128 v49, v[168:171] offset:1024\n"
        "v_mfma_f32_16x16x32_bf16 v[140:143], v[68:71], a[132:135], v[140:143]\n"
        "v_mfma_f32_16x16x32_bf16 v[128:131], v[72:75], a[160:163], v[128:131]\n"
        "v_cvt_pk_bf16_f32 v176, v176, v177\n"
        "v_cvt_pk_bf16_f32 v177, v178, v179\n"
        "v_mfma_f32_16x16x32_bf16 v[132:135], v[76:79], a[160:163], v[132:135]\n"
        "v_cvt_pk_bf16_f32 v178, v180, v181\n"
        "v_cvt_pk_bf16_f32 v179, v182, v183\n"
        "v_mfma_f32_16x16x32_bf16 v[136:139], v[72:75], a[164:167], v[136:139]\n"
        "ds_write_b128 v50, v[176:179] offset:0\n"
        "v_mfma_f32_16x16x32_bf16 v[140:143], v[76:79], a[164:167], v[140:143]\n"
        "v_mfma_f32_16x16x32_bf16 v[128:131], v[80:83], a[192:195], v[128:131]\n"
        "v_cvt_pk_bf16_f32 v184, v184, v185\n"
        "v_cvt_pk_bf16_f32 v185, v186, v187\n"
        "v_mfma_f32_16x16x32_bf16 v[132:135], v[84:87], a[192:195], v[132:135]\n"
        "v_cvt_pk_bf16_f32 v186, v188, v189\n"
        "v_cvt_pk_bf16_f32 v187, v190, v191\n"
        "v_mfma_f32_16x16x32_bf16 v[136:139], v[80:83], a[196:199], v[136:139]\n"
        "ds_write_b128 v50, v[184:187] offset:1024\n"
        "v_add_u32 v31, s25, %[vl16]\n"
        "v_add_u32 v31, 16384, v31\n"
        "global_load_dwordx4 v[208:211], v31, %[sws] offset:0 sc1\n"
        "global_load_dwordx4 v[212:215], v31, %[sws] offset:1024 sc1\n"
        "v_mfma_f32_16x16x32_bf16 v[140:143], v[84:87], a[196:199], v[140:143]\n"
        "v_mfma_f32_16x16x32_bf16 v[128:131], v[88:91], a[224:227], v[128:131]\n"
        "v_mfma_f32_16x16x32_bf16 v[132:135], v[92:95], a[224:227], v[132:135]\n"
        "v_mfma_f32_16x16x32_bf16 v[136:139], v[88:91], a[228:231], v[136:139]\n"
        "v_mfma_f32_16x16x32_bf16 v[140:143], v[92:95], a[228:231], v[140:143]\n"
        "s_waitcnt lgkmcnt(0)\n"
        "s_barrier\n"
        "ds_read_b128 v[64:67], v51 offset:0\n"
        "ds_read_b128 v[68:71], v51 offset:1024\n"
        "ds_read_b128 v[72:75], v51 offset:2048\n"
        "ds_read_b128 v[76:79], v51 offset:3072\n"
        "ds_read_b128 v[80:83], v51 offset:4096\n"
        "ds_read_b128 v[84:87], v51 offset:5120\n"
        "s_waitcnt vmcnt(2)\n"
        "v_mfma_f32_16x16x32_bf16 v[128:131], v[40:43], v[224:227], v[128:131]\n"
        "v_mfma_f32_16x16x32_bf16 v[132:135], v[42:45], v[224:227], v[132:135]\n"
        "v_mfma_f32_16x16x32_bf16 v[136:139], v[40:43], v[228:231], v[136:139]\n"
        "v_mfma_f32_16x16x32_bf16 v[140:143], v[42:45], v[228:231], v[140:143]\n"
        "s_waitcnt lgkmcnt(5)\n"
        "v_mfma_f32_16x16x32_bf16 v[128:131], v[40:43], v[64:67], v[128:131]\n"
        "v_mfma_f32_16x16x32_bf16 v[132:135], v[42:45], v[64:67], v[132:135]\n"
        "s_waitcnt lgkmcnt(4)\n"
        "v_mfma_f32_16x16x32_bf16 v[136:139], v[40:43], v[68:71], v[136:139]\n"
        "v_mfma_f32_16x16x32_bf16 v[140:143], v[42:45], v[68:71], v[140:143]\n"
        "s_waitcnt lgkmcnt(3)\n"
        "v_mfma_f32_16x16x32_bf16 v[128:131], v[40:43], v[72:75], v[128:131]\n"
        "v_mfma_f32_16x16x32_bf16 v[132:135], v[42:45], v[72:75], v[132:135]\n"
        "s_waitcnt lgkmcnt(2)\n"
        "v_mfma_f32_16x16x32_bf16 v[136:139], v[40:43], v[76:79], v[136:139]\n"
        "v_mfma_f32_16x16x32_bf16 v[140:143], v[42:45], v[76:79], v[140:143]\n"
        "s_waitcnt lgkmcnt(1)\n"
        "v_mfma_f32_16x16x32_bf16 v[128:131], v[40:43], v[80:83], v[128:131]\n"
        "v_mfma_f32_16x16x32_bf16 v[132:135], v[42:45], v[80:83], v[132:135]\n"
        "s_waitcnt lgkmcnt(0)\n"
        "v_mfma_f32_16x16x32_bf16 v[136:139], v[40:43], v[84:87], v[136:139]\n"
        "v_mfma_f32_16x16x32_bf16 v[140:143], v[42:45], v[84:87], v[140:143]\n"
        "s_waitcnt vmcnt(0)\n"
        "v_max3_u32 v59, v208, v209, v210\n"
        "v_max3_u32 v59, v211, v212, v59\n"
        "v_max3_u32 v59, v213, v214, v59\n"
        "v_max_u32 v59, v215, v59\n"
        "v_cmp_eq_u32 vcc, -1, v59\n"
        "s_cbranch_vccnz Rlate_%=\n"
        "Clate_%=:\n"
        "v_mfma_f32_16x16x32_bf16 v[128:131], v[40:43], v[208:211], v[128:131]\n"
        "v_mfma_f32_16x16x32_bf16 v[132:135], v[42:45], v[208:211], v[132:135]\n"
        "v_mfma_f32_16x16x32_bf16 v[136:139], v[40:43], v[212:215], v[136:139]\n"
        "v_mfma_f32_16x16x32_bf16 v[140:143], v[42:45], v[212:215], v[140:143]\n"
        "v_mfma_f32_16x16x32_bf16 v[192:195], v[40:43], v[24:27], v[192:195]\n"
        "s_barrier\n"
        "v_mov_b32 v64, 0x3f803f80\n"
        "v_mov_b32 v65, 0x3f803f80\n"
        "v_mov_b32 v66, 0x3f803f80\n"
        "v_mov_b32 v67, 0x3f803f80\n"
        "s_nop 2\n"
        "v_cvt_pk_bf16_f32 v120, v128, v129\n"
        "v_cvt_pk_bf16_f32 v121, v130, v131\n"
        "v_cvt_pk_bf16_f32 v122, v132, v133\n"
        "v_cvt_pk_bf16_f32 v123, v134, v135\n"
        "v_cvt_pk_bf16_f32 v124, v136, v137\n"
        "v_cvt_pk_bf16_f32 v125, v138, v139\n"
        "v_cvt_pk_bf16_f32 v126, v140, v141\n"
        "v_cvt_pk_bf16_f32 v127, v142, v143\n"
        "v_cvt_pk_bf16_f32 v118, v192, v193\n"
        "v_cvt_pk_bf16_f32 v119, v194, v195\n"
        "v_permlane32_swap_b32 v120, v122\n"
        "v_permlane32_swap_b32 v121, v123\n"
        "v_permlane32_swap_b32 v124, v126\n"
        "v_permlane32_swap_b32 v125, v127\n"
        "v_min_u32 v118, -2, v118\n"
        "v_min_u32 v119, -2, v119\n"
        "v_permlane16_swap_b32 v120, v122\n"
        "v_permlane16_swap_b32 v121, v123\n"
        "v_permlane16_swap_b32 v124, v126\n"
        "v_permlane16_swap_b32 v125, v127\n"
        "v_add_u32 v62, 4096, %[vl16]\n"
        "global_load_dwordx4 v[224:227], v62, %[sws] offset:0 sc1\n"
        "global_load_dwordx4 v[228:231], v62, %[sws] offset:1024 sc1\n"
        "v_add_u32 v31, s32, v36\n"
        "global_store_dwordx2 v31, v[118:119], %[sws] sc1\n"
        "v_add_u32 v31, s31, %[vl16]\n"
        "v_min_u32 v120, -2, v120\n"
        "v_min_u32 v121, -2, v121\n"
        "v_min_u32 v122, -2, v122\n"
        "v_min_u32 v123, -2, v123\n"
        "v_min_u32 v124, -2, v124\n"
        "v_min_u32 v125, -2, v125\n"
        "v_min_u32 v126, -2, v126\n"
        "v_min_u32 v127, -2, v127\n"
        "v_add_u32 v58, 20480, v31\n"
        "global_store_dwordx4 v31, v[120:123], %[sws] sc1\n"
        "global_store_dwordx4 v58, v[124:127], %[sws] sc1\n"
        "v_mfma_f32_16x16x16_bf16 v[196:199], v[118:119], v[64:65], 0\n"
        "v_mfma_f32_16x16x16_bf16 v[200:203], v[118:119], v[118:119], 0\n"
        "v_mfma_f32_16x16x16_bf16 v[204:207], v[118:119], v[26:27], 0\n"
        "v_mfma_f32_16x16x32_bf16 v[128:131], v[120:123], v[64:67], 0\n"
        "v_mfma_f32_16x16x32_bf16 v[132:135], v[120:123], v[120:123], 0\n"
        "v_mfma_f32_16x16x32_bf16 v[136:139], v[120:123], v[12:15], 0\n"
        "v_mfma_f32_16x16x32_bf16 v[140:143], v[124:127], v[64:67], 0\n"
        "v_mfma_f32_16x16x32_bf16 v[144:147], v[124:127], v[124:127], 0\n"
        "v_mfma_f32_16x16x32_bf16 v[148:151], v[124:127], v[12:15], 0\n"
        "s_mov_b32 s60, 0xaaaaaaaa\n"
        "s_mov_b32 s61, 0xaaaaaaaa\n"
        "s_mov_b32 s62, 0xcccccccc\n"
        "s_mov_b32 s63, 0xcccccccc\n"
        "s_cmp_lt_u32 s49, 2\n"
        "s_cselect_b32 s64, 1.0, 0\n"
        "s_cselect_b32 s65, 0, 1.0\n"
        "v_cndmask_b32 v196, v196, v197, s[60:61]\n"
        "v_cndmask_b32 v198, v198, v199, s[60:61]\n"
        "v_cndmask_b32 v196, v196, v198, s[62:63]\n"
        "v_cndmask_b32 v200, v200, v201, s[60:61]\n"
        "v_cndmask_b32 v202, v202, v203, s[60:61]\n"
        "v_cndmask_b32 v200, v200, v202, s[62:63]\n"
        "v_cndmask_b32 v204, v204, v205, s[60:61]\n"
        "v_cndmask_b32 v206, v206, v207, s[60:61]\n"
        "v_cndmask_b32 v204, v204, v206, s[62:63]\n"
        "v_cndmask_b32 v128, v128, v129, s[60:61]\n"
        "v_cndmask_b32 v130, v130, v131, s[60:61]\n"
        "v_cndmask_b32 v160, v128, v130, s[62:63]\n"
        "v_cndmask_b32 v132, v132, v133, s[60:61]\n"
        "v_cndmask_b32 v134, v134, v135, s[60:61]\n"
        "v_cndmask_b32 v161, v132, v134, s[62:63]\n"
        "v_cndmask_b32 v136, v136, v137, s[60:61]\n"
        "v_cndmask_b32 v138, v138, v139, s[60:61]\n"
        "v_cndmask_b32 v162, v136, v138, s[62:63]\n"
        "v_fmac_f32 v160, s64, v196\n"
        "v_fmac_f32 v161, s64, v200\n"
        "v_fmac_f32 v162, s64, v204\n"
        "v_min_u32 v160, -2, v160\n"
        "v_min_u32 v161, -2, v161\n"
        "v_min_u32 v162, -2, v162\n"
        "v_mov_b32 v163, 0\n"
        "v_cndmask_b32 v140, v140, v141, s[60:61]\n"
        "v_cndmask_b32 v142, v142, v143, s[60:61]\n"
        "v_cndmask_b32 v164, v140, v142, s[62:63]\n"
        "v_cndmask_b32 v144, v144, v145, s[60:61]\n"
        "v_cndmask_b32 v146, v146, v147, s[60:61]\n"
        "v_cndmask_b32 v165, v144, v146, s[62:63]\n"
        "v_cndmask_b32 v148, v148, v149, s[60:61]\n"
        "v_cndmask_b32 v150, v150, v151, s[60:61]\n"
        "v_cndmask_b32 v166, v148, v150, s[62:63]\n"
        "v_fmac_f32 v164, s65, v196\n"
        "v_fmac_f32 v165, s65, v200\n"
        "v_fmac_f32 v166, s65, v204\n"
        "v_min_u32 v164, -2, v164\n"
        "v_min_u32 v165, -2, v165\n"
        "v_min_u32 v166, -2, v166\n"
        "v_mov_b32 v167, 0\n"
        "v_and_b32 v58, 0xf0, %[vl16]\n"
        "v_add_u32 v58, s33, v58\n"
        "s_mov_b32 exec_lo, 0x00f0000f\n"
        "s_mov_b32 exec_hi, 0xf0000f00\n"
        "global_store_dwordx4 v58, v[160:163], %[sws] sc1\n"
        "global_store_dwordx4 v58, v[164:167], %[sws] offset:256 sc1\n"
        "s_mov_b64 exec, -1\n"
        "s_waitcnt vmcnt(5)\n"
        "v_cmp_ne_u32 vcc, s56, v224\n"
        "v_cmp_ne_u32 s[82:83], s57, v225\n"
        "s_or_b64 vcc, vcc, s[82:83]\n"
        "v_cmp_ne_u32 s[82:83], s56, v226\n"
        "s_or_b64 vcc, vcc, s[82:83]\n"
        "v_cmp_ne_u32 s[82:83], s57, v227\n"
        "s_or_b64 vcc, vcc, s[82:83]\n"
        "v_cmp_ne_u32 s[82:83], s56, v228\n"
        "s_or_b64 vcc, vcc, s[82:83]\n"
        "v_cmp_ne_u32 s[82:83], s57, v229\n"
        "s_or_b64 vcc, vcc, s[82:83]\n"
        "v_cmp_ne_u32 s[82:83], s56, v230\n"
        "s_or_b64 vcc, vcc, s[82:83]\n"
        "v_cmp_ne_u32 s[82:83], s57, v231\n"
        "s_or_b64 vcc, vcc, s[82:83]\n"
        "s_cbranch_vccnz Rcf_%=\n"
        "Ccf_%=:\n"
        "s_nop 1\n"
        "s_branch END_%=\n"
        "Rpf0_%=:\n"
        "s_sub_u32 s81, s81, 1\n"
        "s_cbranch_scc1 Cpf0_%=\n"
        "s_sleep 1\n"
        "global_load_dwordx2 v[60:61], v32, %[sws] sc1\n"
        "s_waitcnt vmcnt(0)\n"
        "v_bfe_u32 v59, v33, 0, 4\n"
        "v_subrev_u32 v60, s56, v60\n"
        "v_sub_u32 v60, v60, v59\n"
        "v_cmp_lt_u32 vcc, 7, v60\n"
        "v_cmp_ne_u32 s[82:83], s57, v61\n"
        "s_or_b64 vcc, vcc, s[82:83]\n"
        "s_cbranch_vccnz Rpf0_%=\n"
        "s_branch Cpf0_%=\n"
        "Rpf1_%=:\n"
        "s_sub_u32 s81, s81, 1\n"
        "s_cbranch_scc1 Cpf1_%=\n"
        "s_sleep 1\n"
        "global_load_dwordx2 v[60:61], v32, %[sws] sc1\n"
        "s_waitcnt vmcnt(0)\n"
        "v_bfe_u32 v59, v33, 4, 4\n"
        "v_subrev_u32 v60, s56, v60\n"
        "v_sub_u32 v60, v60, v59\n"
        "v_cmp_lt_u32 vcc, 7, v60\n"
        "v_cmp_ne_u32 s[82:83], s57, v61\n"
        "s_or_b64 vcc, vcc, s[82:83]\n"
        "s_cbranch_vccnz Rpf1_%=\n"
        "s_branch Cpf1_%=\n"
        "Rpf4_%=:\n"
        "s_sub_u32 s81, s81, 1\n"
        "s_cbranch_scc1 Cpf4_%=\n"
        "s_sleep 1\n"
        "global_load_dwordx2 v[60:61], v32, %[sws] sc1\n"
        "s_waitcnt vmcnt(0)\n"
        "v_bfe_u32 v59, v33, 16, 4\n"
        "v_subrev_u32 v60, s56, v60\n"
        "v_sub_u32 v60, v60, v59\n"
        "v_cmp_lt_u32 vcc, 7, v60\n"
        "v_cmp_ne_u32 s[82:83], s57, v61\n"
        "s_or_b64 vcc, vcc, s[82:83]\n"
        "s_cbranch_vccnz Rpf4_%=\n"
        "s_branch Cpf4_%=\n"
        "Rxfc_%=:\n"
        "s_sub_u32 s81, s81, 1\n"
        "s_cbranch_scc1 Cxfc_%=\n"
        "s_sleep 1\n"
        "global_load_dwordx2 v[60:61], v34, %[sws] sc1\n"
        "s_waitcnt vmcnt(0)\n"
        "v_bfe_u32 v59, v33, 20, 4\n"
        "v_subrev_u32 v60, s56, v60\n"
        "v_sub_u32 v60, v60, v59\n"
        "v_cmp_lt_u32 vcc, 7, v60\n"
        "v_cmp_ne_u32 s[82:83], s57, v61\n"
        "s_or_b64 vcc, vcc, s[82:83]\n"
        "s_cbranch_vccnz Rxfc_%=\n"
        "s_branch Cxfc_%=\n"
        "Rxfd_%=:\n"
        "s_sub_u32 s81, s81, 1\n"
        "s_cbranch_scc1 Cxfd_%=\n"
        "s_sleep 1\n"
        "global_load_dwordx2 v[60:61], v34, %[sws] sc1\n"
        "s_waitcnt vmcnt(0)\n"
        "v_bfe_u32 v59, v33, 24, 4\n"
        "v_subrev_u32 v60, s56, v60\n"
        "v_sub_u32 v60, v60, v59\n"
        "v_cmp_lt_u32 vcc, 7, v60\n"
        "v_cmp_ne_u32 s[82:83], s57, v61\n"
        "s_or_b64 vcc, vcc, s[82:83]\n"
        "s_cbranch_vccnz Rxfd_%=\n"
        "s_branch Cxfd_%=\n"
        "Rlate_%=:\n"
        "s_sub_u32 s81, s81, 1\n"
        "s_cbranch_scc1 Clate_%=\n"
        "s_sleep 1\n"
        "v_add_u32 v31, s25, %[vl16]\n"
        "v_add_u32 v31, 16384, v31\n"
        "global_load_dwordx4 v[208:211], v31, %[sws] offset:0 sc1\n"
        "global_load_dwordx4 v[212:215], v31, %[sws] offset:1024 sc1\n"
        "s_waitcnt vmcnt(0)\n"
        "v_max3_u32 v59, v208, v209, v210\n"
        "v_max3_u32 v59, v211, v212, v59\n"
        "v_max3_u32 v59, v213, v214, v59\n"
        "v_max_u32 v59, v215, v59\n"
        "v_cmp_eq_u32 vcc, -1, v59\n"
        "s_cbranch_vccnz Rlate_%=\n"
        "s_branch Clate_%=\n"
        "Rcf_%=:\n"
        "s_sub_u32 s81, s81, 1\n"
        "s_cbranch_scc1 Ccf_%=\n"
        "s_sleep 1\n"
        "v_add_u32 v62, 4096, %[vl16]\n"
        "global_load_dwordx4 v[224:227], v62, %[sws] offset:0 sc1\n"
        "global_load_dwordx4 v[228:231], v62, %[sws] offset:1024 sc1\n"
        "s_waitcnt vmcnt(0)\n"
        "v_cmp_ne_u32 vcc, s56, v224\n"
        "v_cmp_ne_u32 s[82:83], s57, v225\n"
        "s_or_b64 vcc, vcc, s[82:83]\n"
        "v_cmp_ne_u32 s[82:83], s56, v226\n"
        "s_or_b64 vcc, vcc, s[82:83]\n"
        "v_cmp_ne_u32 s[82:83], s57, v227\n"
        "s_or_b64 vcc, vcc, s[82:83]\n"
        "v_cmp_ne_u32 s[82:83], s56, v228\n"
        "s_or_b64 vcc, vcc, s[82:83]\n"
        "v_cmp_ne_u32 s[82:83], s57, v229\n"
        "s_or_b64 vcc, vcc, s[82:83]\n"
        "v_cmp_ne_u32 s[82:83], s56, v230\n"
        "s_or_b64 vcc, vcc, s[82:83]\n"
        "v_cmp_ne_u32 s[82:83], s57, v231\n"
        "s_or_b64 vcc, vcc, s[82:83]\n"
        "s_cbranch_vccnz Rcf_%=\n"
        "s_branch Ccf_%=\n"
        "END_%=:\n"
        : "=&{v[112:127]}"(XO)
        : [vl16] "v"(l * 16),
          [sws] "s"(ws),
          [sxr] "s"(sxr),
          [swr] "s"(ARG(5)),
          "{s[56:57]}"(tag),
          "{s[24:39]}"(KP),
          "{s[40:43]}"(LB),
          "{s[44:47]}"(DP),
          "{s[48:55]}"(MP)
        : "scc", "vcc", "memory", "s59", "s60", "s61", "s62", "s63", "s64", "s65", "s81", "s82", "s83", "v12", "v13", "v14", "v15", "v16", "v17", "v18", "v19", "v20", "v21", "v22", "v23", "v24", "v25", "v26", "v27", "v28", "v29", "v30", "v31", "v32", "v33", "v34", "v35", "v36", "v37", "v38", "v39", "v40", "v41", "v42", "v43", "v44", "v45", "v46", "v47", "v48", "v49", "v50", "v51", "v52", "v53", "v54", "v55", "v56", "v57", "v58", "v59", "v60", "v61", "v62", "v63", "v64", "v65", "v66", "v67", "v68", "v69", "v70", "v71", "v72", "v73", "v74", "v75", "v76", "v77", "v78", "v79", "v80", "v81", "v82", "v83", "v84", "v85", "v86", "v87", "v88", "v89", "v90", "v91", "v92", "v93", "v94", "v95", "v96", "v97", "v98", "v99", "v100", "v101", "v102", "v103", "v104", "v105", "v106", "v107", "v108", "v109", "v110", "v111", "v128", "v129", "v130", "v131", "v132", "v133", "v134", "v135", "v136", "v137", "v138", "v139", "v140", "v141", "v142", "v143", "v144", "v145", "v146", "v147", "v148", "v149", "v150", "v151", "v152", "v153", "v154", "v155", "v156", "v157", "v158", "v159", "v160", "v161", "v162", "v163", "v164", "v165", "v166", "v167", "v168", "v169", "v170", "v171", "v172", "v173", "v174", "v175", "v176", "v177", "v178", "v179", "v180", "v181", "v182", "v183", "v184", "v185", "v186", "v187", "v188", "v189", "v190", "v191", "v192", "v193", "v194", "v195", "v196", "v197", "v198", "v199", "v200", "v201", "v202", "v203", "v204", "v205", "v206", "v207", "v208", "v209", "v210", "v211", "v212", "v213", "v214", "v215", "v216", "v217", "v218", "v219", "v220", "v221", "v222", "v223", "v224", "v225", "v226", "v227", "v228", "v229", "v230", "v231", "v232", "v233", "v234", "v235", "v236", "v237", "v238", "v239", "v240", "v241", "v242", "v243", "v244", "v245", "v246", "v247", "v248", "v249", "v250", "v251", "v252", "v253", "v254", "v255", "a0", "a1", "a2", "a3", "a4", "a5", "a6", "a7", "a8", "a9", "a10", "a11", "a12", "a13", "a14", "a15", "a16", "a17", "a18", "a19", "a20", "a21", "a22", "a23", "a24", "a25", "a26", "a27", "a28", "a29", "a30", "a31", "a32", "a33", "a34", "a35", "a36", "a37", "a38", "a39", "a40", "a41", "a42", "a43", "a44", "a45", "a46", "a47", "a48", "a49", "a50", "a51", "a52", "a53", "a54", "a55", "a56", "a57", "a58", "a59", "a60", "a61", "a62", "a63", "a64", "a65", "a66", "a67", "a68", "a69", "a70", "a71", "a72", "a73", "a74", "a75", "a76", "a77", "a78", "a79", "a80", "a81", "a82", "a83", "a84", "a85", "a86", "a87", "a88", "a89", "a90", "a91", "a92", "a93", "a94", "a95", "a96", "a97", "a98", "a99", "a100", "a101", "a102", "a103", "a104", "a105", "a106", "a107", "a108", "a109", "a110", "a111", "a112", "a113", "a114", "a115", "a116", "a117", "a118", "a119", "a120", "a121", "a122", "a123", "a124", "a125", "a126", "a127", "a128", "a129", "a130", "a131", "a132", "a133", "a134", "a135", "a136", "a137", "a138", "a139", "a140", "a141", "a142", "a143", "a144", "a145", "a146", "a147", "a148", "a149", "a150", "a151", "a152", "a153", "a154", "a155", "a156", "a157", "a158", "a159", "a160", "a161", "a162", "a163", "a164", "a165", "a166", "a167", "a168", "a169", "a170", "a171", "a172", "a173", "a174", "a175", "a176", "a177", "a178", "a179", "a180", "a181", "a182", "a183", "a184", "a185", "a186", "a187", "a188", "a189", "a190", "a191", "a192", "a193", "a194", "a195", "a196", "a197", "a198", "a199", "a200", "a201", "a202", "a203", "a204", "a205", "a206", "a207", "a208", "a209", "a210", "a211", "a212", "a213", "a214", "a215", "a216", "a217", "a218", "a219", "a220", "a221", "a222", "a223", "a224", "a225", "a226", "a227", "a228", "a229", "a230", "a231", "a232", "a233", "a234", "a235", "a236", "a237", "a238", "a239", "a240", "a241", "a242", "a243", "a244", "a245", "a246", "a247", "a248", "a249", "a250", "a251", "a252", "a253", "a254", "a255");
        ASTAMP_G2A;
    }
    LANE();
    STAMP(3);
    float wsum = 0;
    {
        unsigned wq[5];
        const unsigned* wrp = (const unsigned*)ARG(5);
#pragma unroll
        for (int i = 0; i < 5; ++i) wq[i] = wrp[i * 64 + l];
        if (f1 >= 0) { stage_w<20>(ARG(6) + f1 * 16, 1728, LRED, w, l); stage_w<20>(ARG(7) + f1 * 16, 1728, LRED + 20480, w, l); }
        if (b == 0 && tid == 0) *(u64*)ws = tag >> 4;
        KP += u32x16{AB_SZ, IB_SZ, IB_SZ, 0, 0, 0, 0, A_X3 - A_X1, A_X3 - A_X1, ST_SZ, 131072, 2048, 0, 0, 0, 0};
        KP[3] = KP[7] - A_X3 + A_X1; KP[4] = KP[8] - A_X3 + A_X1; KP[15] = A_IDX;
        DP += IB_SZ; abo += AB_SZ;
#pragma unroll
        for (int i = 0; i < 5; ++i) wsum += bflo(wq[i]) + bfhi(wq[i]);
        wsum = __uint_as_float(__builtin_amdgcn_readfirstlane(__float_as_uint(sum32(sum16(sumrow(wsum))))));
    }
    STAMP(4);
    f32x32 W0, W1, P1a, P1b, P2a, P2b;
    {
        LANE();
        asm volatile(
        "s_nop 4\n"
        "s_bfe_u32 s59, %[sjo], 262144\n"
        "s_mul_i32 s60, s59, 20480\n"
        "s_bfe_u32 s59, %[sjo], 262148\n"
        "s_mul_i32 s61, s59, 20480\n"
        "s_bfe_u32 s59, %[sjo], 262152\n"
        "s_mul_i32 s62, s59, 20480\n"
        "s_bfe_u32 s59, %[sjo], 262156\n"
        "s_mul_i32 s63, s59, 20480\n"
        "s_bfe_u32 s59, %[sjo], 262160\n"
        "s_mul_i32 s64, s59, 20480\n"
        "v_mov_b32 v16, %[vdr]\n"
        "v_xor_b32 v17, 64, %[vdr]\n"
        "v_xor_b32 v18, 0x80, %[vdr]\n"
        "v_xor_b32 v19, 0xc0, %[vdr]\n"
        "s_mov_b32 s81, 0x40000\n"
        "v_add_u32 v20, 20480, %[vl16]\n"
        "G1S_%=:\n"
        "s_mov_b64 s[82:83], %[sx]\n"
        "s_mov_b64 s[84:85], %[sst]\n"
        "global_load_dwordx4 v[224:227], %[vst], s[84:85] offset:-4096 sc1\n"
        "global_load_dwordx4 v[228:231], %[vst], s[84:85] offset:-2048 sc1\n"
        "global_load_dwordx4 v[232:235], %[vst], s[84:85] offset:0 sc1\n"
        "global_load_dwordx4 v[236:239], %[vst], s[84:85] offset:2048 sc1\n"
        "s_add_u32 s84, s84, 8192\n"
        "s_addc_u32 s85, s85, 0\n"
        "global_load_dwordx4 v[240:243], %[vst], s[84:85] offset:-4096 sc1\n"
        "global_load_dwordx4 v[244:247], %[vst], s[84:85] offset:-2048 sc1\n"
        "global_load_dwordx4 v[248:251], %[vst], s[84:85] offset:0 sc1\n"
        "global_load_dwordx4 v[252:255], %[vst], s[84:85] offset:2048 sc1\n"
        "global_load_dwordx4 v[96:99], %[vl16], s[82:83] offset:-4096 sc1\n"
        "global_load_dwordx4 v[100:103], v20, s[82:83] offset:-4096 sc1\n"
        "global_load_dwordx4 v[104:107], %[vl16], s[82:83] offset:-3072 sc1\n"
        "global_load_dwordx4 v[108:111], v20, s[82:83] offset:-3072 sc1\n"
        "s_waitcnt vmcnt(4)\n"
        "v_max3_u32 v27, v224, v225, v226\n"
        "v_max3_u32 v27, v228, v229, v27\n"
        "v_max_u32 v27, v230, v27\n"
        "v_max3_u32 v27, v232, v233, v27\n"
        "v_max_u32 v27, v234, v27\n"
        "v_max3_u32 v27, v236, v237, v27\n"
        "v_max_u32 v27, v238, v27\n"
        "v_max3_u32 v27, v240, v241, v27\n"
        "v_max_u32 v27, v242, v27\n"
        "v_max3_u32 v27, v244, v245, v27\n"
        "v_max_u32 v27, v246, v27\n"
        "v_max3_u32 v27, v248, v249, v27\n"
        "v_max_u32 v27, v250, v27\n"
        "v_max3_u32 v27, v252, v253, v27\n"
        "v_max_u32 v27, v254, v27\n"
        "v_cmp_eq_u32 vcc, -1, v27\n"
        "s_cbranch_vccnz G1R_%=\n"
        "G1P_%=:\n"
        "global_load_dwordx4 v[112:115], %[vl16], s[82:83] offset:-2048 sc1\n"
        "global_load_dwordx4 v[116:119], v20, s[82:83] offset:-2048 sc1\n"
        "global_load_dwordx4 v[120:123], %[vl16], s[82:83] offset:-1024 sc1\n"
        "global_load_dwordx4 v[124:127], v20, s[82:83] offset:-1024 sc1\n"
        "global_load_dwordx4 v[128:131], %[vl16], s[82:83] offset:0 sc1\n"
        "global_load_dwordx4 v[132:135], v20, s[82:83] offset:0 sc1\n"
        "v_add_u32 v22, s60, v16\n"
        "ds_read_b128 v[44:47], v22 offset:0\n"
        "v_add_u32 v23, s61, v16\n"
        "ds_read_b128 v[48:51], v23 offset:0\n"
        "v_add_u32 v22, s62, v16\n"
        "ds_read_b128 v[52:55], v22 offset:0\n"
        "v_add_u32 v23, s60, v17\n"
        "ds_read_b128 v[56:59], v23 offset:0\n"
        "v_mov_b32 v27, 0\n"
        "global_load_dwordx4 v[136:139], %[vl16], s[82:83] offset:1024 sc1\n"
        "global_load_dwordx4 v[140:143], v20, s[82:83] offset:1024 sc1\n"
        "s_waitcnt vmcnt(10)\n"
        "v_max3_u32 v27, v96, v97, v27\n"
        "v_max3_u32 v27, v98, v99, v27\n"
        "v_max3_u32 v27, v100, v101, v27\n"
        "v_max3_u32 v27, v102, v103, v27\n"
        "v_add_u32 v22, s61, v17\n"
        "ds_read_b128 v[60:63], v22 offset:0\n"
        "s_waitcnt lgkmcnt(4)\n"
        "v_mfma_f32_16x16x32_bf16 v[64:67], v[96:99], v[44:47], 0\n"
        "v_add_f32 v224, v224, v228\n"
        "v_add_f32 v225, v225, v229\n"
        "v_add_f32 v226, v226, v230\n"
        "v_mfma_f32_16x16x32_bf16 v[68:71], v[100:103], v[44:47], 0\n"
        "v_add_f32 v224, v224, v232\n"
        "v_add_f32 v225, v225, v233\n"
        "v_add_f32 v226, v226, v234\n"
        "v_add_u32 v23, s62, v17\n"
        "ds_read_b128 v[44:47], v23 offset:0\n"
        "s_waitcnt lgkmcnt(4)\n"
        "v_mfma_f32_16x16x32_bf16 v[72:75], v[96:99], v[48:51], 0\n"
        "v_add_f32 v224, v224, v236\n"
        "v_add_f32 v225, v225, v237\n"
        "v_add_f32 v226, v226, v238\n"
        "v_mfma_f32_16x16x32_bf16 v[76:79], v[100:103], v[48:51], 0\n"
        "v_add_f32 v224, v224, v240\n"
        "v_add_f32 v225, v225, v241\n"
        "v_add_f32 v226, v226, v242\n"
        "v_add_u32 v22, s60, v18\n"
        "ds_read_b128 v[48:51], v22 offset:0\n"
        "s_waitcnt lgkmcnt(4)\n"
        "v_mfma_f32_16x16x32_bf16 v[80:83], v[96:99], v[52:55], 0\n"
        "v_add_f32 v224, v224, v244\n"
        "v_add_f32 v225, v225, v245\n"
        "v_add_f32 v226, v226, v246\n"
        "v_mfma_f32_16x16x32_bf16 v[84:87], v[100:103], v[52:55], 0\n"
        "v_add_f32 v224, v224, v248\n"
        "v_add_f32 v225, v225, v249\n"
        "v_add_f32 v226, v226, v250\n"
        "global_load_dwordx4 v[144:147], %[vl16], s[82:83] offset:2048 sc1\n"
        "global_load_dwordx4 v[148:151], v20, s[82:83] offset:2048 sc1\n"
        "s_waitcnt vmcnt(10)\n"
        "v_max3_u32 v27, v104, v105, v27\n"
        "v_max3_u32 v27, v106, v107, v27\n"
        "v_max3_u32 v27, v108, v109, v27\n"
        "v_max3_u32 v27, v110, v111, v27\n"
        "v_add_u32 v23, s61, v18\n"
        "ds_read_b128 v[52:55], v23 offset:0\n"
        "s_waitcnt lgkmcnt(4)\n"
        "v_mfma_f32_16x16x32_bf16 v[64:67], v[104:107], v[56:59], v[64:67]\n"
        "v_add_f32 v224, v224, v252\n"
        "v_add_f32 v225, v225, v253\n"
        "v_add_f32 v226, v226, v254\n"
        "v_mfma_f32_16x16x32_bf16 v[68:71], v[108:111], v[56:59], v[68:71]\n"
        "v_mov_b32 v227, v224\n"
        "s_nop 1\n"
        "v_permlane32_swap_b32 v224, v227\n"
        "s_nop 0\n"
        "v_add_f32 v224, v224, v227\n"
        "v_add_u32 v22, s62, v18\n"
        "ds_read_b128 v[56:59], v22 offset:0\n"
        "s_waitcnt lgkmcnt(4)\n"
        "v_mfma_f32_16x16x32_bf16 v[72:75], v[104:107], v[60:63], v[72:75]\n"
        "v_mov_b32 v227, v225\n"
        "s_nop 1\n"
        "v_permlane32_swap_b32 v225, v227\n"
        "s_nop 0\n"
        "v_add_f32 v225, v225, v227\n"
        "v_mfma_f32_16x16x32_bf16 v[76:79], v[108:111], v[60:63], v[76:79]\n"
        "v_mov_b32 v227, v226\n"
        "s_nop 1\n"
        "v_permlane32_swap_b32 v226, v227\n"
        "s_nop 0\n"
        "v_add_f32 v226, v226, v227\n"
        "v_add_u32 v23, s60, v19\n"
        "ds_read_b128 v[60:63], v23 offset:0\n"
        "s_waitcnt lgkmcnt(4)\n"
        "v_mfma_f32_16x16x32_bf16 v[80:83], v[104:107], v[44:47], v[80:83]\n"
        "v_mul_f32 v228, 0x3acccccd, v224\n"
        "v_mul_f32 v229, 0x3acccccd, v225\n"
        "v_mfma_f32_16x16x32_bf16 v[84:87], v[108:111], v[44:47], v[84:87]\n"
        "v_fma_f32 v229, -v228, v228, v229\n"
        "v_max_f32 v229, 0, v229\n"
        "global_load_dwordx4 v[152:155], %[vl16], s[82:83] offset:3072 sc1\n"
        "global_load_dwordx4 v[156:159], v20, s[82:83] offset:3072 sc1\n"
        "s_waitcnt vmcnt(10)\n"
        "v_max3_u32 v27, v112, v113, v27\n"
        "v_max3_u32 v27, v114, v115, v27\n"
        "v_max3_u32 v27, v116, v117, v27\n"
        "v_max3_u32 v27, v118, v119, v27\n"
        "v_add_u32 v22, s61, v19\n"
        "ds_read_b128 v[44:47], v22 offset:0\n"
        "s_waitcnt lgkmcnt(4)\n"
        "v_mfma_f32_16x16x32_bf16 v[64:67], v[112:115], v[48:51], v[64:67]\n"
        "v_cvt_pk_bf16_f32 v230, v228, v228\n"
        "v_lshlrev_b32 v230, 16, v230\n"
        "v_mfma_f32_16x16x32_bf16 v[68:71], v[116:119], v[48:51], v[68:71]\n"
        "v_cvt_pk_bf16_f32 v227, v229, v229\n"
        "v_lshlrev_b32 v227, 16, v227\n"
        "v_add_u32 v23, s62, v19\n"
        "ds_read_b128 v[48:51], v23 offset:0\n"
        "s_waitcnt lgkmcnt(4)\n"
        "v_mfma_f32_16x16x32_bf16 v[72:75], v[112:115], v[52:55], v[72:75]\n"
        "v_add_f32 v227, 0x3727c5ac, v227\n"
        "v_mfma_f32_16x16x32_bf16 v[76:79], v[116:119], v[52:55], v[76:79]\n"
        "v_cvt_pk_bf16_f32 v227, v227, v227\n"
        "v_lshlrev_b32 v227, 16, v227\n"
        "v_add_u32 v22, s60, v16\n"
        "ds_read_b128 v[52:55], v22 offset:256\n"
        "s_waitcnt lgkmcnt(4)\n"
        "v_mfma_f32_16x16x32_bf16 v[80:83], v[112:115], v[56:59], v[80:83]\n"
        "v_sqrt_f32 v227, v227\n"
        "s_nop 0\n"
        "v_mfma_f32_16x16x32_bf16 v[84:87], v[116:119], v[56:59], v[84:87]\n"
        "v_cvt_pk_bf16_f32 v227, v227, v227\n"
        "v_lshlrev_b32 v227, 16, v227\n"
        "s_add_u32 s82, s82, 8192\n"
        "s_addc_u32 s83, s83, 0\n"
        "global_load_dwordx4 v[160:163], %[vl16], s[82:83] offset:-4096 sc1\n"
        "global_load_dwordx4 v[164:167], v20, s[82:83] offset:-4096 sc1\n"
        "s_waitcnt vmcnt(10)\n"
        "v_max3_u32 v27, v120, v121, v27\n"
        "v_max3_u32 v27, v122, v123, v27\n"
        "v_max3_u32 v27, v124, v125, v27\n"
        "v_max3_u32 v27, v126, v127, v27\n"
        "v_add_u32 v23, s61, v16\n"
        "ds_read_b128 v[56:59], v23 offset:256\n"
        "s_waitcnt lgkmcnt(4)\n"
        "v_mfma_f32_16x16x32_bf16 v[64:67], v[120:123], v[60:63], v[64:67]\n"
        "v_rcp_f32 v231, v227\n"
        "s_nop 0\n"
        "v_mfma_f32_16x16x32_bf16 v[68:71], v[124:127], v[60:63], v[68:71]\n"
        "v_fma_f32 v232, -v230, %[swsum], v226\n"
        "v_mul_f32 v232, v232, v231\n"
        "v_add_u32 v22, s62, v16\n"
        "ds_read_b128 v[60:63], v22 offset:256\n"
        "s_waitcnt lgkmcnt(4)\n"
        "v_mfma_f32_16x16x32_bf16 v[72:75], v[120:123], v[44:47], v[72:75]\n"
        "v_cvt_pk_bf16_f32 v232, v232, v232\n"
        "v_lshlrev_b32 v232, 16, v232\n"
        "v_ashrrev_i32 v227, 31, v232\n"
        "v_cmp_o_f32 s[84:85], v232, v232\n"
        "v_mfma_f32_16x16x32_bf16 v[76:79], v[124:127], v[44:47], v[76:79]\n"
        "v_or_b32 v227, 0x80000000, v227\n"
        "v_xor_b32 v232, v232, v227\n"
        "v_and_b32 v232, 0xffff0000, v232\n"
        "v_add_u32 v23, s60, v17\n"
        "ds_read_b128 v[44:47], v23 offset:256\n"
        "s_waitcnt lgkmcnt(4)\n"
        "v_mfma_f32_16x16x32_bf16 v[80:83], v[120:123], v[48:51], v[80:83]\n"
        "v_cndmask_b32 v232, 0, v232, s[84:85]\n"
        "v_lshrrev_b32 v227, 4, %[vl16]\n"
        "v_sub_u32 v227, %[srowc], v227\n"
        "v_or_b32 v232, v232, v227\n"
        "v_mfma_f32_16x16x32_bf16 v[84:87], v[124:127], v[48:51], v[84:87]\n"
        "v_add_f32 v233, 1.0, v231\n"
        "v_sub_f32 v227, v228, v230\n"
        "global_load_dwordx4 v[168:171], %[vl16], s[82:83] offset:-3072 sc1\n"
        "global_load_dwordx4 v[172:175], v20, s[82:83] offset:-3072 sc1\n"
        "s_waitcnt vmcnt(10)\n"
        "v_max3_u32 v27, v128, v129, v27\n"
        "v_max3_u32 v27, v130, v131, v27\n"
        "v_max3_u32 v27, v132, v133, v27\n"
        "v_max3_u32 v27, v134, v135, v27\n"
        "v_add_u32 v22, s61, v17\n"
        "ds_read_b128 v[48:51], v22 offset:256\n"
        "s_waitcnt lgkmcnt(4)\n"
        "v_mfma_f32_16x16x32_bf16 v[64:67], v[128:131], v[52:55], v[64:67]\n"
        "v_fmac_f32 v228, v227, v231\n"
        "v_mul_f32 v229, v229, v233\n"
        "v_mul_f32 v229, v229, v233\n"
        "v_mfma_f32_16x16x32_bf16 v[68:71], v[132:135], v[52:55], v[68:71]\n"
        "v_cvt_pk_bf16_f32 v234, v228, v228\n"
        "v_lshlrev_b32 v234, 16, v234\n"
        "v_add_u32 v23, s62, v17\n"
        "ds_read_b128 v[52:55], v23 offset:256\n"
        "s_waitcnt lgkmcnt(4)\n"
        "v_mfma_f32_16x16x32_bf16 v[72:75], v[128:131], v[56:59], v[72:75]\n"
        "v_cvt_pk_bf16_f32 v227, v229, v229\n"
        "v_lshlrev_b32 v227, 16, v227\n"
        "v_mfma_f32_16x16x32_bf16 v[76:79], v[132:135], v[56:59], v[76:79]\n"
        "v_add_f32 v227, 0x3727c5ac, v227\n"
        "v_add_u32 v22, s60, v18\n"
        "ds_read_b128 v[56:59], v22 offset:256\n"
        "s_waitcnt lgkmcnt(4)\n"
        "v_mfma_f32_16x16x32_bf16 v[80:83], v[128:131], v[60:63], v[80:83]\n"
        "v_cvt_pk_bf16_f32 v227, v227, v227\n"
        "v_lshlrev_b32 v227, 16, v227\n"
        "v_mfma_f32_16x16x32_bf16 v[84:87], v[132:135], v[60:63], v[84:87]\n"
        "v_sqrt_f32 v227, v227\n"
        "s_nop 0\n"
        "global_load_dwordx4 v[176:179], %[vl16], s[82:83] offset:-2048 sc1\n"
        "global_load_dwordx4 v[180:183], v20, s[82:83] offset:-2048 sc1\n"
        "s_waitcnt vmcnt(10)\n"
        "v_max3_u32 v27, v136, v137, v27\n"
        "v_max3_u32 v27, v138, v139, v27\n"
        "v_max3_u32 v27, v140, v141, v27\n"
        "v_max3_u32 v27, v142, v143, v27\n"
        "v_add_u32 v23, s61, v18\n"
        "ds_read_b128 v[60:63], v23 offset:256\n"
        "s_waitcnt lgkmcnt(4)\n"
        "v_mfma_f32_16x16x32_bf16 v[64:67], v[136:139], v[44:47], v[64:67]\n"
        "v_cvt_pk_bf16_f32 v227, v227, v227\n"
        "v_lshlrev_b32 v227, 16, v227\n"
        "v_mfma_f32_16x16x32_bf16 v[68:71], v[140:143], v[44:47], v[68:71]\n"
        "v_rcp_f32 v235, v227\n"
        "s_nop 0\n"
        "v_add_u32 v22, s62, v18\n"
        "ds_read_b128 v[44:47], v22 offset:256\n"
        "s_waitcnt lgkmcnt(4)\n"
        "v_mfma_f32_16x16x32_bf16 v[72:75], v[136:139], v[48:51], v[72:75]\n"
        "v_mul_f32 v228, -v230, v231\n"
        "v_sub_f32 v227, v228, v234\n"
        "v_rcp_f32 v229, v233\n"
        "v_mfma_f32_16x16x32_bf16 v[76:79], v[140:143], v[48:51], v[76:79]\n"
        "ds_write_b32 %[vtw], v233 offset:512\n"
        "ds_write_b32 %[vtw], v228 offset:1024\n"
        "v_mul_f32 v229, v227, v229\n"
        "v_add_u32 v23, s60, v19\n"
        "ds_read_b128 v[48:51], v23 offset:256\n"
        "s_waitcnt lgkmcnt(6)\n"
        "v_mfma_f32_16x16x32_bf16 v[80:83], v[136:139], v[52:55], v[80:83]\n"
        "v_mul_f32 v227, v227, v235\n"
        "v_mul_f32 v226, v233, v235\n"
        "v_cvt_pk_bf16_f32 v234, v229, v229\n"
        "v_mfma_f32_16x16x32_bf16 v[84:87], v[140:143], v[52:55], v[84:87]\n"
        "v_and_b32 v228, 0xffff0000, v234\n"
        "v_sub_f32 v229, v229, v228\n"
        "v_cvt_pk_bf16_f32 v235, v229, v229\n"
        "global_load_dwordx4 v[184:187], %[vl16], s[82:83] offset:-1024 sc1\n"
        "global_load_dwordx4 v[188:191], v20, s[82:83] offset:-1024 sc1\n"
        "s_waitcnt vmcnt(10)\n"
        "v_max3_u32 v27, v144, v145, v27\n"
        "v_max3_u32 v27, v146, v147, v27\n"
        "v_max3_u32 v27, v148, v149, v27\n"
        "v_max3_u32 v27, v150, v151, v27\n"
        "v_add_u32 v22, s61, v19\n"
        "ds_read_b128 v[52:55], v22 offset:256\n"
        "s_waitcnt lgkmcnt(6)\n"
        "v_mfma_f32_16x16x32_bf16 v[64:67], v[144:147], v[56:59], v[64:67]\n"
        "v_mul_f32 v233, %[ssgn], v226\n"
        "v_lshl_add_u32 v228, %[vtw], 1, %[smh]\n"
        "v_mfma_f32_16x16x32_bf16 v[68:71], v[148:151], v[56:59], v[68:71]\n"
        "ds_write_b32 %[vtw], v233\n"
        "ds_write_b64 v228, v[234:235]\n"
        "v_add_u32 v23, s62, v19\n"
        "ds_read_b128 v[56:59], v23 offset:256\n"
        "s_waitcnt lgkmcnt(8)\n"
        "v_mfma_f32_16x16x32_bf16 v[72:75], v[144:147], v[60:63], v[72:75]\n"
        "v_min_u32 v224, -2, v230\n"
        "v_min_u32 v225, -2, v231\n"
        "v_min_u32 v226, -2, v226\n"
        "v_min_u32 v227, -2, v227\n"
        "v_mfma_f32_16x16x32_bf16 v[76:79], v[148:151], v[60:63], v[76:79]\n"
        "s_cmp_lg_u32 %[sbep], 0\n"
        "s_cbranch_scc1 G1N_%=\n"
        "s_mov_b32 exec_hi, 0\n"
        "v_lshrrev_b32 v228, 2, %[vl16]\n"
        "global_store_dword v228, v232, %[srb] sc1\n"
        "global_store_dwordx4 %[vl16], v[224:227], %[sms] sc1\n"
        "s_mov_b64 exec, -1\n"
        "s_nop 1\n"
        "G1N_%=:\n"
        "v_add_u32 v22, s60, v16\n"
        "ds_read_b128 v[60:63], v22 offset:512\n"
        "s_waitcnt lgkmcnt(8)\n"
        "v_mfma_f32_16x16x32_bf16 v[80:83], v[144:147], v[44:47], v[80:83]\n"
        "v_mfma_f32_16x16x32_bf16 v[84:87], v[148:151], v[44:47], v[84:87]\n"
        "global_load_dwordx4 v[192:195], %[vl16], s[82:83] offset:0 sc1\n"
        "global_load_dwordx4 v[196:199], v20, s[82:83] offset:0 sc1\n"
        "s_waitcnt vmcnt(10)\n"
        "v_max3_u32 v27, v152, v153, v27\n"
        "v_max3_u32 v27, v154, v155, v27\n"
        "v_max3_u32 v27, v156, v157, v27\n"
        "v_max3_u32 v27, v158, v159, v27\n"
        "v_add_u32 v23, s61, v16\n"
        "ds_read_b128 v[44:47], v23 offset:512\n"
        "s_waitcnt lgkmcnt(6)\n"
        "v_mfma_f32_16x16x32_bf16 v[64:67], v[152:155], v[48:51], v[64:67]\n"
        "v_mfma_f32_16x16x32_bf16 v[68:71], v[156:159], v[48:51], v[68:71]\n"
        "v_add_u32 v22, s62, v16\n"
        "ds_read_b128 v[48:51], v22 offset:512\n"
        "s_waitcnt lgkmcnt(6)\n"
        "v_mfma_f32_16x16x32_bf16 v[72:75], v[152:155], v[52:55], v[72:75]\n"
        "v_mfma_f32_16x16x32_bf16 v[76:79], v[156:159], v[52:55], v[76:79]\n"
        "v_add_u32 v23, s60, v17\n"
        "ds_read_b128 v[52:55], v23 offset:512\n"
        "s_waitcnt lgkmcnt(4)\n"
        "v_mfma_f32_16x16x32_bf16 v[80:83], v[152:155], v[56:59], v[80:83]\n"
        "v_mfma_f32_16x16x32_bf16 v[84:87], v[156:159], v[56:59], v[84:87]\n"
        "global_load_dwordx4 v[200:203], %[vl16], s[82:83] offset:1024 sc1\n"
        "global_load_dwordx4 v[204:207], v20, s[82:83] offset:1024 sc1\n"
        "s_waitcnt vmcnt(10)\n"
        "v_max3_u32 v27, v160, v161, v27\n"
        "v_max3_u32 v27, v162, v163, v27\n"
        "v_max3_u32 v27, v164, v165, v27\n"
        "v_max3_u32 v27, v166, v167, v27\n"
        "v_add_u32 v22, s61, v17\n"
        "ds_read_b128 v[56:59], v22 offset:512\n"
        "s_waitcnt lgkmcnt(4)\n"
        "v_mfma_f32_16x16x32_bf16 v[64:67], v[160:163], v[60:63], v[64:67]\n"
        "v_mfma_f32_16x16x32_bf16 v[68:71], v[164:167], v[60:63], v[68:71]\n"
        "v_add_u32 v23, s62, v17\n"
        "ds_read_b128 v[60:63], v23 offset:512\n"
        "s_waitcnt lgkmcnt(4)\n"
        "v_mfma_f32_16x16x32_bf16 v[72:75], v[160:163], v[44:47], v[72:75]\n"
        "v_mfma_f32_16x16x32_bf16 v[76:79], v[164:167], v[44:47], v[76:79]\n"
        "v_add_u32 v22, s60, v18\n"
        "ds_read_b128 v[44:47], v22 offset:512\n"
        "s_waitcnt lgkmcnt(4)\n"
        "v_mfma_f32_16x16x32_bf16 v[80:83], v[160:163], v[48:51], v[80:83]\n"
        "v_mfma_f32_16x16x32_bf16 v[84:87], v[164:167], v[48:51], v[84:87]\n"
        "global_load_dwordx4 v[208:211], %[vl16], s[82:83] offset:2048 sc1\n"
        "global_load_dwordx4 v[212:215], v20, s[82:83] offset:2048 sc1\n"
        "s_waitcnt vmcnt(10)\n"
        "v_max3_u32 v27, v168, v169, v27\n"
        "v_max3_u32 v27, v170, v171, v27\n"
        "v_max3_u32 v27, v172, v173, v27\n"
        "v_max3_u32 v27, v174, v175, v27\n"
        "v_add_u32 v23, s61, v18\n"
        "ds_read_b128 v[48:51], v23 offset:512\n"
        "s_waitcnt lgkmcnt(4)\n"
        "v_mfma_f32_16x16x32_bf16 v[64:67], v[168:171], v[52:55], v[64:67]\n"
        "v_mfma_f32_16x16x32_bf16 v[68:71], v[172:175], v[52:55], v[68:71]\n"
        "v_add_u32 v22, s62, v18\n"
        "ds_read_b128 v[52:55], v22 offset:512\n"
        "s_waitcnt lgkmcnt(4)\n"
        "v_mfma_f32_16x16x32_bf16 v[72:75], v[168:171], v[56:59], v[72:75]\n"
        "v_mfma_f32_16x16x32_bf16 v[76:79], v[172:175], v[56:59], v[76:79]\n"
        "v_add_u32 v23, s60, v19\n"
        "ds_read_b128 v[56:59], v23 offset:512\n"
        "s_waitcnt lgkmcnt(4)\n"
        "v_mfma_f32_16x16x32_bf16 v[80:83], v[168:171], v[60:63], v[80:83]\n"
        "v_mfma_f32_16x16x32_bf16 v[84:87], v[172:175], v[60:63], v[84:87]\n"
        "global_load_dwordx4 v[216:219], %[vl16], s[82:83] offset:3072 sc1\n"
        "global_load_dwordx4 v[220:223], v20, s[82:83] offset:3072 sc1\n"
        "s_waitcnt vmcnt(10)\n"
        "v_max3_u32 v27, v176, v177, v27\n"
        "v_max3_u32 v27, v178, v179, v27\n"
        "v_max3_u32 v27, v180, v181, v27\n"
        "v_max3_u32 v27, v182, v183, v27\n"
        "v_add_u32 v22, s61, v19\n"
        "ds_read_b128 v[60:63], v22 offset:512\n"
        "s_waitcnt lgkmcnt(4)\n"
        "v_mfma_f32_16x16x32_bf16 v[64:67], v[176:179], v[44:47], v[64:67]\n"
        "v_mfma_f32_16x16x32_bf16 v[68:71], v[180:183], v[44:47], v[68:71]\n"
        "v_add_u32 v23, s62, v19\n"
        "ds_read_b128 v[44:47], v23 offset:512\n"
        "s_waitcnt lgkmcnt(4)\n"
        "v_mfma_f32_16x16x32_bf16 v[72:75], v[176:179], v[48:51], v[72:75]\n"
        "v_mfma_f32_16x16x32_bf16 v[76:79], v[180:183], v[48:51], v[76:79]\n"
        "v_add_u32 v22, s60, v16\n"
        "ds_read_b128 v[48:51], v22 offset:768\n"
        "s_waitcnt lgkmcnt(4)\n"
        "v_mfma_f32_16x16x32_bf16 v[80:83], v[176:179], v[52:55], v[80:83]\n"
        "v_mfma_f32_16x16x32_bf16 v[84:87], v[180:183], v[52:55], v[84:87]\n"
        "s_add_u32 s82, s82, 8192\n"
        "s_addc_u32 s83, s83, 0\n"
        "global_load_dwordx4 v[224:227], %[vl16], s[82:83] offset:-4096 sc1\n"
        "global_load_dwordx4 v[228:231], v20, s[82:83] offset:-4096 sc1\n"
        "s_waitcnt vmcnt(10)\n"
        "v_max3_u32 v27, v184, v185, v27\n"
        "v_max3_u32 v27, v186, v187, v27\n"
        "v_max3_u32 v27, v188, v189, v27\n"
        "v_max3_u32 v27, v190, v191, v27\n"
        "v_add_u32 v23, s61, v16\n"
        "ds_read_b128 v[52:55], v23 offset:768\n"
        "s_waitcnt lgkmcnt(4)\n"
        "v_mfma_f32_16x16x32_bf16 v[64:67], v[184:187], v[56:59], v[64:67]\n"
        "v_mfma_f32_16x16x32_bf16 v[68:71], v[188:191], v[56:59], v[68:71]\n"
        "v_add_u32 v22, s62, v16\n"
        "ds_read_b128 v[56:59], v22 offset:768\n"
        "s_waitcnt lgkmcnt(4)\n"
        "v_mfma_f32_16x16x32_bf16 v[72:75], v[184:187], v[60:63], v[72:75]\n"
        "v_mfma_f32_16x16x32_bf16 v[76:79], v[188:191], v[60:63], v[76:79]\n"
        "v_add_u32 v23, s60, v17\n"
        "ds_read_b128 v[60:63], v23 offset:768\n"
        "s_waitcnt lgkmcnt(4)\n"
        "v_mfma_f32_16x16x32_bf16 v[80:83], v[184:187], v[44:47], v[80:83]\n"
        "v_mfma_f32_16x16x32_bf16 v[84:87], v[188:191], v[44:47], v[84:87]\n"
        "global_load_dwordx4 v[232:235], %[vl16], s[82:83] offset:-3072 sc1\n"
        "global_load_dwordx4 v[236:239], v20, s[82:83] offset:-3072 sc1\n"
        "s_waitcnt vmcnt(10)\n"
        "v_max3_u32 v27, v192, v193, v27\n"
        "v_max3_u32 v27, v194, v195, v27\n"
        "v_max3_u32 v27, v196, v197, v27\n"
        "v_max3_u32 v27, v198, v199, v27\n"
        "v_add_u32 v22, s61, v17\n"
        "ds_read_b128 v[44:47], v22 offset:768\n"
        "s_waitcnt lgkmcnt(4)\n"
        "v_mfma_f32_16x16x32_bf16 v[64:67], v[192:195], v[48:51], v[64:67]\n"
        "v_mfma_f32_16x16x32_bf16 v[68:71], v[196:199], v[48:51], v[68:71]\n"
        "v_add_u32 v23, s62, v17\n"
        "ds_read_b128 v[48:51], v23 offset:768\n"
        "s_waitcnt lgkmcnt(4)\n"
        "v_mfma_f32_16x16x32_bf16 v[72:75], v[192:195], v[52:55], v[72:75]\n"
        "v_mfma_f32_16x16x32_bf16 v[76:79], v[196:199], v[52:55], v[76:79]\n"
        "v_add_u32 v22, s60, v18\n"
        "ds_read_b128 v[52:55], v22 offset:768\n"
        "s_waitcnt lgkmcnt(4)\n"
        "v_mfma_f32_16x16x32_bf16 v[80:83], v[192:195], v[56:59], v[80:83]\n"
        "v_mfma_f32_16x16x32_bf16 v[84:87], v[196:199], v[56:59], v[84:87]\n"
        "global_load_dwordx4 v[240:243], %[vl16], s[82:83] offset:-2048 sc1\n"
        "global_load_dwordx4 v[244:247], v20, s[82:83] offset:-2048 sc1\n"
        "s_waitcnt vmcnt(10)\n"
        "v_max3_u32 v27, v200, v201, v27\n"
        "v_max3_u32 v27, v202, v203, v27\n"
        "v_max3_u32 v27, v204, v205, v27\n"
        "v_max3_u32 v27, v206, v207, v27\n"
        "v_add_u32 v23, s61, v18\n"
        "ds_read_b128 v[56:59], v23 offset:768\n"
        "s_waitcnt lgkmcnt(4)\n"
        "v_mfma_f32_16x16x32_bf16 v[64:67], v[200:203], v[60:63], v[64:67]\n"
        "v_mfma_f32_16x16x32_bf16 v[68:71], v[204:207], v[60:63], v[68:71]\n"
        "v_add_u32 v22, s62, v18\n"
        "ds_read_b128 v[60:63], v22 offset:768\n"
        "s_waitcnt lgkmcnt(4)\n"
        "v_mfma_f32_16x16x32_bf16 v[72:75], v[200:203], v[44:47], v[72:75]\n"
        "v_mfma_f32_16x16x32_bf16 v[76:79], v[204:207], v[44:47], v[76:79]\n"
        "v_add_u32 v23, s60, v19\n"
        "ds_read_b128 v[44:47], v23 offset:768\n"
        "s_waitcnt lgkmcnt(4)\n"
        "v_mfma_f32_16x16x32_bf16 v[80:83], v[200:203], v[48:51], v[80:83]\n"
        "v_mfma_f32_16x16x32_bf16 v[84:87], v[204:207], v[48:51], v[84:87]\n"
        "global_load_dwordx4 v[248:251], %[vl16], s[82:83] offset:-1024 sc1\n"
        "global_load_dwordx4 v[252:255], v20, s[82:83] offset:-1024 sc1\n"
        "s_waitcnt vmcnt(10)\n"
        "v_max3_u32 v27, v208, v209, v27\n"
        "v_max3_u32 v27, v210, v211, v27\n"
        "v_max3_u32 v27, v212, v213, v27\n"
        "v_max3_u32 v27, v214, v215, v27\n"
        "v_add_u32 v22, s61, v19\n"
        "ds_read_b128 v[48:51], v22 offset:768\n"
        "s_waitcnt lgkmcnt(4)\n"
        "v_mfma_f32_16x16x32_bf16 v[64:67], v[208:211], v[52:55], v[64:67]\n"
        "v_mfma_f32_16x16x32_bf16 v[68:71], v[212:215], v[52:55], v[68:71]\n"
        "v_add_u32 v23, s62, v19\n"
        "ds_read_b128 v[52:55], v23 offset:768\n"
        "s_waitcnt lgkmcnt(4)\n"
        "v_mfma_f32_16x16x32_bf16 v[72:75], v[208:211], v[56:59], v[72:75]\n"
        "v_mfma_f32_16x16x32_bf16 v[76:79], v[212:215], v[56:59], v[76:79]\n"
        "v_add_u32 v22, s60, v16\n"
        "ds_read_b128 v[56:59], v22 offset:1024\n"
        "s_waitcnt lgkmcnt(4)\n"
        "v_mfma_f32_16x16x32_bf16 v[80:83], v[208:211], v[60:63], v[80:83]\n"
        "v_mfma_f32_16x16x32_bf16 v[84:87], v[212:215], v[60:63], v[84:87]\n"
        "s_waitcnt vmcnt(8)\n"
        "v_max3_u32 v27, v216, v217, v27\n"
        "v_max3_u32 v27, v218, v219, v27\n"
        "v_max3_u32 v27, v220, v221, v27\n"
        "v_max3_u32 v27, v222, v223, v27\n"
        "v_add_u32 v23, s61, v16\n"
        "ds_read_b128 v[60:63], v23 offset:1024\n"
        "s_waitcnt lgkmcnt(4)\n"
        "v_mfma_f32_16x16x32_bf16 v[64:67], v[216:219], v[44:47], v[64:67]\n"
        "v_mfma_f32_16x16x32_bf16 v[68:71], v[220:223], v[44:47], v[68:71]\n"
        "v_add_u32 v22, s62, v16\n"
        "ds_read_b128 v[44:47], v22 offset:1024\n"
        "s_waitcnt lgkmcnt(4)\n"
        "v_mfma_f32_16x16x32_bf16 v[72:75], v[216:219], v[48:51], v[72:75]\n"
        "v_mfma_f32_16x16x32_bf16 v[76:79], v[220:223], v[48:51], v[76:79]\n"
        "v_add_u32 v23, s60, v17\n"
        "ds_read_b128 v[48:51], v23 offset:1024\n"
        "s_waitcnt lgkmcnt(4)\n"
        "v_mfma_f32_16x16x32_bf16 v[80:83], v[216:219], v[52:55], v[80:83]\n"
        "v_mfma_f32_16x16x32_bf16 v[84:87], v[220:223], v[52:55], v[84:87]\n"
        "s_waitcnt vmcnt(6)\n"
        "v_max3_u32 v27, v224, v225, v27\n"
        "v_max3_u32 v27, v226, v227, v27\n"
        "v_max3_u32 v27, v228, v229, v27\n"
        "v_max3_u32 v27, v230, v231, v27\n"
        "v_add_u32 v22, s61, v17\n"
        "ds_read_b128 v[52:55], v22 offset:1024\n"
        "s_waitcnt lgkmcnt(4)\n"
        "v_mfma_f32_16x16x32_bf16 v[64:67], v[224:227], v[56:59], v[64:67]\n"
        "v_mfma_f32_16x16x32_bf16 v[68:71], v[228:231], v[56:59], v[68:71]\n"
        "v_add_u32 v23, s62, v17\n"
        "ds_read_b128 v[56:59], v23 offset:1024\n"
        "s_waitcnt lgkmcnt(4)\n"
        "v_mfma_f32_16x16x32_bf16 v[72:75], v[224:227], v[60:63], v[72:75]\n"
        "v_mfma_f32_16x16x32_bf16 v[76:79], v[228:231], v[60:63], v[76:79]\n"
        "v_add_u32 v22, s60, v18\n"
        "ds_read_b128 v[60:63], v22 offset:1024\n"
        "s_waitcnt lgkmcnt(4)\n"
        "v_mfma_f32_16x16x32_bf16 v[80:83], v[224:227], v[44:47], v[80:83]\n"
        "v_mfma_f32_16x16x32_bf16 v[84:87], v[228:231], v[44:47], v[84:87]\n"
        "s_waitcnt vmcnt(4)\n"
        "v_max3_u32 v27, v232, v233, v27\n"
        "v_max3_u32 v27, v234, v235, v27\n"
        "v_max3_u32 v27, v236, v237, v27\n"
        "v_max3_u32 v27, v238, v239, v27\n"
        "v_add_u32 v23, s61, v18\n"
        "ds_read_b128 v[44:47], v23 offset:1024\n"
        "s_waitcnt lgkmcnt(4)\n"
        "v_mfma_f32_16x16x32_bf16 v[64:67], v[232:235], v[48:51], v[64:67]\n"
        "v_mfma_f32_16x16x32_bf16 v[68:71], v[236:239], v[48:51], v[68:71]\n"
        "v_add_u32 v22, s62, v18\n"
        "ds_read_b128 v[48:51], v22 offset:1024\n"
        "s_waitcnt lgkmcnt(4)\n"
        "v_mfma_f32_16x16x32_bf16 v[72:75], v[232:235], v[52:55], v[72:75]\n"
        "v_mfma_f32_16x16x32_bf16 v[76:79], v[236:239], v[52:55], v[76:79]\n"
        "v_add_u32 v23, s60, v19\n"
        "ds_read_b128 v[52:55], v23 offset:1024\n"
        "s_waitcnt lgkmcnt(4)\n"
        "v_mfma_f32_16x16x32_bf16 v[80:83], v[232:235], v[56:59], v[80:83]\n"
        "v_mfma_f32_16x16x32_bf16 v[84:87], v[236:239], v[56:59], v[84:87]\n"
        "s_waitcnt vmcnt(2)\n"
        "v_max3_u32 v27, v240, v241, v27\n"
        "v_max3_u32 v27, v242, v243, v27\n"
        "v_max3_u32 v27, v244, v245, v27\n"
        "v_max3_u32 v27, v246, v247, v27\n"
        "v_add_u32 v22, s61, v19\n"
        "ds_read_b128 v[56:59], v22 offset:1024\n"
        "s_waitcnt lgkmcnt(4)\n"
        "v_mfma_f32_16x16x32_bf16 v[64:67], v[240:243], v[60:63], v[64:67]\n"
        "v_mfma_f32_16x16x32_bf16 v[68:71], v[244:247], v[60:63], v[68:71]\n"
        "v_add_u32 v23, s62, v19\n"
        "ds_read_b128 v[60:63], v23 offset:1024\n"
        "s_waitcnt lgkmcnt(4)\n"
        "v_mfma_f32_16x16x32_bf16 v[72:75], v[240:243], v[44:47], v[72:75]\n"
        "v_mfma_f32_16x16x32_bf16 v[76:79], v[244:247], v[44:47], v[76:79]\n"
        "v_add_u32 v22, s63, v16\n"
        "ds_read_b128 v[44:47], v22 offset:0\n"
        "s_waitcnt lgkmcnt(4)\n"
        "v_mfma_f32_16x16x32_bf16 v[80:83], v[240:243], v[48:51], v[80:83]\n"
        "v_mfma_f32_16x16x32_bf16 v[84:87], v[244:247], v[48:51], v[84:87]\n"
        "s_waitcnt vmcnt(0)\n"
        "v_max3_u32 v27, v248, v249, v27\n"
        "v_max3_u32 v27, v250, v251, v27\n"
        "v_max3_u32 v27, v252, v253, v27\n"
        "v_max3_u32 v27, v254, v255, v27\n"
        "v_add_u32 v23, s63, v17\n"
        "ds_read_b128 v[48:51], v23 offset:0\n"
        "s_waitcnt lgkmcnt(4)\n"
        "v_mfma_f32_16x16x32_bf16 v[64:67], v[248:251], v[52:55], v[64:67]\n"
        "v_mfma_f32_16x16x32_bf16 v[68:71], v[252:255], v[52:55], v[68:71]\n"
        "v_add_u32 v22, s63, v18\n"
        "ds_read_b128 v[52:55], v22 offset:0\n"
        "s_waitcnt lgkmcnt(4)\n"
        "v_mfma_f32_16x16x32_bf16 v[72:75], v[248:251], v[56:59], v[72:75]\n"
        "v_mfma_f32_16x16x32_bf16 v[76:79], v[252:255], v[56:59], v[76:79]\n"
        "v_add_u32 v23, s63, v19\n"
        "ds_read_b128 v[56:59], v23 offset:0\n"
        "s_waitcnt lgkmcnt(4)\n"
        "v_mfma_f32_16x16x32_bf16 v[80:83], v[248:251], v[60:63], v[80:83]\n"
        "v_mfma_f32_16x16x32_bf16 v[84:87], v[252:255], v[60:63], v[84:87]\n"
        "v_cmp_eq_u32 vcc, -1, v27\n"
        "s_cbranch_vccnz G1Q_%=\n"
        "G1C_%=:\n"
        "ds_read_b128 v[28:31], %[vtab]\n"
        "ds_read_b128 v[32:35], %[vtab] offset:64\n"
        "ds_read_b64 v[36:37], %[vmh]\n"
        "ds_read_b64 v[38:39], %[vmh] offset:128\n"
        "v_add_u32 v22, s63, v16\n"
        "ds_read_b128 v[60:63], v22 offset:256\n"
        "s_waitcnt lgkmcnt(8)\n"
        "v_mfma_f32_16x16x32_bf16 v[88:91], v[96:99], v[44:47], 0\n"
        "s_bfe_u32 s59, %[sjo], 262144\n"
        "v_lshl_add_u32 v21, s59, 7, %[vcs]\n"
        "ds_read_b64 v[24:25], v21\n"
        "v_mfma_f32_16x16x32_bf16 v[92:95], v[100:103], v[44:47], 0\n"
        "v_add_u32 v23, s63, v17\n"
        "ds_read_b128 v[44:47], v23 offset:256\n"
        "s_waitcnt lgkmcnt(9)\n"
        "v_mfma_f32_16x16x32_bf16 v[88:91], v[104:107], v[48:51], v[88:91]\n"
        "s_waitcnt lgkmcnt(1)\n"
        "v_mfma_f32_16x16x16_bf16 v[64:67], v[36:37], v[24:25], v[64:67]\n"
        "v_mfma_f32_16x16x16_bf16 v[68:71], v[38:39], v[24:25], v[68:71]\n"
        "v_mfma_f32_16x16x32_bf16 v[92:95], v[108:111], v[48:51], v[92:95]\n"
        "v_add_u32 v22, s63, v18\n"
        "ds_read_b128 v[48:51], v22 offset:256\n"
        "v_mfma_f32_16x16x32_bf16 v[88:91], v[112:115], v[52:55], v[88:91]\n"
        "v_mfma_f32_16x16x32_bf16 v[92:95], v[116:119], v[52:55], v[92:95]\n"
        "v_add_u32 v23, s63, v19\n"
        "ds_read_b128 v[52:55], v23 offset:256\n"
        "v_mfma_f32_16x16x32_bf16 v[88:91], v[120:123], v[56:59], v[88:91]\n"
        "v_mfma_f32_16x16x32_bf16 v[92:95], v[124:127], v[56:59], v[92:95]\n"
        "v_mul_f32 v64, v28, v64\n"
        "v_mul_f32 v65, v29, v65\n"
        "v_mul_f32 v66, v30, v66\n"
        "v_mul_f32 v67, v31, v67\n"
        "v_add_u32 v22, s63, v16\n"
        "ds_read_b128 v[56:59], v22 offset:512\n"
        "v_mfma_f32_16x16x32_bf16 v[88:91], v[128:131], v[60:63], v[88:91]\n"
        "v_mul_f32 v68, v32, v68\n"
        "v_mul_f32 v69, v33, v69\n"
        "v_mul_f32 v70, v34, v70\n"
        "v_mul_f32 v71, v35, v71\n"
        "v_mfma_f32_16x16x32_bf16 v[92:95], v[132:135], v[60:63], v[92:95]\n"
        "v_cvt_pk_bf16_f32 v64, v64, v65\n"
        "v_cvt_pk_bf16_f32 v65, v66, v67\n"
        "v_cvt_pk_bf16_f32 v66, v68, v69\n"
        "v_cvt_pk_bf16_f32 v67, v70, v71\n"
        "v_add_u32 v23, s63, v17\n"
        "ds_read_b128 v[60:63], v23 offset:512\n"
        "s_waitcnt lgkmcnt(4)\n"
        "v_mfma_f32_16x16x32_bf16 v[88:91], v[136:139], v[44:47], v[88:91]\n"
        "s_nop 1\n"
        "v_permlane32_swap_b32 v64, v66\n"
        "v_permlane32_swap_b32 v65, v67\n"
        "v_mfma_f32_16x16x32_bf16 v[92:95], v[140:143], v[44:47], v[92:95]\n"
        "s_nop 1\n"
        "v_permlane16_swap_b32 v64, v66\n"
        "v_permlane16_swap_b32 v65, v67\n"
        "v_add_u32 v22, s63, v18\n"
        "ds_read_b128 v[44:47], v22 offset:512\n"
        "s_waitcnt lgkmcnt(4)\n"
        "v_mfma_f32_16x16x32_bf16 v[88:91], v[144:147], v[48:51], v[88:91]\n"
        "v_min_u32 v64, -2, v64\n"
        "v_min_u32 v65, -2, v65\n"
        "v_min_u32 v66, -2, v66\n"
        "v_min_u32 v67, -2, v67\n"
        "s_bfe_u32 s59, %[sjo], 262144\n"
        "v_lshl_add_u32 v68, s59, 13, %[vab]\n"
        "global_store_dwordx4 v68, v[64:67], %[sws] sc1\n"
        "v_mfma_f32_16x16x32_bf16 v[92:95], v[148:151], v[48:51], v[92:95]\n"
        "s_bfe_u32 s59, %[sjo], 262148\n"
        "v_lshl_add_u32 v21, s59, 7, %[vcs]\n"
        "ds_read_b64 v[26:27], v21\n"
        "v_add_u32 v23, s63, v19\n"
        "ds_read_b128 v[48:51], v23 offset:512\n"
        "s_waitcnt lgkmcnt(5)\n"
        "v_mfma_f32_16x16x32_bf16 v[88:91], v[152:155], v[52:55], v[88:91]\n"
        "v_mfma_f32_16x16x32_bf16 v[92:95], v[156:159], v[52:55], v[92:95]\n"
        "s_waitcnt lgkmcnt(1)\n"
        "v_mfma_f32_16x16x16_bf16 v[72:75], v[36:37], v[26:27], v[72:75]\n"
        "v_mfma_f32_16x16x16_bf16 v[76:79], v[38:39], v[26:27], v[76:79]\n"
        "v_add_u32 v22, s63, v16\n"
        "ds_read_b128 v[52:55], v22 offset:768\n"
        "v_mfma_f32_16x16x32_bf16 v[88:91], v[160:163], v[56:59], v[88:91]\n"
        "v_mfma_f32_16x16x32_bf16 v[92:95], v[164:167], v[56:59], v[92:95]\n"
        "v_add_u32 v23, s63, v17\n"
        "ds_read_b128 v[56:59], v23 offset:768\n"
        "v_mfma_f32_16x16x32_bf16 v[88:91], v[168:171], v[60:63], v[88:91]\n"
        "v_mfma_f32_16x16x32_bf16 v[92:95], v[172:175], v[60:63], v[92:95]\n"
        "v_add_u32 v22, s63, v18\n"
        "ds_read_b128 v[60:63], v22 offset:768\n"
        "v_mfma_f32_16x16x32_bf16 v[88:91], v[176:179], v[44:47], v[88:91]\n"
        "v_mul_f32 v72, v28, v72\n"
        "v_mul_f32 v73, v29, v73\n"
        "v_mul_f32 v74, v30, v74\n"
        "v_mul_f32 v75, v31, v75\n"
        "v_mfma_f32_16x16x32_bf16 v[92:95], v[180:183], v[44:47], v[92:95]\n"
        "v_mul_f32 v76, v32, v76\n"
        "v_mul_f32 v77, v33, v77\n"
        "v_mul_f32 v78, v34, v78\n"
        "v_mul_f32 v79, v35, v79\n"
        "v_add_u32 v23, s63, v19\n"
        "ds_read_b128 v[44:47], v23 offset:768\n"
        "s_waitcnt lgkmcnt(4)\n"
        "v_mfma_f32_16x16x32_bf16 v[88:91], v[184:187], v[48:51], v[88:91]\n"
        "v_cvt_pk_bf16_f32 v72, v72, v73\n"
        "v_cvt_pk_bf16_f32 v73, v74, v75\n"
        "v_cvt_pk_bf16_f32 v74, v76, v77\n"
        "v_cvt_pk_bf16_f32 v75, v78, v79\n"
        "v_mfma_f32_16x16x32_bf16 v[92:95], v[188:191], v[48:51], v[92:95]\n"
        "s_nop 1\n"
        "v_permlane32_swap_b32 v72, v74\n"
        "v_permlane32_swap_b32 v73, v75\n"
        "v_add_u32 v22, s63, v16\n"
        "ds_read_b128 v[48:51], v22 offset:1024\n"
        "s_waitcnt lgkmcnt(4)\n"
        "v_mfma_f32_16x16x32_bf16 v[88:91], v[192:195], v[52:55], v[88:91]\n"
        "s_nop 1\n"
        "v_permlane16_swap_b32 v72, v74\n"
        "v_permlane16_swap_b32 v73, v75\n"
        "v_mfma_f32_16x16x32_bf16 v[92:95], v[196:199], v[52:55], v[92:95]\n"
        "v_min_u32 v72, -2, v72\n"
        "v_min_u32 v73, -2, v73\n"
        "v_min_u32 v74, -2, v74\n"
        "v_min_u32 v75, -2, v75\n"
        "s_bfe_u32 s59, %[sjo], 262148\n"
        "v_lshl_add_u32 v76, s59, 13, %[vab]\n"
        "global_store_dwordx4 v76, v[72:75], %[sws] sc1\n"
        "v_add_u32 v23, s63, v17\n"
        "ds_read_b128 v[52:55], v23 offset:1024\n"
        "s_waitcnt lgkmcnt(4)\n"
        "v_mfma_f32_16x16x32_bf16 v[88:91], v[200:203], v[56:59], v[88:91]\n"
        "s_bfe_u32 s59, %[sjo], 262152\n"
        "v_lshl_add_u32 v21, s59, 7, %[vcs]\n"
        "ds_read_b64 v[24:25], v21\n"
        "v_mfma_f32_16x16x32_bf16 v[92:95], v[204:207], v[56:59], v[92:95]\n"
        "v_add_u32 v22, s63, v18\n"
        "ds_read_b128 v[56:59], v22 offset:1024\n"
        "s_waitcnt lgkmcnt(5)\n"
        "v_mfma_f32_16x16x32_bf16 v[88:91], v[208:211], v[60:63], v[88:91]\n"
        "s_waitcnt lgkmcnt(1)\n"
        "v_mfma_f32_16x16x16_bf16 v[80:83], v[36:37], v[24:25], v[80:83]\n"
        "v_mfma_f32_16x16x16_bf16 v[84:87], v[38:39], v[24:25], v[84:87]\n"
        "v_mfma_f32_16x16x32_bf16 v[92:95], v[212:215], v[60:63], v[92:95]\n"
        "v_add_u32 v23, s63, v19\n"
        "ds_read_b128 v[60:63], v23 offset:1024\n"
        "v_mfma_f32_16x16x32_bf16 v[88:91], v[216:219], v[44:47], v[88:91]\n"
        "v_mfma_f32_16x16x32_bf16 v[92:95], v[220:223], v[44:47], v[92:95]\n"
        "v_add_u32 v22, s64, v16\n"
        "ds_read_b128 v[44:47], v22 offset:0\n"
        "v_mfma_f32_16x16x32_bf16 v[88:91], v[224:227], v[48:51], v[88:91]\n"
        "v_mfma_f32_16x16x32_bf16 v[92:95], v[228:231], v[48:51], v[92:95]\n"
        "v_mul_f32 v80, v28, v80\n"
        "v_mul_f32 v81, v29, v81\n"
        "v_mul_f32 v82, v30, v82\n"
        "v_mul_f32 v83, v31, v83\n"
        "v_add_u32 v23, s64, v17\n"
        "ds_read_b128 v[48:51], v23 offset:0\n"
        "v_mfma_f32_16x16x32_bf16 v[88:91], v[232:235], v[52:55], v[88:91]\n"
        "v_mul_f32 v84, v32, v84\n"
        "v_mul_f32 v85, v33, v85\n"
        "v_mul_f32 v86, v34, v86\n"
        "v_mul_f32 v87, v35, v87\n"
        "v_mfma_f32_16x16x32_bf16 v[92:95], v[236:239], v[52:55], v[92:95]\n"
        "v_cvt_pk_bf16_f32 v80, v80, v81\n"
        "v_cvt_pk_bf16_f32 v81, v82, v83\n"
        "v_cvt_pk_bf16_f32 v82, v84, v85\n"
        "v_cvt_pk_bf16_f32 v83, v86, v87\n"
        "v_add_u32 v22, s64, v18\n"
        "ds_read_b128 v[52:55], v22 offset:0\n"
        "s_waitcnt lgkmcnt(4)\n"
        "v_mfma_f32_16x16x32_bf16 v[88:91], v[240:243], v[56:59], v[88:91]\n"
        "s_nop 1\n"
        "v_permlane32_swap_b32 v80, v82\n"
        "v_permlane32_swap_b32 v81, v83\n"
        "v_mfma_f32_16x16x32_bf16 v[92:95], v[244:247], v[56:59], v[92:95]\n"
        "s_nop 1\n"
        "v_permlane16_swap_b32 v80, v82\n"
        "v_permlane16_swap_b32 v81, v83\n"
        "v_add_u32 v23, s64, v19\n"
        "ds_read_b128 v[56:59], v23 offset:0\n"
        "s_waitcnt lgkmcnt(4)\n"
        "v_mfma_f32_16x16x32_bf16 v[88:91], v[248:251], v[60:63], v[88:91]\n"
        "v_min_u32 v80, -2, v80\n"
        "v_min_u32 v81, -2, v81\n"
        "v_min_u32 v82, -2, v82\n"
        "v_min_u32 v83, -2, v83\n"
        "s_bfe_u32 s59, %[sjo], 262152\n"
        "v_lshl_add_u32 v84, s59, 13, %[vab]\n"
        "global_store_dwordx4 v84, v[80:83], %[sws] sc1\n"
        "v_mfma_f32_16x16x32_bf16 v[92:95], v[252:255], v[60:63], v[92:95]\n"
        "v_add_u32 v22, s64, v16\n"
        "ds_read_b128 v[60:63], v22 offset:256\n"
        "s_waitcnt lgkmcnt(4)\n"
        "v_mfma_f32_16x16x32_bf16 v[64:67], v[96:99], v[44:47], 0\n"
        "v_mfma_f32_16x16x32_bf16 v[68:71], v[100:103], v[44:47], 0\n"
        "v_add_u32 v23, s64, v17\n"
        "ds_read_b128 v[44:47], v23 offset:256\n"
        "s_waitcnt lgkmcnt(4)\n"
        "v_mfma_f32_16x16x32_bf16 v[64:67], v[104:107], v[48:51], v[64:67]\n"
        "s_bfe_u32 s59, %[sjo], 262156\n"
        "v_lshl_add_u32 v21, s59, 7, %[vcs]\n"
        "ds_read_b64 v[26:27], v21\n"
        "v_mfma_f32_16x16x32_bf16 v[68:71], v[108:111], v[48:51], v[68:71]\n"
        "v_add_u32 v22, s64, v18\n"
        "ds_read_b128 v[48:51], v22 offset:256\n"
        "s_waitcnt lgkmcnt(5)\n"
        "v_mfma_f32_16x16x32_bf16 v[64:67], v[112:115], v[52:55], v[64:67]\n"
        "v_mfma_f32_16x16x32_bf16 v[68:71], v[116:119], v[52:55], v[68:71]\n"
        "v_add_u32 v23, s64, v19\n"
        "ds_read_b128 v[52:55], v23 offset:256\n"
        "s_waitcnt lgkmcnt(5)\n"
        "v_mfma_f32_16x16x32_bf16 v[64:67], v[120:123], v[56:59], v[64:67]\n"
        "s_waitcnt lgkmcnt(2)\n"
        "v_mfma_f32_16x16x16_bf16 v[88:91], v[36:37], v[26:27], v[88:91]\n"
        "v_mfma_f32_16x16x32_bf16 v[68:71], v[124:127], v[56:59], v[68:71]\n"
        "v_mfma_f32_16x16x16_bf16 v[92:95], v[38:39], v[26:27], v[92:95]\n"
        "v_add_u32 v22, s64, v16\n"
        "ds_read_b128 v[56:59], v22 offset:512\n"
        "v_mfma_f32_16x16x32_bf16 v[64:67], v[128:131], v[60:63], v[64:67]\n"
        "v_mfma_f32_16x16x32_bf16 v[68:71], v[132:135], v[60:63], v[68:71]\n"
        "v_add_u32 v23, s64, v17\n"
        "ds_read_b128 v[60:63], v23 offset:512\n"
        "v_mfma_f32_16x16x32_bf16 v[64:67], v[136:139], v[44:47], v[64:67]\n"
        "v_mfma_f32_16x16x32_bf16 v[68:71], v[140:143], v[44:47], v[68:71]\n"
        "v_add_u32 v22, s64, v18\n"
        "ds_read_b128 v[44:47], v22 offset:512\n"
        "s_waitcnt lgkmcnt(4)\n"
        "v_mfma_f32_16x16x32_bf16 v[64:67], v[144:147], v[48:51], v[64:67]\n"
        "v_mul_f32 v88, v28, v88\n"
        "v_mul_f32 v89, v29, v89\n"
        "v_mfma_f32_16x16x32_bf16 v[68:71], v[148:151], v[48:51], v[68:71]\n"
        "v_mul_f32 v90, v30, v90\n"
        "v_mul_f32 v91, v31, v91\n"
        "v_add_u32 v23, s64, v19\n"
        "ds_read_b128 v[48:51], v23 offset:512\n"
        "s_waitcnt lgkmcnt(4)\n"
        "v_mfma_f32_16x16x32_bf16 v[64:67], v[152:155], v[52:55], v[64:67]\n"
        "v_mul_f32 v92, v32, v92\n"
        "v_mul_f32 v93, v33, v93\n"
        "v_mfma_f32_16x16x32_bf16 v[68:71], v[156:159], v[52:55], v[68:71]\n"
        "v_mul_f32 v94, v34, v94\n"
        "v_mul_f32 v95, v35, v95\n"
        "v_add_u32 v22, s64, v16\n"
        "ds_read_b128 v[52:55], v22 offset:768\n"
        "s_waitcnt lgkmcnt(4)\n"
        "v_mfma_f32_16x16x32_bf16 v[64:67], v[160:163], v[56:59], v[64:67]\n"
        "v_cvt_pk_bf16_f32 v88, v88, v89\n"
        "v_cvt_pk_bf16_f32 v89, v90, v91\n"
        "v_mfma_f32_16x16x32_bf16 v[68:71], v[164:167], v[56:59], v[68:71]\n"
        "v_cvt_pk_bf16_f32 v90, v92, v93\n"
        "v_cvt_pk_bf16_f32 v91, v94, v95\n"
        "v_add_u32 v23, s64, v17\n"
        "ds_read_b128 v[56:59], v23 offset:768\n"
        "s_waitcnt lgkmcnt(4)\n"
        "v_mfma_f32_16x16x32_bf16 v[64:67], v[168:171], v[60:63], v[64:67]\n"
        "s_nop 1\n"
        "v_mfma_f32_16x16x32_bf16 v[68:71], v[172:175], v[60:63], v[68:71]\n"
        "v_permlane32_swap_b32 v88, v90\n"
        "v_permlane32_swap_b32 v89, v91\n"
        "v_add_u32 v22, s64, v18\n"
        "ds_read_b128 v[60:63], v22 offset:768\n"
        "s_waitcnt lgkmcnt(4)\n"
        "v_mfma_f32_16x16x32_bf16 v[64:67], v[176:179], v[44:47], v[64:67]\n"
        "s_nop 1\n"
        "v_mfma_f32_16x16x32_bf16 v[68:71], v[180:183], v[44:47], v[68:71]\n"
        "v_permlane16_swap_b32 v88, v90\n"
        "v_permlane16_swap_b32 v89, v91\n"
        "v_add_u32 v23, s64, v19\n"
        "ds_read_b128 v[44:47], v23 offset:768\n"
        "s_waitcnt lgkmcnt(4)\n"
        "v_mfma_f32_16x16x32_bf16 v[64:67], v[184:187], v[48:51], v[64:67]\n"
        "v_min_u32 v88, -2, v88\n"
        "v_min_u32 v89, -2, v89\n"
        "v_min_u32 v90, -2, v90\n"
        "v_min_u32 v91, -2, v91\n"
        "v_mfma_f32_16x16x32_bf16 v[68:71], v[188:191], v[48:51], v[68:71]\n"
        "s_and_b32 s59, s48, 15\n"
        "s_lshl_b32 s59, s59, 14\n"
        "s_add_u32 s59, s59, s24\n"
        "v_add_u32 v20, s59, %[vl16]\n"
        "v_add_u32 v27, 8192, v20\n"
        "global_load_dwordx4 v[96:99], v20, %[sws] offset:-4096 sc1\n"
        "global_load_dwordx4 v[100:103], v27, %[sws] offset:-4096 sc1\n"
        "v_add_u32 v22, s64, v16\n"
        "ds_read_b128 v[48:51], v22 offset:1024\n"
        "s_waitcnt lgkmcnt(4)\n"
        "v_mfma_f32_16x16x32_bf16 v[64:67], v[192:195], v[52:55], v[64:67]\n"
        "v_mfma_f32_16x16x32_bf16 v[68:71], v[196:199], v[52:55], v[68:71]\n"
        "global_load_dwordx4 v[104:107], v20, %[sws] offset:-3072 sc1\n"
        "global_load_dwordx4 v[108:111], v27, %[sws] offset:-3072 sc1\n"
        "v_add_u32 v23, s64, v17\n"
        "ds_read_b128 v[52:55], v23 offset:1024\n"
        "s_waitcnt lgkmcnt(4)\n"
        "v_mfma_f32_16x16x32_bf16 v[64:67], v[200:203], v[56:59], v[64:67]\n"
        "v_mfma_f32_16x16x32_bf16 v[68:71], v[204:207], v[56:59], v[68:71]\n"
        "global_load_dwordx4 v[112:115], v20, %[sws] offset:-2048 sc1\n"
        "global_load_dwordx4 v[116:119], v27, %[sws] offset:-2048 sc1\n"
        "v_add_u32 v22, s64, v18\n"
        "ds_read_b128 v[56:59], v22 offset:1024\n"
        "s_waitcnt lgkmcnt(4)\n"
        "v_mfma_f32_16x16x32_bf16 v[64:67], v[208:211], v[60:63], v[64:67]\n"
        "v_mfma_f32_16x16x32_bf16 v[68:71], v[212:215], v[60:63], v[68:71]\n"
        "global_load_dwordx4 v[120:123], v20, %[sws] offset:-1024 sc1\n"
        "global_load_dwordx4 v[124:127], v27, %[sws] offset:-1024 sc1\n"
        "v_add_u32 v23, s64, v19\n"
        "ds_read_b128 v[60:63], v23 offset:1024\n"
        "s_waitcnt lgkmcnt(4)\n"
        "v_mfma_f32_16x16x32_bf16 v[64:67], v[216:219], v[44:47], v[64:67]\n"
        "v_mfma_f32_16x16x32_bf16 v[68:71], v[220:223], v[44:47], v[68:71]\n"
        "s_bfe_u32 s59, %[sjo], 262156\n"
        "v_lshl_add_u32 v92, s59, 13, %[vab]\n"
        "global_store_dwordx4 v92, v[88:91], %[sws] sc1\n"
        "s_waitcnt lgkmcnt(3)\n"
        "v_mfma_f32_16x16x32_bf16 v[64:67], v[224:227], v[48:51], v[64:67]\n"
        "v_mfma_f32_16x16x32_bf16 v[68:71], v[228:231], v[48:51], v[68:71]\n"
        "s_bfe_u32 s59, %[sjo], 262160\n"
        "v_lshl_add_u32 v21, s59, 7, %[vcs]\n"
        "ds_read_b64 v[24:25], v21\n"
        "s_waitcnt lgkmcnt(3)\n"
        "v_mfma_f32_16x16x32_bf16 v[64:67], v[232:235], v[52:55], v[64:67]\n"
        "v_mfma_f32_16x16x32_bf16 v[68:71], v[236:239], v[52:55], v[68:71]\n"
        "s_waitcnt lgkmcnt(2)\n"
        "v_mfma_f32_16x16x32_bf16 v[64:67], v[240:243], v[56:59], v[64:67]\n"
        "v_mfma_f32_16x16x32_bf16 v[68:71], v[244:247], v[56:59], v[68:71]\n"
        "s_waitcnt lgkmcnt(1)\n"
        "v_mfma_f32_16x16x32_bf16 v[64:67], v[248:251], v[60:63], v[64:67]\n"
        "v_mfma_f32_16x16x32_bf16 v[68:71], v[252:255], v[60:63], v[68:71]\n"
        "s_waitcnt lgkmcnt(0)\n"
        "v_mfma_f32_16x16x16_bf16 v[64:67], v[36:37], v[24:25], v[64:67]\n"
        "v_mfma_f32_16x16x16_bf16 v[68:71], v[38:39], v[24:25], v[68:71]\n"
        "s_nop 7\n"
        "v_mul_f32 v64, v28, v64\n"
        "v_mul_f32 v65, v29, v65\n"
        "v_mul_f32 v66, v30, v66\n"
        "v_mul_f32 v67, v31, v67\n"
        "v_mul_f32 v68, v32, v68\n"
        "v_mul_f32 v69, v33, v69\n"
        "v_mul_f32 v70, v34, v70\n"
        "v_mul_f32 v71, v35, v71\n"
        "v_cvt_pk_bf16_f32 v64, v64, v65\n"
        "v_cvt_pk_bf16_f32 v65, v66, v67\n"
        "v_cvt_pk_bf16_f32 v66, v68, v69\n"
        "v_cvt_pk_bf16_f32 v67, v70, v71\n"
        "s_nop 1\n"
        "v_permlane32_swap_b32 v64, v66\n"
        "v_permlane32_swap_b32 v65, v67\n"
        "s_nop 1\n"
        "v_permlane16_swap_b32 v64, v66\n"
        "v_permlane16_swap_b32 v65, v67\n"
        "v_min_u32 v64, -2, v64\n"
        "v_min_u32 v65, -2, v65\n"
        "v_min_u32 v66, -2, v66\n"
        "v_min_u32 v67, -2, v67\n"
        "s_bfe_u32 s59, %[sjo], 262160\n"
        "v_lshl_add_u32 v68, s59, 13, %[vab]\n"
        "global_store_dwordx4 v68, v[64:67], %[sws] sc1\n"
        "s_waitcnt lgkmcnt(0)\n"
        "s_barrier\n"
        "s_branch END_%=\n"
        "G1R_%=:\n"
        "s_waitcnt vmcnt(0)\n"
        "s_sub_u32 s81, s81, 1\n"
        "s_cbranch_scc1 G1P_%=\n"
        "s_sleep 1\n"
        "s_branch G1S_%=\n"
        "G1Q_%=:\n"
        "s_waitcnt vmcnt(0) lgkmcnt(0)\n"
        "s_sub_u32 s81, s81, 1\n"
        "s_cbranch_scc1 G1C_%=\n"
        "s_sleep 2\n"
        "s_branch G1S_%=\n"
        "END_%=:\n"
        : "=&{v[96:127]}"(W1)
        : [vl16] "v"(l * 16),
          [vdr] "v"(LD + (l & 15) * 1280 + (((l >> 4) ^ (l & 15)) << 4)),
          [vtab] "v"(T_AL + (w * 32 + (l >> 4) * 4) * 4),
          [vcs] "v"(T_CSF + (l & 15) * 8),
          [vmh] "v"(l < 16 ? T_MH + (w * 32 + l) * 8 : T_ZERO),
          [sjo] "s"(sjo),
          [sws] "s"(ws),
          [vab] "v"(abo + l * 16),
          [sx] "s"(ws + A_X1 + xrow + 4096),
          [sst] "s"(ws + A_ST + bt * 32768 + 4096),
          [vst] "v"(((l >> 5) * 1024 + 32 * w + (l & 31)) * 16),
          [vtw] "v"(T_AL + (w * 32 + (l & 31)) * 4),
          [swsum] "s"(wsum),
          [ssgn] "s"(0x3f800000u | ((unsigned)(bep & 8) << 28)),
          [sbep] "s"(bep),
          [smh] "s"(T_MH - 2 * T_AL),
          [srowc] "s"(0xffffu - (bt * 128 + w * 32)),
          [srb] "s"(ws + A_RB + (bt * 128 + w * 32) * 4),
          [sms] "s"(ws + A_MS + (bt * 128 + w * 32) * 16),
          "{s[24:39]}"(KP),
          "{s[40:43]}"(LB),
          "{s[44:47]}"(DP),
          "{s[48:55]}"(MP)
        : "scc", "vcc", "memory", "s59", "s60", "s61", "s62", "s63", "s64", "s81", "s82", "s83", "s84", "s85", "v16", "v17", "v18", "v19", "v20", "v21", "v22", "v23", "v24", "v25", "v26", "v27", "v28", "v29", "v30", "v31", "v32", "v33", "v34", "v35", "v36", "v37", "v38", "v39", "v40", "v41", "v42", "v43", "v44", "v45", "v46", "v47", "v48", "v49", "v50", "v51", "v52", "v53", "v54", "v55", "v56", "v57", "v58", "v59", "v60", "v61", "v62", "v63", "v64", "v65", "v66", "v67", "v68", "v69", "v70", "v71", "v72", "v73", "v74", "v75", "v76", "v77", "v78", "v79", "v80", "v81", "v82", "v83", "v84", "v85", "v86", "v87", "v88", "v89", "v90", "v91", "v92", "v93", "v94", "v95", "v128", "v129", "v130", "v131", "v132", "v133", "v134", "v135", "v136", "v137", "v138", "v139", "v140", "v141", "v142", "v143", "v144", "v145", "v146", "v147", "v148", "v149", "v150", "v151", "v152", "v153", "v154", "v155", "v156", "v157", "v158", "v159", "v160", "v161", "v162", "v163", "v164", "v165", "v166", "v167", "v168", "v169", "v170", "v171", "v172", "v173", "v174", "v175", "v176", "v177", "v178", "v179", "v180", "v181", "v182", "v183", "v184", "v185", "v186", "v187", "v188", "v189", "v190", "v191", "v192", "v193", "v194", "v195", "v196", "v197", "v198", "v199", "v200", "v201", "v202", "v203", "v204", "v205", "v206", "v207", "v208", "v209", "v210", "v211", "v212", "v213", "v214", "v215", "v216", "v217", "v218", "v219", "v220", "v221", "v222", "v223", "v224", "v225", "v226", "v227", "v228", "v229", "v230", "v231", "v232", "v233", "v234", "v235", "v236", "v237", "v238", "v239", "v240", "v241", "v242", "v243", "v244", "v245", "v246", "v247", "v248", "v249", "v250", "v251", "v252", "v253", "v254", "v255", "a0", "a1", "a2", "a3", "a4", "a5", "a6", "a7", "a8", "a9", "a10", "a11", "a12", "a13", "a14", "a15", "a16", "a17", "a18", "a19", "a20", "a21", "a22", "a23", "a24", "a25", "a26", "a27", "a28", "a29", "a30", "a31", "a32", "a33", "a34", "a35", "a36", "a37", "a38", "a39", "a40", "a41", "a42", "a43", "a44", "a45", "a46", "a47", "a48", "a49", "a50", "a51", "a52", "a53", "a54", "a55", "a56", "a57", "a58", "a59", "a60", "a61", "a62", "a63", "a64", "a65", "a66", "a67", "a68", "a69", "a70", "a71", "a72", "a73", "a74", "a75", "a76", "a77", "a78", "a79", "a80", "a81", "a82", "a83", "a84", "a85", "a86", "a87", "a88", "a89", "a90", "a91", "a92", "a93", "a94", "a95", "a96", "a97", "a98", "a99", "a100", "a101", "a102", "a103", "a104", "a105", "a106", "a107", "a108", "a109", "a110", "a111", "a112", "a113", "a114", "a115", "a116", "a117", "a118", "a119", "a120", "a121", "a122", "a123", "a124", "a125", "a126", "a127", "a128", "a129", "a130", "a131", "a132", "a133", "a134", "a135", "a136", "a137", "a138", "a139", "a140", "a141", "a142", "a143", "a144", "a145", "a146", "a147", "a148", "a149", "a150", "a151", "a152", "a153", "a154", "a155", "a156", "a157", "a158", "a159", "a160", "a161", "a162", "a163", "a164", "a165", "a166", "a167", "a168", "a169", "a170", "a171", "a172", "a173", "a174", "a175", "a176", "a177", "a178", "a179", "a180", "a181", "a182", "a183", "a184", "a185", "a186", "a187", "a188", "a189", "a190", "a191", "a192", "a193", "a194", "a195", "a196", "a197", "a198", "a199", "a200", "a201", "a202", "a203", "a204", "a205", "a206", "a207", "a208", "a209", "a210", "a211", "a212", "a213", "a214", "a215", "a216", "a217", "a218", "a219", "a220", "a221", "a222", "a223", "a224", "a225", "a226", "a227", "a228", "a229", "a230", "a231", "a232", "a233", "a234", "a235", "a236", "a237", "a238", "a239", "a240", "a241", "a242", "a243", "a244", "a245", "a246", "a247", "a248", "a249", "a250", "a251", "a252", "a253", "a254", "a255");
        ASTAMP_G1B;
    }
    LANE();
    STAMP(5);
    if (f1 >= 0) {
        u32x4 ka, kb;
        SPIN(n) {
            ka = ld16(R, A_RB + tid * 32); kb = ld16(R, A_RB + tid * 32 + 16);
            if (__all(mx4(ka, mx4(kb)) != SENT) || giveup(n)) break;
        }
        FST(20);
        unsigned rank;
        const unsigned me = topk(ka, kb, T_CAND, w, l, rank);
        FST(22);
        ffn1<0>(R, me, rank, f1, LRED, LD, ARG(1), ARG(2), w, l, ws, tid);
        FST(25);
        stage_w<20>(ARG(6) + f1 * 16, 1728, LD + 24576, w, l); stage_w<20>(ARG(7) + f1 * 16, 1728, LD + 24576 + 20480, w, l);
        bar();
    } else if (f2 >= 0) {
        stage_w<54>(ARG(8) + f2 * 16, 640, LD, w, l);
    }
    LANE();
    STAMP(6);
    asm volatile(
        "s_nop 4\n"
        "s_mov_b32 s81, 0x40000\n"
        "v_add_u32 v58, s54, %[vl16]\n"
        "v_lshrrev_b32 v59, 1, %[vl16]\n"
        "ds_read_b128 v[32:35], v58\n"
        "v_add_u32 v59, 156224, v59\n"
        "ds_read_b64 v[40:41], v59\n"
        "ds_read_b64 v[44:45], v59\n"
        "ds_read_b64 v[46:47], v59\n"
        "v_add_u32 v58, 161408, %[vl16]\n"
        "ds_read_b128 v[36:39], v58\n"
        "v_mov_b32 v42, 0\n"
        "v_mov_b32 v43, 0\n"
        "v_add_u32 v48, s40, %[vl16]\n"
        "v_add_u32 v49, s41, %[vl16]\n"
        "v_add_u32 v50, s42, %[vl16]\n"
        "v_add_u32 v51, s43, %[vl16]\n"
        "v_mov_b32 v63, s57\n"
        "s_waitcnt lgkmcnt(0)\n"
        "v_add_u32 v32, 0x20000, v32\n"
        "v_add_u32 v34, 0x20000, v34\n"
        "s_bfe_u32 s59, s48, 262144\n"
        "s_lshl_b32 s59, s59, 14\n"
        "s_add_u32 s59, s59, s24\n"
        "v_add_u32 v56, s59, %[vl16]\n"
        "v_add_u32 v57, 8192, v56\n"
        "s_waitcnt vmcnt(8)\n"
        "v_max3_u32 v59, v96, v97, v98\n"
        "v_max3_u32 v59, v99, v100, v59\n"
        "v_max3_u32 v59, v101, v102, v59\n"
        "v_max_u32 v59, v103, v59\n"
        "v_cmp_eq_u32 vcc, -1, v59\n"
        "s_cbranch_vccnz Rab0_%=\n"
        "Cab0_%=:\n"
        "v_mfma_f32_16x16x32_bf16 v[128:131], v[96:99], a[0:3], 0\n"
        "v_mfma_f32_16x16x32_bf16 v[132:135], v[100:103], a[0:3], 0\n"
        "v_mov_b32 v52, -1\n"
        "v_mov_b32 v53, -1\n"
        "v_mov_b32 v54, -1\n"
        "v_mov_b32 v55, -1\n"
        "v_mfma_f32_16x16x32_bf16 v[136:139], v[96:99], a[4:7], 0\n"
        "v_mfma_f32_16x16x32_bf16 v[140:143], v[100:103], a[4:7], 0\n"
        "v_mfma_f32_16x16x32_bf16 v[144:147], v[96:99], a[8:11], 0\n"
        "v_mfma_f32_16x16x32_bf16 v[148:151], v[100:103], a[8:11], 0\n"
        "v_mfma_f32_16x16x32_bf16 v[152:155], v[96:99], a[12:15], 0\n"
        "v_mfma_f32_16x16x32_bf16 v[156:159], v[100:103], a[12:15], 0\n"
        "v_mfma_f32_16x16x32_bf16 v[160:163], v[96:99], a[16:19], 0\n"
        "v_mfma_f32_16x16x32_bf16 v[164:167], v[100:103], a[16:19], 0\n"
        "v_mfma_f32_16x16x32_bf16 v[168:171], v[96:99], a[20:23], 0\n"
        "v_mfma_f32_16x16x32_bf16 v[172:175], v[100:103], a[20:23], 0\n"
        "v_mfma_f32_16x16x32_bf16 v[176:179], v[96:99], a[24:27], 0\n"
        "v_mfma_f32_16x16x32_bf16 v[180:183], v[100:103], a[24:27], 0\n"
        "v_mfma_f32_16x16x32_bf16 v[184:187], v[96:99], a[28:31], 0\n"
        "global_load_dwordx4 v[64:67], v56, %[sws] offset:0 sc1\n"
        "v_mfma_f32_16x16x32_bf16 v[188:191], v[100:103], a[28:31], 0\n"
        "global_load_dwordx4 v[68:71], v57, %[sws] offset:0 sc1\n"
        "s_waitcnt vmcnt(8)\n"
        "v_max3_u32 v59, v104, v105, v106\n"
        "v_max3_u32 v59, v107, v108, v59\n"
        "v_max3_u32 v59, v109, v110, v59\n"
        "v_max_u32 v59, v111, v59\n"
        "v_cmp_eq_u32 vcc, -1, v59\n"
        "s_cbranch_vccnz Rab1_%=\n"
        "Cab1_%=:\n"
        "v_mfma_f32_16x16x32_bf16 v[128:131], v[104:107], a[32:35], v[128:131]\n"
        "v_mfma_f32_16x16x32_bf16 v[132:135], v[108:111], a[32:35], v[132:135]\n"
        "v_mfma_f32_16x16x32_bf16 v[136:139], v[104:107], a[36:39], v[136:139]\n"
        "v_mfma_f32_16x16x32_bf16 v[140:143], v[108:111], a[36:39], v[140:143]\n"
        "v_mfma_f32_16x16x32_bf16 v[144:147], v[104:107], a[40:43], v[144:147]\n"
        "v_mfma_f32_16x16x32_bf16 v[148:151], v[108:111], a[40:43], v[148:151]\n"
        "v_mfma_f32_16x16x32_bf16 v[152:155], v[104:107], a[44:47], v[152:155]\n"
        "v_mfma_f32_16x16x32_bf16 v[156:159], v[108:111], a[44:47], v[156:159]\n"
        "v_mfma_f32_16x16x32_bf16 v[160:163], v[104:107], a[48:51], v[160:163]\n"
        "v_mfma_f32_16x16x32_bf16 v[164:167], v[108:111], a[48:51], v[164:167]\n"
        "v_mfma_f32_16x16x32_bf16 v[168:171], v[104:107], a[52:55], v[168:171]\n"
        "v_mfma_f32_16x16x32_bf16 v[172:175], v[108:111], a[52:55], v[172:175]\n"
        "v_mfma_f32_16x16x32_bf16 v[176:179], v[104:107], a[56:59], v[176:179]\n"
        "global_load_dwordx4 v[72:75], v56, %[sws] offset:1024 sc1\n"
        "v_mfma_f32_16x16x32_bf16 v[180:183], v[108:111], a[56:59], v[180:183]\n"
        "global_load_dwordx4 v[76:79], v57, %[sws] offset:1024 sc1\n"
        "v_mfma_f32_16x16x32_bf16 v[184:187], v[104:107], a[60:63], v[184:187]\n"
        "global_load_dwordx4 v[80:83], v56, %[sws] offset:2048 sc1\n"
        "v_mfma_f32_16x16x32_bf16 v[188:191], v[108:111], a[60:63], v[188:191]\n"
        "global_load_dwordx4 v[84:87], v57, %[sws] offset:2048 sc1\n"
        "s_waitcnt vmcnt(10)\n"
        "v_max3_u32 v59, v112, v113, v114\n"
        "v_max3_u32 v59, v115, v116, v59\n"
        "v_max3_u32 v59, v117, v118, v59\n"
        "v_max_u32 v59, v119, v59\n"
        "v_cmp_eq_u32 vcc, -1, v59\n"
        "s_cbranch_vccnz Rab2_%=\n"
        "Cab2_%=:\n"
        "v_mfma_f32_16x16x32_bf16 v[128:131], v[112:115], a[64:67], v[128:131]\n"
        "v_mfma_f32_16x16x32_bf16 v[132:135], v[116:119], a[64:67], v[132:135]\n"
        "v_mfma_f32_16x16x32_bf16 v[136:139], v[112:115], a[68:71], v[136:139]\n"
        "v_mfma_f32_16x16x32_bf16 v[140:143], v[116:119], a[68:71], v[140:143]\n"
        "v_mfma_f32_16x16x32_bf16 v[144:147], v[112:115], a[72:75], v[144:147]\n"
        "v_add_u32 v62, s47, %[vl16]\n"
        "global_store_dwordx4 v62, v[52:55], %[sws] offset:0 sc1\n"
        "v_mfma_f32_16x16x32_bf16 v[148:151], v[116:119], a[72:75], v[148:151]\n"
        "v_mfma_f32_16x16x32_bf16 v[152:155], v[112:115], a[76:79], v[152:155]\n"
        "v_mfma_f32_16x16x32_bf16 v[156:159], v[116:119], a[76:79], v[156:159]\n"
        "v_mfma_f32_16x16x32_bf16 v[160:163], v[112:115], a[80:83], v[160:163]\n"
        "v_mfma_f32_16x16x32_bf16 v[164:167], v[116:119], a[80:83], v[164:167]\n"
        "v_mfma_f32_16x16x32_bf16 v[168:171], v[112:115], a[84:87], v[168:171]\n"
        "v_mfma_f32_16x16x32_bf16 v[172:175], v[116:119], a[84:87], v[172:175]\n"
        "v_mfma_f32_16x16x32_bf16 v[176:179], v[112:115], a[88:91], v[176:179]\n"
        "v_mfma_f32_16x16x32_bf16 v[180:183], v[116:119], a[88:91], v[180:183]\n"
        "v_mfma_f32_16x16x32_bf16 v[184:187], v[112:115], a[92:95], v[184:187]\n"
        "global_load_dwordx4 v[88:91], v56, %[sws] offset:3072 sc1\n"
        "v_mfma_f32_16x16x32_bf16 v[188:191], v[116:119], a[92:95], v[188:191]\n"
        "global_load_dwordx4 v[92:95], v57, %[sws] offset:3072 sc1\n"
        "s_waitcnt vmcnt(11)\n"
        "v_max3_u32 v59, v120, v121, v122\n"
        "v_max3_u32 v59, v123, v124, v59\n"
        "v_max3_u32 v59, v125, v126, v59\n"
        "v_max_u32 v59, v127, v59\n"
        "v_cmp_eq_u32 vcc, -1, v59\n"
        "s_cbranch_vccnz Rab3_%=\n"
        "Cab3_%=:\n"
        "v_mfma_f32_16x16x32_bf16 v[128:131], v[120:123], a[96:99], v[128:131]\n"
        "v_mfma_f32_16x16x32_bf16 v[132:135], v[124:127], a[96:99], v[132:135]\n"
        "v_mfma_f32_16x16x32_bf16 v[136:139], v[120:123], a[100:103], v[136:139]\n"
        "v_mfma_f32_16x16x32_bf16 v[140:143], v[124:127], a[100:103], v[140:143]\n"
        "v_mfma_f32_16x16x32_bf16 v[144:147], v[120:123], a[104:107], v[144:147]\n"
        "v_mfma_f32_16x16x32_bf16 v[148:151], v[124:127], a[104:107], v[148:151]\n"
        "v_mfma_f32_16x16x32_bf16 v[152:155], v[120:123], a[108:111], v[152:155]\n"
        "v_mfma_f32_16x16x32_bf16 v[156:159], v[124:127], a[108:111], v[156:159]\n"
        "v_mfma_f32_16x16x32_bf16 v[160:163], v[120:123], a[112:115], v[160:163]\n"
        "v_mfma_f32_16x16x32_bf16 v[164:167], v[124:127], a[112:115], v[164:167]\n"
        "v_mfma_f32_16x16x32_bf16 v[168:171], v[120:123], a[116:119], v[168:171]\n"
        "v_mfma_f32_16x16x32_bf16 v[172:175], v[124:127], a[116:119], v[172:175]\n"
        "v_mfma_f32_16x16x32_bf16 v[176:179], v[120:123], a[120:123], v[176:179]\n"
        "s_bfe_u32 s59, s48, 262148\n"
        "s_lshl_b32 s59, s59, 14\n"
        "s_add_u32 s59, s59, s24\n"
        "v_add_u32 v56, s59, %[vl16]\n"
        "v_add_u32 v57, 8192, v56\n"
        "global_load_dwordx4 v[96:99], v56, %[sws] offset:-4096 sc1\n"
        "v_mfma_f32_16x16x32_bf16 v[180:183], v[124:127], a[120:123], v[180:183]\n"
        "global_load_dwordx4 v[100:103], v57, %[sws] offset:-4096 sc1\n"
        "v_mfma_f32_16x16x32_bf16 v[184:187], v[120:123], a[124:127], v[184:187]\n"
        "v_add_u32 v62, s47, %[vl16]\n"
        "global_store_dwordx4 v62, v[52:55], %[sws] offset:1024 sc1\n"
        "global_load_dwordx4 v[104:107], v56, %[sws] offset:-3072 sc1\n"
        "v_mfma_f32_16x16x32_bf16 v[188:191], v[124:127], a[124:127], v[188:191]\n"
        "global_load_dwordx4 v[108:111], v57, %[sws] offset:-3072 sc1\n"
        "s_waitcnt vmcnt(12)\n"
        "v_max3_u32 v59, v64, v65, v66\n"
        "v_max3_u32 v59, v67, v68, v59\n"
        "v_max3_u32 v59, v69, v70, v59\n"
        "v_max_u32 v59, v71, v59\n"
        "v_cmp_eq_u32 vcc, -1, v59\n"
        "s_cbranch_vccnz Rab4_%=\n"
        "Cab4_%=:\n"
        "v_mfma_f32_16x16x32_bf16 v[128:131], v[64:67], a[128:131], v[128:131]\n"
        "v_mfma_f32_16x16x32_bf16 v[132:135], v[68:71], a[128:131], v[132:135]\n"
        "v_mfma_f32_16x16x32_bf16 v[136:139], v[64:67], a[132:135], v[136:139]\n"
        "v_mfma_f32_16x16x32_bf16 v[140:143], v[68:71], a[132:135], v[140:143]\n"
        "v_mfma_f32_16x16x32_bf16 v[144:147], v[64:67], a[136:139], v[144:147]\n"
        "v_mfma_f32_16x16x32_bf16 v[148:151], v[68:71], a[136:139], v[148:151]\n"
        "v_mfma_f32_16x16x32_bf16 v[152:155], v[64:67], a[140:143], v[152:155]\n"
        "v_mfma_f32_16x16x32_bf16 v[156:159], v[68:71], a[140:143], v[156:159]\n"
        "v_mfma_f32_16x16x32_bf16 v[160:163], v[64:67], a[144:147], v[160:163]\n"
        "v_mfma_f32_16x16x32_bf16 v[164:167], v[68:71], a[144:147], v[164:167]\n"
        "v_mfma_f32_16x16x32_bf16 v[168:171], v[64:67], a[148:151], v[168:171]\n"
        "v_mfma_f32_16x16x32_bf16 v[172:175], v[68:71], a[148:151], v[172:175]\n"
        "v_mfma_f32_16x16x32_bf16 v[176:179], v[64:67], a[152:155], v[176:179]\n"
        "v_mfma_f32_16x16x32_bf16 v[180:183], v[68:71], a[152:155], v[180:183]\n"
        "v_mfma_f32_16x16x32_bf16 v[184:187], v[64:67], a[156:159], v[184:187]\n"
        "global_load_dwordx4 v[112:115], v56, %[sws] offset:-2048 sc1\n"
        "v_mfma_f32_16x16x32_bf16 v[188:191], v[68:71], a[156:159], v[188:191]\n"
        "global_load_dwordx4 v[116:119], v57, %[sws] offset:-2048 sc1\n"
        "s_waitcnt vmcnt(12)\n"
        "v_max3_u32 v59, v72, v73, v74\n"
        "v_max3_u32 v59, v75, v76, v59\n"
        "v_max3_u32 v59, v77, v78, v59\n"
        "v_max_u32 v59, v79, v59\n"
        "v_cmp_eq_u32 vcc, -1, v59\n"
        "s_cbranch_vccnz Rab5_%=\n"
        "Cab5_%=:\n"
        "v_mfma_f32_16x16x32_bf16 v[128:131], v[72:75], a[160:163], v[128:131]\n"
        "v_mfma_f32_16x16x32_bf16 v[132:135], v[76:79], a[160:163], v[132:135]\n"
        "v_mfma_f32_16x16x32_bf16 v[136:139], v[72:75], a[164:167], v[136:139]\n"
        "v_mfma_f32_16x16x32_bf16 v[140:143], v[76:79], a[164:167], v[140:143]\n"
        "v_mfma_f32_16x16x32_bf16 v[144:147], v[72:75], a[168:171], v[144:147]\n"
        "v_mfma_f32_16x16x32_bf16 v[148:151], v[76:79], a[168:171], v[148:151]\n"
        "v_mfma_f32_16x16x32_bf16 v[152:155], v[72:75], a[172:175], v[152:155]\n"
        "v_mfma_f32_16x16x32_bf16 v[156:159], v[76:79], a[172:175], v[156:159]\n"
        "v_mfma_f32_16x16x32_bf16 v[160:163], v[72:75], a[176:179], v[160:163]\n"
        "v_add_u32 v62, s31, %[vl16]\n"
        "global_store_dwordx4 v62, v[52:55], %[sws] offset:0 sc1\n"
        "v_mfma_f32_16x16x32_bf16 v[164:167], v[76:79], a[176:179], v[164:167]\n"
        "v_mfma_f32_16x16x32_bf16 v[168:171], v[72:75], a[180:183], v[168:171]\n"
        "v_mfma_f32_16x16x32_bf16 v[172:175], v[76:79], a[180:183], v[172:175]\n"
        "v_mfma_f32_16x16x32_bf16 v[176:179], v[72:75], a[184:187], v[176:179]\n"
        "global_load_dwordx4 v[120:123], v56, %[sws] offset:-1024 sc1\n"
        "v_mfma_f32_16x16x32_bf16 v[180:183], v[76:79], a[184:187], v[180:183]\n"
        "global_load_dwordx4 v[124:127], v57, %[sws] offset:-1024 sc1\n"
        "v_mfma_f32_16x16x32_bf16 v[184:187], v[72:75], a[188:191], v[184:187]\n"
        "global_load_dwordx4 v[64:67], v56, %[sws] offset:0 sc1\n"
        "v_mfma_f32_16x16x32_bf16 v[188:191], v[76:79], a[188:191], v[188:191]\n"
        "global_load_dwordx4 v[68:71], v57, %[sws] offset:0 sc1\n"
        "s_waitcnt vmcnt(15)\n"
        "v_max3_u32 v59, v80, v81, v82\n"
        "v_max3_u32 v59, v83, v84, v59\n"
        "v_max3_u32 v59, v85, v86, v59\n"
        "v_max_u32 v59, v87, v59\n"
        "v_cmp_eq_u32 vcc, -1, v59\n"
        "s_cbranch_vccnz Rab6_%=\n"
        "Cab6_%=:\n"
        "v_mfma_f32_16x16x32_bf16 v[128:131], v[80:83], a[192:195], v[128:131]\n"
        "v_mfma_f32_16x16x32_bf16 v[132:135], v[84:87], a[192:195], v[132:135]\n"
        "v_mfma_f32_16x16x32_bf16 v[136:139], v[80:83], a[196:199], v[136:139]\n"
        "v_mfma_f32_16x16x32_bf16 v[140:143], v[84:87], a[196:199], v[140:143]\n"
        "v_mfma_f32_16x16x32_bf16 v[144:147], v[80:83], a[200:203], v[144:147]\n"
        "v_mfma_f32_16x16x32_bf16 v[148:151], v[84:87], a[200:203], v[148:151]\n"
        "v_mfma_f32_16x16x32_bf16 v[152:155], v[80:83], a[204:207], v[152:155]\n"
        "v_mfma_f32_16x16x32_bf16 v[156:159], v[84:87], a[204:207], v[156:159]\n"
        "v_mfma_f32_16x16x32_bf16 v[160:163], v[80:83], a[208:211], v[160:163]\n"
        "v_mfma_f32_16x16x32_bf16 v[164:167], v[84:87], a[208:211], v[164:167]\n"
        "v_mfma_f32_16x16x32_bf16 v[168:171], v[80:83], a[212:215], v[168:171]\n"
        "v_mfma_f32_16x16x32_bf16 v[172:175], v[84:87], a[212:215], v[172:175]\n"
        "v_mfma_f32_16x16x32_bf16 v[176:179], v[80:83], a[216:219], v[176:179]\n"
        "v_mfma_f32_16x16x32_bf16 v[180:183], v[84:87], a[216:219], v[180:183]\n"
        "v_mfma_f32_16x16x32_bf16 v[184:187], v[80:83], a[220:223], v[184:187]\n"
        "global_load_dwordx4 v[72:75], v56, %[sws] offset:1024 sc1\n"
        "v_mfma_f32_16x16x32_bf16 v[188:191], v[84:87], a[220:223], v[188:191]\n"
        "global_load_dwordx4 v[76:79], v57, %[sws] offset:1024 sc1\n"
        "s_waitcnt vmcnt(14)\n"
        "v_max3_u32 v59, v88, v89, v90\n"
        "v_max3_u32 v59, v91, v92, v59\n"
        "v_max3_u32 v59, v93, v94, v59\n"
        "v_max_u32 v59, v95, v59\n"
        "v_cmp_eq_u32 vcc, -1, v59\n"
        "s_cbranch_vccnz Rab7_%=\n"
        "Cab7_%=:\n"
        "v_mfma_f32_16x16x32_bf16 v[128:131], v[88:91], a[224:227], v[128:131]\n"
        "v_mfma_f32_16x16x32_bf16 v[132:135], v[92:95], a[224:227], v[132:135]\n"
        "v_mfma_f32_16x16x32_bf16 v[136:139], v[88:91], a[228:231], v[136:139]\n"
        "v_add_u32 v62, s31, %[vl16]\n"
        "v_add_u32 v62, 20480, v62\n"
        "global_store_dwordx4 v62, v[52:55], %[sws] offset:0 sc1\n"
        "v_mfma_f32_16x16x32_bf16 v[140:143], v[92:95], a[228:231], v[140:143]\n"
        "v_mfma_f32_16x16x32_bf16 v[144:147], v[88:91], a[232:235], v[144:147]\n"
        "v_mfma_f32_16x16x32_bf16 v[148:151], v[92:95], a[232:235], v[148:151]\n"
        "v_mfma_f32_16x16x32_bf16 v[152:155], v[88:91], a[236:239], v[152:155]\n"
        "v_mfma_f32_16x16x32_bf16 v[156:159], v[92:95], a[236:239], v[156:159]\n"
        "v_mfma_f32_16x16x32_bf16 v[160:163], v[88:91], a[240:243], v[160:163]\n"
        "v_mfma_f32_16x16x32_bf16 v[164:167], v[92:95], a[240:243], v[164:167]\n"
        "v_mfma_f32_16x16x32_bf16 v[168:171], v[88:91], a[244:247], v[168:171]\n"
        "v_mfma_f32_16x16x32_bf16 v[172:175], v[92:95], a[244:247], v[172:175]\n"
        "v_mfma_f32_16x16x32_bf16 v[176:179], v[88:91], a[248:251], v[176:179]\n"
        "v_mfma_f32_16x16x32_bf16 v[180:183], v[92:95], a[248:251], v[180:183]\n"
        "v_mfma_f32_16x16x32_bf16 v[184:187], v[88:91], a[252:255], v[184:187]\n"
        "global_load_dwordx4 v[80:83], v56, %[sws] offset:2048 sc1\n"
        "v_mfma_f32_16x16x32_bf16 v[188:191], v[92:95], a[252:255], v[188:191]\n"
        "global_load_dwordx4 v[84:87], v57, %[sws] offset:2048 sc1\n"
        "s_waitcnt vmcnt(15)\n"
        "v_max3_u32 v59, v96, v97, v98\n"
        "v_max3_u32 v59, v99, v100, v59\n"
        "v_max3_u32 v59, v101, v102, v59\n"
        "v_max_u32 v59, v103, v59\n"
        "v_cmp_eq_u32 vcc, -1, v59\n"
        "s_cbranch_vccnz Rab8_%=\n"
        "Cab8_%=:\n"
        "v_mfma_f32_16x16x32_bf16 v[192:195], v[96:99], a[0:3], 0\n"
        "v_mfma_f32_16x16x32_bf16 v[196:199], v[100:103], a[0:3], 0\n"
        "v_mfma_f32_16x16x32_bf16 v[200:203], v[96:99], a[4:7], 0\n"
        "v_cvt_pk_bf16_f32 v144, v144, v145\n"
        "v_cvt_pk_bf16_f32 v145, v146, v147\n"
        "v_mfma_f32_16x16x32_bf16 v[204:207], v[100:103], a[4:7], 0\n"
        "v_cvt_pk_bf16_f32 v146, v148, v149\n"
        "v_cvt_pk_bf16_f32 v147, v150, v151\n"
        "v_mfma_f32_16x16x32_bf16 v[208:211], v[96:99], a[8:11], 0\n"
        "ds_write_b128 v48, v[144:147] offset:0\n"
        "v_mfma_f32_16x16x32_bf16 v[212:215], v[100:103], a[8:11], 0\n"
        "v_cvt_pk_bf16_f32 v152, v152, v153\n"
        "v_cvt_pk_bf16_f32 v153, v154, v155\n"
        "v_mfma_f32_16x16x32_bf16 v[216:219], v[96:99], a[12:15], 0\n"
        "v_cvt_pk_bf16_f32 v154, v156, v157\n"
        "v_cvt_pk_bf16_f32 v155, v158, v159\n"
        "v_mfma_f32_16x16x32_bf16 v[220:223], v[100:103], a[12:15], 0\n"
        "ds_write_b128 v48, v[152:155] offset:1024\n"
        "v_mfma_f32_16x16x32_bf16 v[224:227], v[96:99], a[16:19], 0\n"
        "v_cvt_pk_bf16_f32 v160, v160, v161\n"
        "v_cvt_pk_bf16_f32 v161, v162, v163\n"
        "v_mfma_f32_16x16x32_bf16 v[228:231], v[100:103], a[16:19], 0\n"
        "v_cvt_pk_bf16_f32 v162, v164, v165\n"
        "v_cvt_pk_bf16_f32 v163, v166, v167\n"
        "v_mfma_f32_16x16x32_bf16 v[232:235], v[96:99], a[20:23], 0\n"
        "ds_write_b128 v49, v[160:163] offset:0\n"
        "v_mfma_f32_16x16x32_bf16 v[236:239], v[100:103], a[20:23], 0\n"
        "v_cvt_pk_bf16_f32 v168, v168, v169\n"
        "v_cvt_pk_bf16_f32 v169, v170, v171\n"
        "v_mfma_f32_16x16x32_bf16 v[240:243], v[96:99], a[24:27], 0\n"
        "v_cvt_pk_bf16_f32 v170, v172, v173\n"
        "v_cvt_pk_bf16_f32 v171, v174, v175\n"
        "v_mfma_f32_16x16x32_bf16 v[244:247], v[100:103], a[24:27], 0\n"
        "ds_write_b128 v49, v[168:171] offset:1024\n"
        "v_mfma_f32_16x16x32_bf16 v[248:251], v[96:99], a[28:31], 0\n"
        "v_cvt_pk_bf16_f32 v176, v176, v177\n"
        "v_cvt_pk_bf16_f32 v177, v178, v179\n"
        "global_load_dwordx4 v[88:91], v56, %[sws] offset:3072 sc1\n"
        "v_mfma_f32_16x16x32_bf16 v[252:255], v[100:103], a[28:31], 0\n"
        "v_cvt_pk_bf16_f32 v178, v180, v181\n"
        "v_cvt_pk_bf16_f32 v179, v182, v183\n"
        "global_load_dwordx4 v[92:95], v57, %[sws] offset:3072 sc1\n"
        "s_waitcnt vmcnt(14)\n"
        "v_max3_u32 v59, v104, v105, v106\n"
        "v_max3_u32 v59, v107, v108, v59\n"
        "v_max3_u32 v59, v109, v110, v59\n"
        "v_max_u32 v59, v111, v59\n"
        "v_cmp_eq_u32 vcc, -1, v59\n"
        "s_cbranch_vccnz Rab9_%=\n"
        "Cab9_%=:\n"
        "v_mfma_f32_16x16x32_bf16 v[192:195], v[104:107], a[32:35], v[192:195]\n"
        "ds_write_b128 v50, v[176:179] offset:0\n"
        "v_mfma_f32_16x16x32_bf16 v[196:199], v[108:111], a[32:35], v[196:199]\n"
        "v_cvt_pk_bf16_f32 v184, v184, v185\n"
        "v_cvt_pk_bf16_f32 v185, v186, v187\n"
        "v_mfma_f32_16x16x32_bf16 v[200:203], v[104:107], a[36:39], v[200:203]\n"
        "v_cvt_pk_bf16_f32 v186, v188, v189\n"
        "v_cvt_pk_bf16_f32 v187, v190, v191\n"
        "v_mfma_f32_16x16x32_bf16 v[204:207], v[108:111], a[36:39], v[204:207]\n"
        "ds_write_b128 v50, v[184:187] offset:1024\n"
        "v_mfma_f32_16x16x32_bf16 v[208:211], v[104:107], a[40:43], v[208:211]\n"
        "v_mfma_f32_16x16x32_bf16 v[212:215], v[108:111], a[40:43], v[212:215]\n"
        "v_mfma_f32_16x16x32_bf16 v[216:219], v[104:107], a[44:47], v[216:219]\n"
        "s_waitcnt lgkmcnt(0)\n"
        "s_barrier\n"
        "v_mfma_f32_16x16x32_bf16 v[220:223], v[108:111], a[44:47], v[220:223]\n"
        "ds_read_b128 v[144:147], v51 offset:0\n"
        "v_mfma_f32_16x16x32_bf16 v[224:227], v[104:107], a[48:51], v[224:227]\n"
        "ds_read_b128 v[148:151], v51 offset:1024\n"
        "v_mfma_f32_16x16x32_bf16 v[228:231], v[108:111], a[48:51], v[228:231]\n"
        "ds_read_b128 v[152:155], v51 offset:2048\n"
        "v_mfma_f32_16x16x32_bf16 v[232:235], v[104:107], a[52:55], v[232:235]\n"
        "ds_read_b128 v[156:159], v51 offset:3072\n"
        "v_mfma_f32_16x16x32_bf16 v[236:239], v[108:111], a[52:55], v[236:239]\n"
        "ds_read_b128 v[160:163], v51 offset:4096\n"
        "v_mfma_f32_16x16x32_bf16 v[240:243], v[104:107], a[56:59], v[240:243]\n"
        "ds_read_b128 v[164:167], v51 offset:5120\n"
        "v_mfma_f32_16x16x32_bf16 v[244:247], v[108:111], a[56:59], v[244:247]\n"
        "v_mfma_f32_16x16x32_bf16 v[248:251], v[104:107], a[60:63], v[248:251]\n"
        "s_bfe_u32 s59, s48, 262152\n"
        "s_lshl_b32 s59, s59, 14\n"
        "s_add_u32 s59, s59, s24\n"
        "v_add_u32 v56, s59, %[vl16]\n"
        "v_add_u32 v57, 8192, v56\n"
        "global_load_dwordx4 v[96:99], v56, %[sws] offset:-4096 sc1\n"
        "v_mfma_f32_16x16x32_bf16 v[252:255], v[108:111], a[60:63], v[252:255]\n"
        "global_load_dwordx4 v[100:103], v57, %[sws] offset:-4096 sc1\n"
        "s_waitcnt vmcnt(14)\n"
        "v_max3_u32 v59, v112, v113, v114\n"
        "v_max3_u32 v59, v115, v116, v59\n"
        "v_max3_u32 v59, v117, v118, v59\n"
        "v_max_u32 v59, v119, v59\n"
        "v_cmp_eq_u32 vcc, -1, v59\n"
        "s_cbranch_vccnz Rab10_%=\n"
        "Cab10_%=:\n"
        "v_mfma_f32_16x16x32_bf16 v[192:195], v[112:115], a[64:67], v[192:195]\n"
        "s_waitcnt lgkmcnt(5)\n"
        "v_mfma_f32_16x16x32_bf16 v[128:131], v[40:43], v[144:147], v[128:131]\n"
        "v_mfma_f32_16x16x32_bf16 v[196:199], v[116:119], a[64:67], v[196:199]\n"
        "v_mfma_f32_16x16x32_bf16 v[132:135], v[42:45], v[144:147], v[132:135]\n"
        "v_mfma_f32_16x16x32_bf16 v[200:203], v[112:115], a[68:71], v[200:203]\n"
        "s_waitcnt lgkmcnt(4)\n"
        "v_mfma_f32_16x16x32_bf16 v[136:139], v[40:43], v[148:151], v[136:139]\n"
        "v_mfma_f32_16x16x32_bf16 v[204:207], v[116:119], a[68:71], v[204:207]\n"
        "v_mfma_f32_16x16x32_bf16 v[140:143], v[42:45], v[148:151], v[140:143]\n"
        "v_mfma_f32_16x16x32_bf16 v[208:211], v[112:115], a[72:75], v[208:211]\n"
        "s_waitcnt lgkmcnt(3)\n"
        "v_mfma_f32_16x16x32_bf16 v[128:131], v[40:43], v[152:155], v[128:131]\n"
        "v_mfma_f32_16x16x32_bf16 v[212:215], v[116:119], a[72:75], v[212:215]\n"
        "v_mfma_f32_16x16x32_bf16 v[132:135], v[42:45], v[152:155], v[132:135]\n"
        "v_mfma_f32_16x16x32_bf16 v[216:219], v[112:115], a[76:79], v[216:219]\n"
        "s_waitcnt lgkmcnt(2)\n"
        "v_mfma_f32_16x16x32_bf16 v[136:139], v[40:43], v[156:159], v[136:139]\n"
        "v_mfma_f32_16x16x32_bf16 v[220:223], v[116:119], a[76:79], v[220:223]\n"
        "v_mfma_f32_16x16x32_bf16 v[140:143], v[42:45], v[156:159], v[140:143]\n"
        "v_mfma_f32_16x16x32_bf16 v[224:227], v[112:115], a[80:83], v[224:227]\n"
        "s_waitcnt lgkmcnt(1)\n"
        "v_mfma_f32_16x16x32_bf16 v[128:131], v[40:43], v[160:163], v[128:131]\n"
        "v_mfma_f32_16x16x32_bf16 v[228:231], v[116:119], a[80:83], v[228:231]\n"
        "v_mfma_f32_16x16x32_bf16 v[132:135], v[42:45], v[160:163], v[132:135]\n"
        "v_mfma_f32_16x16x32_bf16 v[232:235], v[112:115], a[84:87], v[232:235]\n"
        "s_waitcnt lgkmcnt(0)\n"
        "v_mfma_f32_16x16x32_bf16 v[136:139], v[40:43], v[164:167], v[136:139]\n"
        "v_mfma_f32_16x16x32_bf16 v[236:239], v[116:119], a[84:87], v[236:239]\n"
        "v_mfma_f32_16x16x32_bf16 v[140:143], v[42:45], v[164:167], v[140:143]\n"
        "v_mfma_f32_16x16x32_bf16 v[240:243], v[112:115], a[88:91], v[240:243]\n"
        "s_barrier\n"
        "v_mfma_f32_16x16x32_bf16 v[244:247], v[116:119], a[88:91], v[244:247]\n"
        "v_mfma_f32_16x16x32_bf16 v[248:251], v[112:115], a[92:95], v[248:251]\n"
        "global_load_dwordx4 v[104:107], v56, %[sws] offset:-3072 sc1\n"
        "v_mfma_f32_16x16x32_bf16 v[252:255], v[116:119], a[92:95], v[252:255]\n"
        "global_load_dwordx4 v[108:111], v57, %[sws] offset:-3072 sc1\n"
        "s_waitcnt vmcnt(13)\n"
        "v_max3_u32 v59, v120, v121, v122\n"
        "v_max3_u32 v59, v123, v124, v59\n"
        "v_max3_u32 v59, v125, v126, v59\n"
        "v_max_u32 v59, v127, v59\n"
        "v_cmp_eq_u32 vcc, -1, v59\n"
        "s_cbranch_vccnz Rab11_%=\n"
        "Cab11_%=:\n"
        "v_mfma_f32_16x16x32_bf16 v[192:195], v[120:123], a[96:99], v[192:195]\n"
        "v_mfma_f32_16x16x32_bf16 v[196:199], v[124:127], a[96:99], v[196:199]\n"
        "v_mfma_f32_16x16x32_bf16 v[200:203], v[120:123], a[100:103], v[200:203]\n"
        "v_cvt_pk_bf16_f32 v144, v128, v129\n"
        "v_cvt_pk_bf16_f32 v145, v130, v131\n"
        "v_mfma_f32_16x16x32_bf16 v[204:207], v[124:127], a[100:103], v[204:207]\n"
        "v_cvt_pk_bf16_f32 v146, v132, v133\n"
        "v_cvt_pk_bf16_f32 v147, v134, v135\n"
        "v_mfma_f32_16x16x32_bf16 v[208:211], v[120:123], a[104:107], v[208:211]\n"
        "v_cvt_pk_bf16_f32 v148, v136, v137\n"
        "v_cvt_pk_bf16_f32 v149, v138, v139\n"
        "v_mfma_f32_16x16x32_bf16 v[212:215], v[124:127], a[104:107], v[212:215]\n"
        "v_cvt_pk_bf16_f32 v150, v140, v141\n"
        "v_cvt_pk_bf16_f32 v151, v142, v143\n"
        "v_lshrrev_b32 v58, 1, %[vl16]\n"
        "v_mfma_f32_16x16x32_bf16 v[216:219], v[120:123], a[108:111], v[216:219]\n"
        "v_add_u32 v58, s44, v58\n"
        "v_mfma_f32_16x16x32_bf16 v[220:223], v[124:127], a[108:111], v[220:223]\n"
        "global_store_dwordx2 v58, v[144:145], %[sws] sc1\n"
        "v_mfma_f32_16x16x32_bf16 v[224:227], v[120:123], a[112:115], v[224:227]\n"
        "v_add_u32 v58, 32768, v58\n"
        "v_mfma_f32_16x16x32_bf16 v[228:231], v[124:127], a[112:115], v[228:231]\n"
        "global_store_dwordx2 v58, v[146:147], %[sws] sc1\n"
        "v_mfma_f32_16x16x32_bf16 v[232:235], v[120:123], a[116:119], v[232:235]\n"
        "v_add_u32 v58, 32768, v58\n"
        "v_mfma_f32_16x16x32_bf16 v[236:239], v[124:127], a[116:119], v[236:239]\n"
        "global_store_dwordx2 v58, v[148:149], %[sws] sc1\n"
        "v_mfma_f32_16x16x32_bf16 v[240:243], v[120:123], a[120:123], v[240:243]\n"
        "v_add_u32 v58, 32768, v58\n"
        "v_mfma_f32_16x16x32_bf16 v[244:247], v[124:127], a[120:123], v[244:247]\n"
        "global_store_dwordx2 v58, v[150:151], %[sws] sc1\n"
        "v_mfma_f32_16x16x32_bf16 v[248:251], v[120:123], a[124:127], v[248:251]\n"
        "global_load_dwordx4 v[112:115], v56, %[sws] offset:-2048 sc1\n"
        "v_mfma_f32_16x16x32_bf16 v[252:255], v[124:127], a[124:127], v[252:255]\n"
        "global_load_dwordx4 v[116:119], v57, %[sws] offset:-2048 sc1\n"
        "s_waitcnt vmcnt(17)\n"
        "v_max3_u32 v59, v64, v65, v66\n"
        "v_max3_u32 v59, v67, v68, v59\n"
        "v_max3_u32 v59, v69, v70, v59\n"
        "v_max_u32 v59, v71, v59\n"
        "v_cmp_eq_u32 vcc, -1, v59\n"
        "s_cbranch_vccnz Rab12_%=\n"
        "Cab12_%=:\n"
        "v_mfma_f32_16x16x32_bf16 v[192:195], v[64:67], a[128:131], v[192:195]\n"
        "v_mfma_f32_16x16x32_bf16 v[196:199], v[68:71], a[128:131], v[196:199]\n"
        "v_mfma_f32_16x16x32_bf16 v[200:203], v[64:67], a[132:135], v[200:203]\n"
        "v_mfma_f32_16x16x32_bf16 v[204:207], v[68:71], a[132:135], v[204:207]\n"
        "v_mfma_f32_16x16x32_bf16 v[208:211], v[64:67], a[136:139], v[208:211]\n"
        "v_mfma_f32_16x16x32_bf16 v[212:215], v[68:71], a[136:139], v[212:215]\n"
        "v_mfma_f32_16x16x32_bf16 v[216:219], v[64:67], a[140:143], v[216:219]\n"
        "v_mfma_f32_16x16x32_bf16 v[220:223], v[68:71], a[140:143], v[220:223]\n"
        "v_mfma_f32_16x16x32_bf16 v[224:227], v[64:67], a[144:147], v[224:227]\n"
        "v_add_u32 v62, s32, v36\n"
        "global_store_dwordx2 v62, v[52:53], %[sws] offset:0 sc1\n"
        "v_mfma_f32_16x16x32_bf16 v[228:231], v[68:71], a[144:147], v[228:231]\n"
        "v_mfma_f32_16x16x32_bf16 v[232:235], v[64:67], a[148:151], v[232:235]\n"
        "v_mfma_f32_16x16x32_bf16 v[236:239], v[68:71], a[148:151], v[236:239]\n"
        "v_mfma_f32_16x16x32_bf16 v[240:243], v[64:67], a[152:155], v[240:243]\n"
        "v_mfma_f32_16x16x32_bf16 v[244:247], v[68:71], a[152:155], v[244:247]\n"
        "v_mfma_f32_16x16x32_bf16 v[248:251], v[64:67], a[156:159], v[248:251]\n"
        "global_load_dwordx4 v[120:123], v56, %[sws] offset:-1024 sc1\n"
        "v_mfma_f32_16x16x32_bf16 v[252:255], v[68:71], a[156:159], v[252:255]\n"
        "global_load_dwordx4 v[124:127], v57, %[sws] offset:-1024 sc1\n"
        "s_waitcnt vmcnt(18)\n"
        "v_max3_u32 v59, v72, v73, v74\n"
        "v_max3_u32 v59, v75, v76, v59\n"
        "v_max3_u32 v59, v77, v78, v59\n"
        "v_max_u32 v59, v79, v59\n"
        "v_cmp_eq_u32 vcc, -1, v59\n"
        "s_cbranch_vccnz Rab13_%=\n"
        "Cab13_%=:\n"
        "v_mfma_f32_16x16x32_bf16 v[192:195], v[72:75], a[160:163], v[192:195]\n"
        "v_mfma_f32_16x16x32_bf16 v[196:199], v[76:79], a[160:163], v[196:199]\n"
        "v_mfma_f32_16x16x32_bf16 v[200:203], v[72:75], a[164:167], v[200:203]\n"
        "v_mfma_f32_16x16x32_bf16 v[204:207], v[76:79], a[164:167], v[204:207]\n"
        "v_mfma_f32_16x16x32_bf16 v[208:211], v[72:75], a[168:171], v[208:211]\n"
        "v_mfma_f32_16x16x32_bf16 v[212:215], v[76:79], a[168:171], v[212:215]\n"
        "v_mfma_f32_16x16x32_bf16 v[216:219], v[72:75], a[172:175], v[216:219]\n"
        "v_mfma_f32_16x16x32_bf16 v[220:223], v[76:79], a[172:175], v[220:223]\n"
        "v_mfma_f32_16x16x32_bf16 v[224:227], v[72:75], a[176:179], v[224:227]\n"
        "v_mfma_f32_16x16x32_bf16 v[228:231], v[76:79], a[176:179], v[228:231]\n"
        "v_mfma_f32_16x16x32_bf16 v[232:235], v[72:75], a[180:183], v[232:235]\n"
        "v_mfma_f32_16x16x32_bf16 v[236:239], v[76:79], a[180:183], v[236:239]\n"
        "v_mfma_f32_16x16x32_bf16 v[240:243], v[72:75], a[184:187], v[240:243]\n"
        "v_mfma_f32_16x16x32_bf16 v[244:247], v[76:79], a[184:187], v[244:247]\n"
        "v_mfma_f32_16x16x32_bf16 v[248:251], v[72:75], a[188:191], v[248:251]\n"
        "global_load_dwordx4 v[64:67], v56, %[sws] offset:0 sc1\n"
        "v_mfma_f32_16x16x32_bf16 v[252:255], v[76:79], a[188:191], v[252:255]\n"
        "global_load_dwordx4 v[68:71], v57, %[sws] offset:0 sc1\n"
        "s_waitcnt vmcnt(17)\n"
        "v_max3_u32 v59, v80, v81, v82\n"
        "v_max3_u32 v59, v83, v84, v59\n"
        "v_max3_u32 v59, v85, v86, v59\n"
        "v_max_u32 v59, v87, v59\n"
        "v_cmp_eq_u32 vcc, -1, v59\n"
        "s_cbranch_vccnz Rab14_%=\n"
        "Cab14_%=:\n"
        "v_mfma_f32_16x16x32_bf16 v[192:195], v[80:83], a[192:195], v[192:195]\n"
        "v_mfma_f32_16x16x32_bf16 v[196:199], v[84:87], a[192:195], v[196:199]\n"
        "v_mfma_f32_16x16x32_bf16 v[200:203], v[80:83], a[196:199], v[200:203]\n"
        "v_mfma_f32_16x16x32_bf16 v[204:207], v[84:87], a[196:199], v[204:207]\n"
        "v_mfma_f32_16x16x32_bf16 v[208:211], v[80:83], a[200:203], v[208:211]\n"
        "v_mfma_f32_16x16x32_bf16 v[212:215], v[84:87], a[200:203], v[212:215]\n"
        "v_mfma_f32_16x16x32_bf16 v[216:219], v[80:83], a[204:207], v[216:219]\n"
        "v_mfma_f32_16x16x32_bf16 v[220:223], v[84:87], a[204:207], v[220:223]\n"
        "v_mfma_f32_16x16x32_bf16 v[224:227], v[80:83], a[208:211], v[224:227]\n"
        "v_lshrrev_b32 v62, 1, %[vl16]\n"
        "v_add_u32 v62, s33, v62\n"
        "global_store_dwordx2 v62, v[52:53], %[sws] offset:0 sc1\n"
        "v_mfma_f32_16x16x32_bf16 v[228:231], v[84:87], a[208:211], v[228:231]\n"
        "v_mfma_f32_16x16x32_bf16 v[232:235], v[80:83], a[212:215], v[232:235]\n"
        "v_mfma_f32_16x16x32_bf16 v[236:239], v[84:87], a[212:215], v[236:239]\n"
        "v_mfma_f32_16x16x32_bf16 v[240:243], v[80:83], a[216:219], v[240:243]\n"
        "v_mfma_f32_16x16x32_bf16 v[244:247], v[84:87], a[216:219], v[244:247]\n"
        "v_mfma_f32_16x16x32_bf16 v[248:251], v[80:83], a[220:223], v[248:251]\n"
        "global_load_dwordx4 v[72:75], v56, %[sws] offset:1024 sc1\n"
        "v_mfma_f32_16x16x32_bf16 v[252:255], v[84:87], a[220:223], v[252:255]\n"
        "global_load_dwordx4 v[76:79], v57, %[sws] offset:1024 sc1\n"
        "s_waitcnt vmcnt(18)\n"
        "v_max3_u32 v59, v88, v89, v90\n"
        "v_max3_u32 v59, v91, v92, v59\n"
        "v_max3_u32 v59, v93, v94, v59\n"
        "v_max_u32 v59, v95, v59\n"
        "v_cmp_eq_u32 vcc, -1, v59\n"
        "s_cbranch_vccnz Rab15_%=\n"
        "Cab15_%=:\n"
        "v_mfma_f32_16x16x32_bf16 v[192:195], v[88:91], a[224:227], v[192:195]\n"
        "v_mfma_f32_16x16x32_bf16 v[196:199], v[92:95], a[224:227], v[196:199]\n"
        "v_mfma_f32_16x16x32_bf16 v[200:203], v[88:91], a[228:231], v[200:203]\n"
        "v_mfma_f32_16x16x32_bf16 v[204:207], v[92:95], a[228:231], v[204:207]\n"
        "v_mfma_f32_16x16x32_bf16 v[208:211], v[88:91], a[232:235], v[208:211]\n"
        "v_mfma_f32_16x16x32_bf16 v[212:215], v[92:95], a[232:235], v[212:215]\n"
        "v_mfma_f32_16x16x32_bf16 v[216:219], v[88:91], a[236:239], v[216:219]\n"
        "v_mfma_f32_16x16x32_bf16 v[220:223], v[92:95], a[236:239], v[220:223]\n"
        "v_mfma_f32_16x16x32_bf16 v[224:227], v[88:91], a[240:243], v[224:227]\n"
        "v_mfma_f32_16x16x32_bf16 v[228:231], v[92:95], a[240:243], v[228:231]\n"
        "v_mfma_f32_16x16x32_bf16 v[232:235], v[88:91], a[244:247], v[232:235]\n"
        "v_mfma_f32_16x16x32_bf16 v[236:239], v[92:95], a[244:247], v[236:239]\n"
        "v_mfma_f32_16x16x32_bf16 v[240:243], v[88:91], a[248:251], v[240:243]\n"
        "v_mfma_f32_16x16x32_bf16 v[244:247], v[92:95], a[248:251], v[244:247]\n"
        "v_mfma_f32_16x16x32_bf16 v[248:251], v[88:91], a[252:255], v[248:251]\n"
        "global_load_dwordx4 v[80:83], v56, %[sws] offset:2048 sc1\n"
        "v_mfma_f32_16x16x32_bf16 v[252:255], v[92:95], a[252:255], v[252:255]\n"
        "global_load_dwordx4 v[84:87], v57, %[sws] offset:2048 sc1\n"
        "s_waitcnt lgkmcnt(0)\n"
        "s_nop 7\n"
        "s_branch END_%=\n"
        "Rab0_%=:\n"
        "s_sub_u32 s81, s81, 1\n"
        "s_cbranch_scc1 Cab0_%=\n"
        "s_sleep 1\n"
        "s_bfe_u32 s59, s48, 262144\n"
        "s_lshl_b32 s59, s59, 14\n"
        "s_add_u32 s59, s59, s24\n"
        "v_add_u32 v62, s59, %[vl16]\n"
        "global_load_dwordx4 v[96:99], v62, %[sws] offset:-4096 sc1\n"
        "v_add_u32 v62, 8192, v62\n"
        "global_load_dwordx4 v[100:103], v62, %[sws] offset:-4096 sc1\n"
        "s_waitcnt vmcnt(0)\n"
        "v_max3_u32 v59, v96, v97, v98\n"
        "v_max3_u32 v59, v99, v100, v59\n"
        "v_max3_u32 v59, v101, v102, v59\n"
        "v_max_u32 v59, v103, v59\n"
        "v_cmp_eq_u32 vcc, -1, v59\n"
        "s_cbranch_vccnz Rab0_%=\n"
        "s_branch Cab0_%=\n"
        "Rab1_%=:\n"
        "s_sub_u32 s81, s81, 1\n"
        "s_cbranch_scc1 Cab1_%=\n"
        "s_sleep 1\n"
        "s_bfe_u32 s59, s48, 262144\n"
        "s_lshl_b32 s59, s59, 14\n"
        "s_add_u32 s59, s59, s24\n"
        "v_add_u32 v62, s59, %[vl16]\n"
        "global_load_dwordx4 v[104:107], v62, %[sws] offset:-3072 sc1\n"
        "v_add_u32 v62, 8192, v62\n"
        "global_load_dwordx4 v[108:111], v62, %[sws] offset:-3072 sc1\n"
        "s_waitcnt vmcnt(0)\n"
        "v_max3_u32 v59, v104, v105, v106\n"
        "v_max3_u32 v59, v107, v108, v59\n"
        "v_max3_u32 v59, v109, v110, v59\n"
        "v_max_u32 v59, v111, v59\n"
        "v_cmp_eq_u32 vcc, -1, v59\n"
        "s_cbranch_vccnz Rab1_%=\n"
        "s_branch Cab1_%=\n"
        "Rab2_%=:\n"
        "s_sub_u32 s81, s81, 1\n"
        "s_cbranch_scc1 Cab2_%=\n"
        "s_sleep 1\n"
        "s_bfe_u32 s59, s48, 262144\n"
        "s_lshl_b32 s59, s59, 14\n"
        "s_add_u32 s59, s59, s24\n"
        "v_add_u32 v62, s59, %[vl16]\n"
        "global_load_dwordx4 v[112:115], v62, %[sws] offset:-2048 sc1\n"
        "v_add_u32 v62, 8192, v62\n"
        "global_load_dwordx4 v[116:119], v62, %[sws] offset:-2048 sc1\n"
        "s_waitcnt vmcnt(0)\n"
        "v_max3_u32 v59, v112, v113, v114\n"
        "v_max3_u32 v59, v115, v116, v59\n"
        "v_max3_u32 v59, v117, v118, v59\n"
        "v_max_u32 v59, v119, v59\n"
        "v_cmp_eq_u32 vcc, -1, v59\n"
        "s_cbranch_vccnz Rab2_%=\n"
        "s_branch Cab2_%=\n"
        "Rab3_%=:\n"
        "s_sub_u32 s81, s81, 1\n"
        "s_cbranch_scc1 Cab3_%=\n"
        "s_sleep 1\n"
        "s_bfe_u32 s59, s48, 262144\n"
        "s_lshl_b32 s59, s59, 14\n"
        "s_add_u32 s59, s59, s24\n"
        "v_add_u32 v62, s59, %[vl16]\n"
        "global_load_dwordx4 v[120:123], v62, %[sws] offset:-1024 sc1\n"
        "v_add_u32 v62, 8192, v62\n"
        "global_load_dwordx4 v[124:127], v62, %[sws] offset:-1024 sc1\n"
        "s_waitcnt vmcnt(0)\n"
        "v_max3_u32 v59, v120, v121, v122\n"
        "v_max3_u32 v59, v123, v124, v59\n"
        "v_max3_u32 v59, v125, v126, v59\n"
        "v_max_u32 v59, v127, v59\n"
        "v_cmp_eq_u32 vcc, -1, v59\n"
        "s_cbranch_vccnz Rab3_%=\n"
        "s_branch Cab3_%=\n"
        "Rab4_%=:\n"
        "s_sub_u32 s81, s81, 1\n"
        "s_cbranch_scc1 Cab4_%=\n"
        "s_sleep 1\n"
        "s_bfe_u32 s59, s48, 262144\n"
        "s_lshl_b32 s59, s59, 14\n"
        "s_add_u32 s59, s59, s24\n"
        "v_add_u32 v62, s59, %[vl16]\n"
        "global_load_dwordx4 v[64:67], v62, %[sws] offset:0 sc1\n"
        "v_add_u32 v62, 8192, v62\n"
        "global_load_dwordx4 v[68:71], v62, %[sws] offset:0 sc1\n"
        "s_waitcnt vmcnt(0)\n"
        "v_max3_u32 v59, v64, v65, v66\n"
        "v_max3_u32 v59, v67, v68, v59\n"
        "v_max3_u32 v59, v69, v70, v59\n"
        "v_max_u32 v59, v71, v59\n"
        "v_cmp_eq_u32 vcc, -1, v59\n"
        "s_cbranch_vccnz Rab4_%=\n"
        "s_branch Cab4_%=\n"
        "Rab5_%=:\n"
        "s_sub_u32 s81, s81, 1\n"
        "s_cbranch_scc1 Cab5_%=\n"
        "s_sleep 1\n"
        "s_bfe_u32 s59, s48, 262144\n"
        "s_lshl_b32 s59, s59, 14\n"
        "s_add_u32 s59, s59, s24\n"
        "v_add_u32 v62, s59, %[vl16]\n"
        "global_load_dwordx4 v[72:75], v62, %[sws] offset:1024 sc1\n"
        "v_add_u32 v62, 8192, v62\n"
        "global_load_dwordx4 v[76:79], v62, %[sws] offset:1024 sc1\n"
        "s_waitcnt vmcnt(0)\n"
        "v_max3_u32 v59, v72, v73, v74\n"
        "v_max3_u32 v59, v75, v76, v59\n"
        "v_max3_u32 v59, v77, v78, v59\n"
        "v_max_u32 v59, v79, v59\n"
        "v_cmp_eq_u32 vcc, -1, v59\n"
        "s_cbranch_vccnz Rab5_%=\n"
        "s_branch Cab5_%=\n"
        "Rab6_%=:\n"
        "s_sub_u32 s81, s81, 1\n"
        "s_cbranch_scc1 Cab6_%=\n"
        "s_sleep 1\n"
        "s_bfe_u32 s59, s48, 262144\n"
        "s_lshl_b32 s59, s59, 14\n"
        "s_add_u32 s59, s59, s24\n"
        "v_add_u32 v62, s59, %[vl16]\n"
        "global_load_dwordx4 v[80:83], v62, %[sws] offset:2048 sc1\n"
        "v_add_u32 v62, 8192, v62\n"
        "global_load_dwordx4 v[84:87], v62, %[sws] offset:2048 sc1\n"
        "s_waitcnt vmcnt(0)\n"
        "v_max3_u32 v59, v80, v81, v82\n"
        "v_max3_u32 v59, v83, v84, v59\n"
        "v_max3_u32 v59, v85, v86, v59\n"
        "v_max_u32 v59, v87, v59\n"
        "v_cmp_eq_u32 vcc, -1, v59\n"
        "s_cbranch_vccnz Rab6_%=\n"
        "s_branch Cab6_%=\n"
        "Rab7_%=:\n"
        "s_sub_u32 s81, s81, 1\n"
        "s_cbranch_scc1 Cab7_%=\n"
        "s_sleep 1\n"
        "s_bfe_u32 s59, s48, 262144\n"
        "s_lshl_b32 s59, s59, 14\n"
        "s_add_u32 s59, s59, s24\n"
        "v_add_u32 v62, s59, %[vl16]\n"
        "global_load_dwordx4 v[88:91], v62, %[sws] offset:3072 sc1\n"
        "v_add_u32 v62, 8192, v62\n"
        "global_load_dwordx4 v[92:95], v62, %[sws] offset:3072 sc1\n"
        "s_waitcnt vmcnt(0)\n"
        "v_max3_u32 v59, v88, v89, v90\n"
        "v_max3_u32 v59, v91, v92, v59\n"
        "v_max3_u32 v59, v93, v94, v59\n"
        "v_max_u32 v59, v95, v59\n"
        "v_cmp_eq_u32 vcc, -1, v59\n"
        "s_cbranch_vccnz Rab7_%=\n"
        "s_branch Cab7_%=\n"
        "Rab8_%=:\n"
        "s_sub_u32 s81, s81, 1\n"
        "s_cbranch_scc1 Cab8_%=\n"
        "s_sleep 1\n"
        "s_bfe_u32 s59, s48, 262148\n"
        "s_lshl_b32 s59, s59, 14\n"
        "s_add_u32 s59, s59, s24\n"
        "v_add_u32 v62, s59, %[vl16]\n"
        "global_load_dwordx4 v[96:99], v62, %[sws] offset:-4096 sc1\n"
        "v_add_u32 v62, 8192, v62\n"
        "global_load_dwordx4 v[100:103], v62, %[sws] offset:-4096 sc1\n"
        "s_waitcnt vmcnt(0)\n"
        "v_max3_u32 v59, v96, v97, v98\n"
        "v_max3_u32 v59, v99, v100, v59\n"
        "v_max3_u32 v59, v101, v102, v59\n"
        "v_max_u32 v59, v103, v59\n"
        "v_cmp_eq_u32 vcc, -1, v59\n"
        "s_cbranch_vccnz Rab8_%=\n"
        "s_branch Cab8_%=\n"
        "Rab9_%=:\n"
        "s_sub_u32 s81, s81, 1\n"
        "s_cbranch_scc1 Cab9_%=\n"
        "s_sleep 1\n"
        "s_bfe_u32 s59, s48, 262148\n"
        "s_lshl_b32 s59, s59, 14\n"
        "s_add_u32 s59, s59, s24\n"
        "v_add_u32 v62, s59, %[vl16]\n"
        "global_load_dwordx4 v[104:107], v62, %[sws] offset:-3072 sc1\n"
        "v_add_u32 v62, 8192, v62\n"
        "global_load_dwordx4 v[108:111], v62, %[sws] offset:-3072 sc1\n"
        "s_waitcnt vmcnt(0)\n"
        "v_max3_u32 v59, v104, v105, v106\n"
        "v_max3_u32 v59, v107, v108, v59\n"
        "v_max3_u32 v59, v109, v110, v59\n"
        "v_max_u32 v59, v111, v59\n"
        "v_cmp_eq_u32 vcc, -1, v59\n"
        "s_cbranch_vccnz Rab9_%=\n"
        "s_branch Cab9_%=\n"
        "Rab10_%=:\n"
        "s_sub_u32 s81, s81, 1\n"
        "s_cbranch_scc1 Cab10_%=\n"
        "s_sleep 1\n"
        "s_bfe_u32 s59, s48, 262148\n"
        "s_lshl_b32 s59, s59, 14\n"
        "s_add_u32 s59, s59, s24\n"
        "v_add_u32 v62, s59, %[vl16]\n"
        "global_load_dwordx4 v[112:115], v62, %[sws] offset:-2048 sc1\n"
        "v_add_u32 v62, 8192, v62\n"
        "global_load_dwordx4 v[116:119], v62, %[sws] offset:-2048 sc1\n"
        "s_waitcnt vmcnt(0)\n"
        "v_max3_u32 v59, v112, v113, v114\n"
        "v_max3_u32 v59, v115, v116, v59\n"
        "v_max3_u32 v59, v117, v118, v59\n"
        "v_max_u32 v59, v119, v59\n"
        "v_cmp_eq_u32 vcc, -1, v59\n"
        "s_cbranch_vccnz Rab10_%=\n"
        "s_branch Cab10_%=\n"
        "Rab11_%=:\n"
        "s_sub_u32 s81, s81, 1\n"
        "s_cbranch_scc1 Cab11_%=\n"
        "s_sleep 1\n"
        "s_bfe_u32 s59, s48, 262148\n"
        "s_lshl_b32 s59, s59, 14\n"
        "s_add_u32 s59, s59, s24\n"
        "v_add_u32 v62, s59, %[vl16]\n"
        "global_load_dwordx4 v[120:123], v62, %[sws] offset:-1024 sc1\n"
        "v_add_u32 v62, 8192, v62\n"
        "global_load_dwordx4 v[124:127], v62, %[sws] offset:-1024 sc1\n"
        "s_waitcnt vmcnt(0)\n"
        "v_max3_u32 v59, v120, v121, v122\n"
        "v_max3_u32 v59, v123, v124, v59\n"
        "v_max3_u32 v59, v125, v126, v59\n"
        "v_max_u32 v59, v127, v59\n"
        "v_cmp_eq_u32 vcc, -1, v59\n"
        "s_cbranch_vccnz Rab11_%=\n"
        "s_branch Cab11_%=\n"
        "Rab12_%=:\n"
        "s_sub_u32 s81, s81, 1\n"
        "s_cbranch_scc1 Cab12_%=\n"
        "s_sleep 1\n"
        "s_bfe_u32 s59, s48, 262148\n"
        "s_lshl_b32 s59, s59, 14\n"
        "s_add_u32 s59, s59, s24\n"
        "v_add_u32 v62, s59, %[vl16]\n"
        "global_load_dwordx4 v[64:67], v62, %[sws] offset:0 sc1\n"
        "v_add_u32 v62, 8192, v62\n"
        "global_load_dwordx4 v[68:71], v62, %[sws] offset:0 sc1\n"
        "s_waitcnt vmcnt(0)\n"
        "v_max3_u32 v59, v64, v65, v66\n"
        "v_max3_u32 v59, v67, v68, v59\n"
        "v_max3_u32 v59, v69, v70, v59\n"
        "v_max_u32 v59, v71, v59\n"
        "v_cmp_eq_u32 vcc, -1, v59\n"
        "s_cbranch_vccnz Rab12_%=\n"
        "s_branch Cab12_%=\n"
        "Rab13_%=:\n"
        "s_sub_u32 s81, s81, 1\n"
        "s_cbranch_scc1 Cab13_%=\n"
        "s_sleep 1\n"
        "s_bfe_u32 s59, s48, 262148\n"
        "s_lshl_b32 s59, s59, 14\n"
        "s_add_u32 s59, s59, s24\n"
        "v_add_u32 v62, s59, %[vl16]\n"
        "global_load_dwordx4 v[72:75], v62, %[sws] offset:1024 sc1\n"
        "v_add_u32 v62, 8192, v62\n"
        "global_load_dwordx4 v[76:79], v62, %[sws] offset:1024 sc1\n"
        "s_waitcnt vmcnt(0)\n"
        "v_max3_u32 v59, v72, v73, v74\n"
        "v_max3_u32 v59, v75, v76, v59\n"
        "v_max3_u32 v59, v77, v78, v59\n"
        "v_max_u32 v59, v79, v59\n"
        "v_cmp_eq_u32 vcc, -1, v59\n"
        "s_cbranch_vccnz Rab13_%=\n"
        "s_branch Cab13_%=\n"
        "Rab14_%=:\n"
        "s_sub_u32 s81, s81, 1\n"
        "s_cbranch_scc1 Cab14_%=\n"
        "s_sleep 1\n"
        "s_bfe_u32 s59, s48, 262148\n"
        "s_lshl_b32 s59, s59, 14\n"
        "s_add_u32 s59, s59, s24\n"
        "v_add_u32 v62, s59, %[vl16]\n"
        "global_load_dwordx4 v[80:83], v62, %[sws] offset:2048 sc1\n"
        "v_add_u32 v62, 8192, v62\n"
        "global_load_dwordx4 v[84:87], v62, %[sws] offset:2048 sc1\n"
        "s_waitcnt vmcnt(0)\n"
        "v_max3_u32 v59, v80, v81, v82\n"
        "v_max3_u32 v59, v83, v84, v59\n"
        "v_max3_u32 v59, v85, v86, v59\n"
        "v_max_u32 v59, v87, v59\n"
        "v_cmp_eq_u32 vcc, -1, v59\n"
        "s_cbranch_vccnz Rab14_%=\n"
        "s_branch Cab14_%=\n"
        "Rab15_%=:\n"
        "s_sub_u32 s81, s81, 1\n"
        "s_cbranch_scc1 Cab15_%=\n"
        "s_sleep 1\n"
        "s_bfe_u32 s59, s48, 262148\n"
        "s_lshl_b32 s59, s59, 14\n"
        "s_add_u32 s59, s59, s24\n"
        "v_add_u32 v62, s59, %[vl16]\n"
        "global_load_dwordx4 v[88:91], v62, %[sws] offset:3072 sc1\n"
        "v_add_u32 v62, 8192, v62\n"
        "global_load_dwordx4 v[92:95], v62, %[sws] offset:3072 sc1\n"
        "s_waitcnt vmcnt(0)\n"
        "v_max3_u32 v59, v88, v89, v90\n"
        "v_max3_u32 v59, v91, v92, v59\n"
        "v_max3_u32 v59, v93, v94, v59\n"
        "v_max_u32 v59, v95, v59\n"
        "v_cmp_eq_u32 vcc, -1, v59\n"
        "s_cbranch_vccnz Rab15_%=\n"
        "s_branch Cab15_%=\n"
        "END_%=:\n"
        : "=&{v[64:95]}"(W0), "+{v[96:127]}"(W1), "=&{v[192:223]}"(P1a), "=&{v[224:255]}"(P1b)
        : [vl16] "v"(l * 16),
          [sws] "s"(ws),
          "{s[56:57]}"(tag),
          "{s[24:39]}"(KP),
          "{s[40:43]}"(LB),
          "{s[44:47]}"(DP),
          "{s[48:55]}"(MP)
        : "scc", "vcc", "memory", "s59", "s60", "s61", "s62", "s63", "s64", "s65", "s81", "s82", "s83", "v31", "v32", "v33", "v34", "v35", "v36", "v37", "v38", "v39", "v40", "v41", "v42", "v43", "v44", "v45", "v46", "v47", "v48", "v49", "v50", "v51", "v52", "v53", "v54", "v55", "v56", "v57", "v58", "v59", "v60", "v61", "v62", "v63", "v128", "v129", "v130", "v131", "v132", "v133", "v134", "v135", "v136", "v137", "v138", "v139", "v140", "v141", "v142", "v143", "v144", "v145", "v146", "v147", "v148", "v149", "v150", "v151", "v152", "v153", "v154", "v155", "v156", "v157", "v158", "v159", "v160", "v161", "v162", "v163", "v164", "v165", "v166", "v167", "v168", "v169", "v170", "v171", "v172", "v173", "v174", "v175", "v176", "v177", "v178", "v179", "v180", "v181", "v182", "v183", "v184", "v185", "v186", "v187", "v188", "v189", "v190", "v191", "a0", "a1", "a2", "a3", "a4", "a5", "a6", "a7", "a8", "a9", "a10", "a11", "a12", "a13", "a14", "a15", "a16", "a17", "a18", "a19", "a20", "a21", "a22", "a23", "a24", "a25", "a26", "a27", "a28", "a29", "a30", "a31", "a32", "a33", "a34", "a35", "a36", "a37", "a38", "a39", "a40", "a41", "a42", "a43", "a44", "a45", "a46", "a47", "a48", "a49", "a50", "a51", "a52", "a53", "a54", "a55", "a56", "a57", "a58", "a59", "a60", "a61", "a62", "a63", "a64", "a65", "a66", "a67", "a68", "a69", "a70", "a71", "a72", "a73", "a74", "a75", "a76", "a77", "a78", "a79", "a80", "a81", "a82", "a83", "a84", "a85", "a86", "a87", "a88", "a89", "a90", "a91", "a92", "a93", "a94", "a95", "a96", "a97", "a98", "a99", "a100", "a101", "a102", "a103", "a104", "a105", "a106", "a107", "a108", "a109", "a110", "a111", "a112", "a113", "a114", "a115", "a116", "a117", "a118", "a119", "a120", "a121", "a122", "a123", "a124", "a125", "a126", "a127", "a128", "a129", "a130", "a131", "a132", "a133", "a134", "a135", "a136", "a137", "a138", "a139", "a140", "a141", "a142", "a143", "a144", "a145", "a146", "a147", "a148", "a149", "a150", "a151", "a152", "a153", "a154", "a155", "a156", "a157", "a158", "a159", "a160", "a161", "a162", "a163", "a164", "a165", "a166", "a167", "a168", "a169", "a170", "a171", "a172", "a173", "a174", "a175", "a176", "a177", "a178", "a179", "a180", "a181", "a182", "a183", "a184", "a185", "a186", "a187", "a188", "a189", "a190", "a191", "a192", "a193", "a194", "a195", "a196", "a197", "a198", "a199", "a200", "a201", "a202", "a203", "a204", "a205", "a206", "a207", "a208", "a209", "a210", "a211", "a212", "a213", "a214", "a215", "a216", "a217", "a218", "a219", "a220", "a221", "a222", "a223", "a224", "a225", "a226", "a227", "a228", "a229", "a230", "a231", "a232", "a233", "a234", "a235", "a236", "a237", "a238", "a239", "a240", "a241", "a242", "a243", "a244", "a245", "a246", "a247", "a248", "a249", "a250", "a251", "a252", "a253", "a254", "a255");
    LANE();
    STAMP(7);
    if (f2 >= 0) {
        unsigned ix;
        SPIN(n) {
            ix = ld4(R, A_IDX + (l & 7) * 4);
            if (__all(ix != SENT) || giveup(n)) break;
        }
        if (w == 0 && l < 8) ldsw<unsigned>(T_SEL + l * 4, ix & 2047u);
        bar();
        ffn2<0>(R, f2, LD, nullptr, w, l);
    }
    LANE();
    STAMP(8);
    asm volatile(
        "s_nop 4\n"
        "s_mov_b32 s81, 0x40000\n"
        "v_add_u32 v58, s54, %[vl16]\n"
        "v_lshrrev_b32 v59, 1, %[vl16]\n"
        "ds_read_b128 v[32:35], v58\n"
        "v_add_u32 v59, 156224, v59\n"
        "ds_read_b64 v[40:41], v59\n"
        "ds_read_b64 v[44:45], v59\n"
        "ds_read_b64 v[46:47], v59\n"
        "v_add_u32 v58, 161408, %[vl16]\n"
        "ds_read_b128 v[36:39], v58\n"
        "v_mov_b32 v42, 0\n"
        "v_mov_b32 v43, 0\n"
        "v_add_u32 v48, s40, %[vl16]\n"
        "v_add_u32 v49, s41, %[vl16]\n"
        "v_add_u32 v50, s42, %[vl16]\n"
        "v_add_u32 v51, s43, %[vl16]\n"
        "v_mov_b32 v63, s57\n"
        "s_waitcnt lgkmcnt(0)\n"
        "v_add_u32 v32, 0x20000, v32\n"
        "v_add_u32 v34, 0x20000, v34\n"
        "s_bfe_u32 s59, s48, 262152\n"
        "s_lshl_b32 s59, s59, 14\n"
        "s_add_u32 s59, s59, s24\n"
        "v_add_u32 v56, s59, %[vl16]\n"
        "v_add_u32 v57, 8192, v56\n"
        "s_waitcnt vmcnt(18)\n"
        "v_max3_u32 v59, v96, v97, v98\n"
        "v_max3_u32 v59, v99, v100, v59\n"
        "v_max3_u32 v59, v101, v102, v59\n"
        "v_max_u32 v59, v103, v59\n"
        "v_cmp_eq_u32 vcc, -1, v59\n"
        "s_cbranch_vccnz Rab16_%=\n"
        "Cab16_%=:\n"
        "v_mfma_f32_16x16x32_bf16 v[128:131], v[96:99], a[0:3], 0\n"
        "v_mfma_f32_16x16x32_bf16 v[132:135], v[100:103], a[0:3], 0\n"
        "v_mfma_f32_16x16x32_bf16 v[136:139], v[96:99], a[4:7], 0\n"
        "v_cvt_pk_bf16_f32 v208, v208, v209\n"
        "v_cvt_pk_bf16_f32 v209, v210, v211\n"
        "v_mfma_f32_16x16x32_bf16 v[140:143], v[100:103], a[4:7], 0\n"
        "v_cvt_pk_bf16_f32 v210, v212, v213\n"
        "v_cvt_pk_bf16_f32 v211, v214, v215\n"
        "v_mfma_f32_16x16x32_bf16 v[144:147], v[96:99], a[8:11], 0\n"
        "ds_write_b128 v48, v[208:211] offset:0\n"
        "v_mfma_f32_16x16x32_bf16 v[148:151], v[100:103], a[8:11], 0\n"
        "v_cvt_pk_bf16_f32 v216, v216, v217\n"
        "v_cvt_pk_bf16_f32 v217, v218, v219\n"
        "v_mfma_f32_16x16x32_bf16 v[152:155], v[96:99], a[12:15], 0\n"
        "v_cvt_pk_bf16_f32 v218, v220, v221\n"
        "v_cvt_pk_bf16_f32 v219, v222, v223\n"
        "v_mfma_f32_16x16x32_bf16 v[156:159], v[100:103], a[12:15], 0\n"
        "ds_write_b128 v48, v[216:219] offset:1024\n"
        "v_mfma_f32_16x16x32_bf16 v[160:163], v[96:99], a[16:19], 0\n"
        "v_cvt_pk_bf16_f32 v224, v224, v225\n"
        "v_cvt_pk_bf16_f32 v225, v226, v227\n"
        "v_mfma_f32_16x16x32_bf16 v[164:167], v[100:103], a[16:19], 0\n"
        "v_cvt_pk_bf16_f32 v226, v228, v229\n"
        "v_cvt_pk_bf16_f32 v227, v230, v231\n"
        "v_mfma_f32_16x16x32_bf16 v[168:171], v[96:99], a[20:23], 0\n"
        "ds_write_b128 v49, v[224:227] offset:0\n"
        "v_mfma_f32_16x16x32_bf16 v[172:175], v[100:103], a[20:23], 0\n"
        "v_cvt_pk_bf16_f32 v232, v232, v233\n"
        "v_cvt_pk_bf16_f32 v233, v234, v235\n"
        "v_mfma_f32_16x16x32_bf16 v[176:179], v[96:99], a[24:27], 0\n"
        "v_cvt_pk_bf16_f32 v234, v236, v237\n"
        "v_cvt_pk_bf16_f32 v235, v238, v239\n"
        "v_mfma_f32_16x16x32_bf16 v[180:183], v[100:103], a[24:27], 0\n"
        "ds_write_b128 v49, v[232:235] offset:1024\n"
        "v_mfma_f32_16x16x32_bf16 v[184:187], v[96:99], a[28:31], 0\n"
        "v_cvt_pk_bf16_f32 v240, v240, v241\n"
        "v_cvt_pk_bf16_f32 v241, v242, v243\n"
        "global_load_dwordx4 v[88:91], v56, %[sws] offset:3072 sc1\n"
        "v_mfma_f32_16x16x32_bf16 v[188:191], v[100:103], a[28:31], 0\n"
        "v_cvt_pk_bf16_f32 v242, v244, v245\n"
        "v_cvt_pk_bf16_f32 v243, v246, v247\n"
        "global_load_dwordx4 v[92:95], v57, %[sws] offset:3072 sc1\n"
        "s_waitcnt vmcnt(18)\n"
        "v_max3_u32 v59, v104, v105, v106\n"
        "v_max3_u32 v59, v107, v108, v59\n"
        "v_max3_u32 v59, v109, v110, v59\n"
        "v_max_u32 v59, v111, v59\n"
        "v_cmp_eq_u32 vcc, -1, v59\n"
        "s_cbranch_vccnz Rab17_%=\n"
        "Cab17_%=:\n"
        "v_mfma_f32_16x16x32_bf16 v[128:131], v[104:107], a[32:35], v[128:131]\n"
        "ds_write_b128 v50, v[240:243] offset:0\n"
        "v_mfma_f32_16x16x32_bf16 v[132:135], v[108:111], a[32:35], v[132:135]\n"
        "v_cvt_pk_bf16_f32 v248, v248, v249\n"
        "v_cvt_pk_bf16_f32 v249, v250, v251\n"
        "v_mfma_f32_16x16x32_bf16 v[136:139], v[104:107], a[36:39], v[136:139]\n"
        "v_cvt_pk_bf16_f32 v250, v252, v253\n"
        "v_cvt_pk_bf16_f32 v251, v254, v255\n"
        "v_mfma_f32_16x16x32_bf16 v[140:143], v[108:111], a[36:39], v[140:143]\n"
        "ds_write_b128 v50, v[248:251] offset:1024\n"
        "v_mfma_f32_16x16x32_bf16 v[144:147], v[104:107], a[40:43], v[144:147]\n"
        "v_mfma_f32_16x16x32_bf16 v[148:151], v[108:111], a[40:43], v[148:151]\n"
        "v_mfma_f32_16x16x32_bf16 v[152:155], v[104:107], a[44:47], v[152:155]\n"
        "s_waitcnt lgkmcnt(0)\n"
        "s_barrier\n"
        "v_mfma_f32_16x16x32_bf16 v[156:159], v[108:111], a[44:47], v[156:159]\n"
        "ds_read_b128 v[208:211], v51 offset:0\n"
        "v_mfma_f32_16x16x32_bf16 v[160:163], v[104:107], a[48:51], v[160:163]\n"
        "ds_read_b128 v[212:215], v51 offset:1024\n"
        "v_mfma_f32_16x16x32_bf16 v[164:167], v[108:111], a[48:51], v[164:167]\n"
        "ds_read_b128 v[216:219], v51 offset:2048\n"
        "v_mfma_f32_16x16x32_bf16 v[168:171], v[104:107], a[52:55], v[168:171]\n"
        "ds_read_b128 v[220:223], v51 offset:3072\n"
        "v_mfma_f32_16x16x32_bf16 v[172:175], v[108:111], a[52:55], v[172:175]\n"
        "ds_read_b128 v[224:227], v51 offset:4096\n"
        "v_mfma_f32_16x16x32_bf16 v[176:179], v[104:107], a[56:59], v[176:179]\n"
        "ds_read_b128 v[228:231], v51 offset:5120\n"
        "v_mfma_f32_16x16x32_bf16 v[180:183], v[108:111], a[56:59], v[180:183]\n"
        "v_mfma_f32_16x16x32_bf16 v[184:187], v[104:107], a[60:63], v[184:187]\n"
        "s_waitcnt vmcnt(14)\n"
        "v_mov_b32 v62, s56\n"
        "v_add_u32 v62, 1, v62\n"
        "s_mov_b64 exec, 1\n"
        "v_mov_b32 v31, s34\n"
        "global_store_dwordx2 v31, v[62:63], %[sws] sc1\n"
        "s_mov_b64 exec, -1\n"
        "s_bfe_u32 s59, s48, 262156\n"
        "s_lshl_b32 s59, s59, 14\n"
        "s_add_u32 s59, s59, s24\n"
        "v_add_u32 v56, s59, %[vl16]\n"
        "v_add_u32 v57, 8192, v56\n"
        "global_load_dwordx4 v[96:99], v56, %[sws] offset:-4096 sc1\n"
        "v_mfma_f32_16x16x32_bf16 v[188:191], v[108:111], a[60:63], v[188:191]\n"
        "global_load_dwordx4 v[100:103], v57, %[sws] offset:-4096 sc1\n"
        "s_waitcnt vmcnt(15)\n"
        "v_max3_u32 v59, v112, v113, v114\n"
        "v_max3_u32 v59, v115, v116, v59\n"
        "v_max3_u32 v59, v117, v118, v59\n"
        "v_max_u32 v59, v119, v59\n"
        "v_cmp_eq_u32 vcc, -1, v59\n"
        "s_cbranch_vccnz Rab18_%=\n"
        "Cab18_%=:\n"
        "v_mfma_f32_16x16x32_bf16 v[128:131], v[112:115], a[64:67], v[128:131]\n"
        "s_waitcnt lgkmcnt(5)\n"
        "v_mfma_f32_16x16x32_bf16 v[192:195], v[40:43], v[208:211], v[192:195]\n"
        "v_mfma_f32_16x16x32_bf16 v[132:135], v[116:119], a[64:67], v[132:135]\n"
        "v_mfma_f32_16x16x32_bf16 v[196:199], v[42:45], v[208:211], v[196:199]\n"
        "v_mfma_f32_16x16x32_bf16 v[136:139], v[112:115], a[68:71], v[136:139]\n"
        "s_waitcnt lgkmcnt(4)\n"
        "v_mfma_f32_16x16x32_bf16 v[200:203], v[40:43], v[212:215], v[200:203]\n"
        "v_mfma_f32_16x16x32_bf16 v[140:143], v[116:119], a[68:71], v[140:143]\n"
        "v_mfma_f32_16x16x32_bf16 v[204:207], v[42:45], v[212:215], v[204:207]\n"
        "v_mfma_f32_16x16x32_bf16 v[144:147], v[112:115], a[72:75], v[144:147]\n"
        "s_waitcnt lgkmcnt(3)\n"
        "v_mfma_f32_16x16x32_bf16 v[192:195], v[40:43], v[216:219], v[192:195]\n"
        "v_mfma_f32_16x16x32_bf16 v[148:151], v[116:119], a[72:75], v[148:151]\n"
        "v_mfma_f32_16x16x32_bf16 v[196:199], v[42:45], v[216:219], v[196:199]\n"
        "v_mfma_f32_16x16x32_bf16 v[152:155], v[112:115], a[76:79], v[152:155]\n"
        "s_waitcnt lgkmcnt(2)\n"
        "v_mfma_f32_16x16x32_bf16 v[200:203], v[40:43], v[220:223], v[200:203]\n"
        "v_mfma_f32_16x16x32_bf16 v[156:159], v[116:119], a[76:79], v[156:159]\n"
        "v_mfma_f32_16x16x32_bf16 v[204:207], v[42:45], v[220:223], v[204:207]\n"
        "v_mfma_f32_16x16x32_bf16 v[160:163], v[112:115], a[80:83], v[160:163]\n"
        "s_waitcnt lgkmcnt(1)\n"
        "v_mfma_f32_16x16x32_bf16 v[192:195], v[40:43], v[224:227], v[192:195]\n"
        "v_mfma_f32_16x16x32_bf16 v[164:167], v[116:119], a[80:83], v[164:167]\n"
        "v_mfma_f32_16x16x32_bf16 v[196:199], v[42:45], v[224:227], v[196:199]\n"
        "v_mfma_f32_16x16x32_bf16 v[168:171], v[112:115], a[84:87], v[168:171]\n"
        "s_waitcnt lgkmcnt(0)\n"
        "v_mfma_f32_16x16x32_bf16 v[200:203], v[40:43], v[228:231], v[200:203]\n"
        "v_mfma_f32_16x16x32_bf16 v[172:175], v[116:119], a[84:87], v[172:175]\n"
        "v_mfma_f32_16x16x32_bf16 v[204:207], v[42:45], v[228:231], v[204:207]\n"
        "v_mfma_f32_16x16x32_bf16 v[176:179], v[112:115], a[88:91], v[176:179]\n"
        "s_barrier\n"
        "v_mfma_f32_16x16x32_bf16 v[180:183], v[116:119], a[88:91], v[180:183]\n"
        "v_mfma_f32_16x16x32_bf16 v[184:187], v[112:115], a[92:95], v[184:187]\n"
        "global_load_dwordx4 v[104:107], v56, %[sws] offset:-3072 sc1\n"
        "v_mfma_f32_16x16x32_bf16 v[188:191], v[116:119], a[92:95], v[188:191]\n"
        "global_load_dwordx4 v[108:111], v57, %[sws] offset:-3072 sc1\n"
        "s_waitcnt vmcnt(14)\n"
        "v_max3_u32 v59, v120, v121, v122\n"
        "v_max3_u32 v59, v123, v124, v59\n"
        "v_max3_u32 v59, v125, v126, v59\n"
        "v_max_u32 v59, v127, v59\n"
        "v_cmp_eq_u32 vcc, -1, v59\n"
        "s_cbranch_vccnz Rab19_%=\n"
        "Cab19_%=:\n"
        "v_mfma_f32_16x16x32_bf16 v[128:131], v[120:123], a[96:99], v[128:131]\n"
        "v_mfma_f32_16x16x32_bf16 v[132:135], v[124:127], a[96:99], v[132:135]\n"
        "v_mfma_f32_16x16x32_bf16 v[136:139], v[120:123], a[100:103], v[136:139]\n"
        "v_cvt_pk_bf16_f32 v208, v192, v193\n"
        "v_cvt_pk_bf16_f32 v209, v194, v195\n"
        "v_mfma_f32_16x16x32_bf16 v[140:143], v[124:127], a[100:103], v[140:143]\n"
        "v_cvt_pk_bf16_f32 v210, v196, v197\n"
        "v_cvt_pk_bf16_f32 v211, v198, v199\n"
        "v_mfma_f32_16x16x32_bf16 v[144:147], v[120:123], a[104:107], v[144:147]\n"
        "v_cvt_pk_bf16_f32 v212, v200, v201\n"
        "v_cvt_pk_bf16_f32 v213, v202, v203\n"
        "v_mfma_f32_16x16x32_bf16 v[148:151], v[124:127], a[104:107], v[148:151]\n"
        "v_cvt_pk_bf16_f32 v214, v204, v205\n"
        "v_cvt_pk_bf16_f32 v215, v206, v207\n"
        "v_add_u32 v58, s45, %[vl16]\n"
        "v_mfma_f32_16x16x32_bf16 v[152:155], v[120:123], a[108:111], v[152:155]\n"
        "v_mfma_f32_16x16x32_bf16 v[156:159], v[124:127], a[108:111], v[156:159]\n"
        "global_store_dwordx4 v58, v[208:211], %[sws] sc1\n"
        "v_mfma_f32_16x16x32_bf16 v[160:163], v[120:123], a[112:115], v[160:163]\n"
        "v_mfma_f32_16x16x32_bf16 v[164:167], v[124:127], a[112:115], v[164:167]\n"
        "global_store_dwordx4 v58, v[212:215], %[sws] offset:1024 sc1\n"
        "v_mfma_f32_16x16x32_bf16 v[168:171], v[120:123], a[116:119], v[168:171]\n"
        "v_mfma_f32_16x16x32_bf16 v[172:175], v[124:127], a[116:119], v[172:175]\n"
        "v_mfma_f32_16x16x32_bf16 v[176:179], v[120:123], a[120:123], v[176:179]\n"
        "v_mfma_f32_16x16x32_bf16 v[180:183], v[124:127], a[120:123], v[180:183]\n"
        "v_mfma_f32_16x16x32_bf16 v[184:187], v[120:123], a[124:127], v[184:187]\n"
        "global_load_dwordx4 v[112:115], v56, %[sws] offset:-2048 sc1\n"
        "v_mfma_f32_16x16x32_bf16 v[188:191], v[124:127], a[124:127], v[188:191]\n"
        "global_load_dwordx4 v[116:119], v57, %[sws] offset:-2048 sc1\n"
        "s_waitcnt vmcnt(16)\n"
        "v_max3_u32 v59, v64, v65, v66\n"
        "v_max3_u32 v59, v67, v68, v59\n"
        "v_max3_u32 v59, v69, v70, v59\n"
        "v_max_u32 v59, v71, v59\n"
        "v_cmp_eq_u32 vcc, -1, v59\n"
        "s_cbranch_vccnz Rab20_%=\n"
        "Cab20_%=:\n"
        "v_mfma_f32_16x16x32_bf16 v[128:131], v[64:67], a[128:131], v[128:131]\n"
        "v_mfma_f32_16x16x32_bf16 v[132:135], v[68:71], a[128:131], v[132:135]\n"
        "v_mfma_f32_16x16x32_bf16 v[136:139], v[64:67], a[132:135], v[136:139]\n"
        "v_mfma_f32_16x16x32_bf16 v[140:143], v[68:71], a[132:135], v[140:143]\n"
        "v_mfma_f32_16x16x32_bf16 v[144:147], v[64:67], a[136:139], v[144:147]\n"
        "v_mfma_f32_16x16x32_bf16 v[148:151], v[68:71], a[136:139], v[148:151]\n"
        "v_mfma_f32_16x16x32_bf16 v[152:155], v[64:67], a[140:143], v[152:155]\n"
        "v_mfma_f32_16x16x32_bf16 v[156:159], v[68:71], a[140:143], v[156:159]\n"
        "v_mfma_f32_16x16x32_bf16 v[160:163], v[64:67], a[144:147], v[160:163]\n"
        "v_mfma_f32_16x16x32_bf16 v[164:167], v[68:71], a[144:147], v[164:167]\n"
        "v_mfma_f32_16x16x32_bf16 v[168:171], v[64:67], a[148:151], v[168:171]\n"
        "v_mfma_f32_16x16x32_bf16 v[172:175], v[68:71], a[148:151], v[172:175]\n"
        "v_mfma_f32_16x16x32_bf16 v[176:179], v[64:67], a[152:155], v[176:179]\n"
        "v_mfma_f32_16x16x32_bf16 v[180:183], v[68:71], a[152:155], v[180:183]\n"
        "v_mfma_f32_16x16x32_bf16 v[184:187], v[64:67], a[156:159], v[184:187]\n"
        "global_load_dwordx4 v[120:123], v56, %[sws] offset:-1024 sc1\n"
        "v_mfma_f32_16x16x32_bf16 v[188:191], v[68:71], a[156:159], v[188:191]\n"
        "global_load_dwordx4 v[124:127], v57, %[sws] offset:-1024 sc1\n"
        "s_waitcnt vmcnt(15)\n"
        "v_max3_u32 v59, v72, v73, v74\n"
        "v_max3_u32 v59, v75, v76, v59\n"
        "v_max3_u32 v59, v77, v78, v59\n"
        "v_max_u32 v59, v79, v59\n"
        "v_cmp_eq_u32 vcc, -1, v59\n"
        "s_cbranch_vccnz Rab21_%=\n"
        "Cab21_%=:\n"
        "v_mfma_f32_16x16x32_bf16 v[128:131], v[72:75], a[160:163], v[128:131]\n"
        "v_mfma_f32_16x16x32_bf16 v[132:135], v[76:79], a[160:163], v[132:135]\n"
        "v_mfma_f32_16x16x32_bf16 v[136:139], v[72:75], a[164:167], v[136:139]\n"
        "v_mfma_f32_16x16x32_bf16 v[140:143], v[76:79], a[164:167], v[140:143]\n"
        "v_mfma_f32_16x16x32_bf16 v[144:147], v[72:75], a[168:171], v[144:147]\n"
        "v_mfma_f32_16x16x32_bf16 v[148:151], v[76:79], a[168:171], v[148:151]\n"
        "v_mfma_f32_16x16x32_bf16 v[152:155], v[72:75], a[172:175], v[152:155]\n"
        "v_mfma_f32_16x16x32_bf16 v[156:159], v[76:79], a[172:175], v[156:159]\n"
        "v_mfma_f32_16x16x32_bf16 v[160:163], v[72:75], a[176:179], v[160:163]\n"
        "v_mfma_f32_16x16x32_bf16 v[164:167], v[76:79], a[176:179], v[164:167]\n"
        "v_mfma_f32_16x16x32_bf16 v[168:171], v[72:75], a[180:183], v[168:171]\n"
        "v_mfma_f32_16x16x32_bf16 v[172:175], v[76:79], a[180:183], v[172:175]\n"
        "v_mfma_f32_16x16x32_bf16 v[176:179], v[72:75], a[184:187], v[176:179]\n"
        "v_mfma_f32_16x16x32_bf16 v[180:183], v[76:79], a[184:187], v[180:183]\n"
        "v_mfma_f32_16x16x32_bf16 v[184:187], v[72:75], a[188:191], v[184:187]\n"
        "global_load_dwordx4 v[64:67], v56, %[sws] offset:0 sc1\n"
        "v_mfma_f32_16x16x32_bf16 v[188:191], v[76:79], a[188:191], v[188:191]\n"
        "global_load_dwordx4 v[68:71], v57, %[sws] offset:0 sc1\n"
        "s_waitcnt vmcnt(15)\n"
        "v_max3_u32 v59, v80, v81, v82\n"
        "v_max3_u32 v59, v83, v84, v59\n"
        "v_max3_u32 v59, v85, v86, v59\n"
        "v_max_u32 v59, v87, v59\n"
        "v_cmp_eq_u32 vcc, -1, v59\n"
        "s_cbranch_vccnz Rab22_%=\n"
        "Cab22_%=:\n"
        "v_mfma_f32_16x16x32_bf16 v[128:131], v[80:83], a[192:195], v[128:131]\n"
        "v_mfma_f32_16x16x32_bf16 v[132:135], v[84:87], a[192:195], v[132:135]\n"
        "v_mfma_f32_16x16x32_bf16 v[136:139], v[80:83], a[196:199], v[136:139]\n"
        "v_mfma_f32_16x16x32_bf16 v[140:143], v[84:87], a[196:199], v[140:143]\n"
        "v_mfma_f32_16x16x32_bf16 v[144:147], v[80:83], a[200:203], v[144:147]\n"
        "v_mfma_f32_16x16x32_bf16 v[148:151], v[84:87], a[200:203], v[148:151]\n"
        "v_mfma_f32_16x16x32_bf16 v[152:155], v[80:83], a[204:207], v[152:155]\n"
        "v_mfma_f32_16x16x32_bf16 v[156:159], v[84:87], a[204:207], v[156:159]\n"
        "v_mfma_f32_16x16x32_bf16 v[160:163], v[80:83], a[208:211], v[160:163]\n"
        "v_mfma_f32_16x16x32_bf16 v[164:167], v[84:87], a[208:211], v[164:167]\n"
        "v_mfma_f32_16x16x32_bf16 v[168:171], v[80:83], a[212:215], v[168:171]\n"
        "v_mfma_f32_16x16x32_bf16 v[172:175], v[84:87], a[212:215], v[172:175]\n"
        "v_mfma_f32_16x16x32_bf16 v[176:179], v[80:83], a[216:219], v[176:179]\n"
        "v_mfma_f32_16x16x32_bf16 v[180:183], v[84:87], a[216:219], v[180:183]\n"
        "v_mfma_f32_16x16x32_bf16 v[184:187], v[80:83], a[220:223], v[184:187]\n"
        "global_load_dwordx4 v[72:75], v56, %[sws] offset:1024 sc1\n"
        "v_mfma_f32_16x16x32_bf16 v[188:191], v[84:87], a[220:223], v[188:191]\n"
        "global_load_dwordx4 v[76:79], v57, %[sws] offset:1024 sc1\n"
        "s_waitcnt vmcnt(15)\n"
        "v_max3_u32 v59, v88, v89, v90\n"
        "v_max3_u32 v59, v91, v92, v59\n"
        "v_max3_u32 v59, v93, v94, v59\n"
        "v_max_u32 v59, v95, v59\n"
        "v_cmp_eq_u32 vcc, -1, v59\n"
        "s_cbranch_vccnz Rab23_%=\n"
        "Cab23_%=:\n"
        "v_mfma_f32_16x16x32_bf16 v[128:131], v[88:91], a[224:227], v[128:131]\n"
        "v_mfma_f32_16x16x32_bf16 v[132:135], v[92:95], a[224:227], v[132:135]\n"
        "v_mfma_f32_16x16x32_bf16 v[136:139], v[88:91], a[228:231], v[136:139]\n"
        "v_mfma_f32_16x16x32_bf16 v[140:143], v[92:95], a[228:231], v[140:143]\n"
        "v_mfma_f32_16x16x32_bf16 v[144:147], v[88:91], a[232:235], v[144:147]\n"
        "v_mfma_f32_16x16x32_bf16 v[148:151], v[92:95], a[232:235], v[148:151]\n"
        "v_mfma_f32_16x16x32_bf16 v[152:155], v[88:91], a[236:239], v[152:155]\n"
        "v_mfma_f32_16x16x32_bf16 v[156:159], v[92:95], a[236:239], v[156:159]\n"
        "v_mfma_f32_16x16x32_bf16 v[160:163], v[88:91], a[240:243], v[160:163]\n"
        "v_mfma_f32_16x16x32_bf16 v[164:167], v[92:95], a[240:243], v[164:167]\n"
        "v_mfma_f32_16x16x32_bf16 v[168:171], v[88:91], a[244:247], v[168:171]\n"
        "v_mfma_f32_16x16x32_bf16 v[172:175], v[92:95], a[244:247], v[172:175]\n"
        "v_mfma_f32_16x16x32_bf16 v[176:179], v[88:91], a[248:251], v[176:179]\n"
        "v_mfma_f32_16x16x32_bf16 v[180:183], v[92:95], a[248:251], v[180:183]\n"
        "v_mfma_f32_16x16x32_bf16 v[184:187], v[88:91], a[252:255], v[184:187]\n"
        "global_load_dwordx4 v[80:83], v56, %[sws] offset:2048 sc1\n"
        "v_mfma_f32_16x16x32_bf16 v[188:191], v[92:95], a[252:255], v[188:191]\n"
        "global_load_dwordx4 v[84:87], v57, %[sws] offset:2048 sc1\n"
        "s_waitcnt lgkmcnt(0)\n"
        "s_nop 7\n"
        "s_branch END_%=\n"
        "Rab16_%=:\n"
        "s_sub_u32 s81, s81, 1\n"
        "s_cbranch_scc1 Cab16_%=\n"
        "s_sleep 1\n"
        "s_bfe_u32 s59, s48, 262152\n"
        "s_lshl_b32 s59, s59, 14\n"
        "s_add_u32 s59, s59, s24\n"
        "v_add_u32 v62, s59, %[vl16]\n"
        "global_load_dwordx4 v[96:99], v62, %[sws] offset:-4096 sc1\n"
        "v_add_u32 v62, 8192, v62\n"
        "global_load_dwordx4 v[100:103], v62, %[sws] offset:-4096 sc1\n"
        "s_waitcnt vmcnt(0)\n"
        "v_max3_u32 v59, v96, v97, v98\n"
        "v_max3_u32 v59, v99, v100, v59\n"
        "v_max3_u32 v59, v101, v102, v59\n"
        "v_max_u32 v59, v103, v59\n"
        "v_cmp_eq_u32 vcc, -1, v59\n"
        "s_cbranch_vccnz Rab16_%=\n"
        "s_branch Cab16_%=\n"
        "Rab17_%=:\n"
        "s_sub_u32 s81, s81, 1\n"
        "s_cbranch_scc1 Cab17_%=\n"
        "s_sleep 1\n"
        "s_bfe_u32 s59, s48, 262152\n"
        "s_lshl_b32 s59, s59, 14\n"
        "s_add_u32 s59, s59, s24\n"
        "v_add_u32 v62, s59, %[vl16]\n"
        "global_load_dwordx4 v[104:107], v62, %[sws] offset:-3072 sc1\n"
        "v_add_u32 v62, 8192, v62\n"
        "global_load_dwordx4 v[108:111], v62, %[sws] offset:-3072 sc1\n"
        "s_waitcnt vmcnt(0)\n"
        "v_max3_u32 v59, v104, v105, v106\n"
        "v_max3_u32 v59, v107, v108, v59\n"
        "v_max3_u32 v59, v109, v110, v59\n"
        "v_max_u32 v59, v111, v59\n"
        "v_cmp_eq_u32 vcc, -1, v59\n"
        "s_cbranch_vccnz Rab17_%=\n"
        "s_branch Cab17_%=\n"
        "Rab18_%=:\n"
        "s_sub_u32 s81, s81, 1\n"
        "s_cbranch_scc1 Cab18_%=\n"
        "s_sleep 1\n"
        "s_bfe_u32 s59, s48, 262152\n"
        "s_lshl_b32 s59, s59, 14\n"
        "s_add_u32 s59, s59, s24\n"
        "v_add_u32 v62, s59, %[vl16]\n"
        "global_load_dwordx4 v[112:115], v62, %[sws] offset:-2048 sc1\n"
        "v_add_u32 v62, 8192, v62\n"
        "global_load_dwordx4 v[116:119], v62, %[sws] offset:-2048 sc1\n"
        "s_waitcnt vmcnt(0)\n"
        "v_max3_u32 v59, v112, v113, v114\n"
        "v_max3_u32 v59, v115, v116, v59\n"
        "v_max3_u32 v59, v117, v118, v59\n"
        "v_max_u32 v59, v119, v59\n"
        "v_cmp_eq_u32 vcc, -1, v59\n"
        "s_cbranch_vccnz Rab18_%=\n"
        "s_branch Cab18_%=\n"
        "Rab19_%=:\n"
        "s_sub_u32 s81, s81, 1\n"
        "s_cbranch_scc1 Cab19_%=\n"
        "s_sleep 1\n"
        "s_bfe_u32 s59, s48, 262152\n"
        "s_lshl_b32 s59, s59, 14\n"
        "s_add_u32 s59, s59, s24\n"
        "v_add_u32 v62, s59, %[vl16]\n"
        "global_load_dwordx4 v[120:123], v62, %[sws] offset:-1024 sc1\n"
        "v_add_u32 v62, 8192, v62\n"
        "global_load_dwordx4 v[124:127], v62, %[sws] offset:-1024 sc1\n"
        "s_waitcnt vmcnt(0)\n"
        "v_max3_u32 v59, v120, v121, v122\n"
        "v_max3_u32 v59, v123, v124, v59\n"
        "v_max3_u32 v59, v125, v126, v59\n"
        "v_max_u32 v59, v127, v59\n"
        "v_cmp_eq_u32 vcc, -1, v59\n"
        "s_cbranch_vccnz Rab19_%=\n"
        "s_branch Cab19_%=\n"
        "Rab20_%=:\n"
        "s_sub_u32 s81, s81, 1\n"
        "s_cbranch_scc1 Cab20_%=\n"
        "s_sleep 1\n"
        "s_bfe_u32 s59, s48, 262152\n"
        "s_lshl_b32 s59, s59, 14\n"
        "s_add_u32 s59, s59, s24\n"
        "v_add_u32 v62, s59, %[vl16]\n"
        "global_load_dwordx4 v[64:67], v62, %[sws] offset:0 sc1\n"
        "v_add_u32 v62, 8192, v62\n"
        "global_load_dwordx4 v[68:71], v62, %[sws] offset:0 sc1\n"
        "s_waitcnt vmcnt(0)\n"
        "v_max3_u32 v59, v64, v65, v66\n"
        "v_max3_u32 v59, v67, v68, v59\n"
        "v_max3_u32 v59, v69, v70, v59\n"
        "v_max_u32 v59, v71, v59\n"
        "v_cmp_eq_u32 vcc, -1, v59\n"
        "s_cbranch_vccnz Rab20_%=\n"
        "s_branch Cab20_%=\n"
        "Rab21_%=:\n"
        "s_sub_u32 s81, s81, 1\n"
        "s_cbranch_scc1 Cab21_%=\n"
        "s_sleep 1\n"
        "s_bfe_u32 s59, s48, 262152\n"
        "s_lshl_b32 s59, s59, 14\n"
        "s_add_u32 s59, s59, s24\n"
        "v_add_u32 v62, s59, %[vl16]\n"
        "global_load_dwordx4 v[72:75], v62, %[sws] offset:1024 sc1\n"
        "v_add_u32 v62, 8192, v62\n"
        "global_load_dwordx4 v[76:79], v62, %[sws] offset:1024 sc1\n"
        "s_waitcnt vmcnt(0)\n"
        "v_max3_u32 v59, v72, v73, v74\n"
        "v_max3_u32 v59, v75, v76, v59\n"
        "v_max3_u32 v59, v77, v78, v59\n"
        "v_max_u32 v59, v79, v59\n"
        "v_cmp_eq_u32 vcc, -1, v59\n"
        "s_cbranch_vccnz Rab21_%=\n"
        "s_branch Cab21_%=\n"
        "Rab22_%=:\n"
        "s_sub_u32 s81, s81, 1\n"
        "s_cbranch_scc1 Cab22_%=\n"
        "s_sleep 1\n"
        "s_bfe_u32 s59, s48, 262152\n"
        "s_lshl_b32 s59, s59, 14\n"
        "s_add_u32 s59, s59, s24\n"
        "v_add_u32 v62, s59, %[vl16]\n"
        "global_load_dwordx4 v[80:83], v62, %[sws] offset:2048 sc1\n"
        "v_add_u32 v62, 8192, v62\n"
        "global_load_dwordx4 v[84:87], v62, %[sws] offset:2048 sc1\n"
        "s_waitcnt vmcnt(0)\n"
        "v_max3_u32 v59, v80, v81, v82\n"
        "v_max3_u32 v59, v83, v84, v59\n"
        "v_max3_u32 v59, v85, v86, v59\n"
        "v_max_u32 v59, v87, v59\n"
        "v_cmp_eq_u32 vcc, -1, v59\n"
        "s_cbranch_vccnz Rab22_%=\n"
        "s_branch Cab22_%=\n"
        "Rab23_%=:\n"
        "s_sub_u32 s81, s81, 1\n"
        "s_cbranch_scc1 Cab23_%=\n"
        "s_sleep 1\n"
        "s_bfe_u32 s59, s48, 262152\n"
        "s_lshl_b32 s59, s59, 14\n"
        "s_add_u32 s59, s59, s24\n"
        "v_add_u32 v62, s59, %[vl16]\n"
        "global_load_dwordx4 v[88:91], v62, %[sws] offset:3072 sc1\n"
        "v_add_u32 v62, 8192, v62\n"
        "global_load_dwordx4 v[92:95], v62, %[sws] offset:3072 sc1\n"
        "s_waitcnt vmcnt(0)\n"
        "v_max3_u32 v59, v88, v89, v90\n"
        "v_max3_u32 v59, v91, v92, v59\n"
        "v_max3_u32 v59, v93, v94, v59\n"
        "v_max_u32 v59, v95, v59\n"
        "v_cmp_eq_u32 vcc, -1, v59\n"
        "s_cbranch_vccnz Rab23_%=\n"
        "s_branch Cab23_%=\n"
        "END_%=:\n"
        : "+{v[64:95]}"(W0), "+{v[96:127]}"(W1), "=&{v[128:159]}"(P2a), "=&{v[160:191]}"(P2b), "+{v[192:223]}"(P1a), "+{v[224:255]}"(P1b)
        : [vl16] "v"(l * 16),
          [sws] "s"(ws),
          "{s[56:57]}"(tag),
          "{s[24:39]}"(KP),
          "{s[40:43]}"(LB),
          "{s[44:47]}"(DP),
          "{s[48:55]}"(MP)
        : "scc", "vcc", "memory", "s59", "s60", "s61", "s62", "s63", "s64", "s65", "s81", "s82", "s83", "v31", "v32", "v33", "v34", "v35", "v36", "v37", "v38", "v39", "v40", "v41", "v42", "v43", "v44", "v45", "v46", "v47", "v48", "v49", "v50", "v51", "v52", "v53", "v54", "v55", "v56", "v57", "v58", "v59", "v60", "v61", "v62", "v63", "a0", "a1", "a2", "a3", "a4", "a5", "a6", "a7", "a8", "a9", "a10", "a11", "a12", "a13", "a14", "a15", "a16", "a17", "a18", "a19", "a20", "a21", "a22", "a23", "a24", "a25", "a26", "a27", "a28", "a29", "a30", "a31", "a32", "a33", "a34", "a35", "a36", "a37", "a38", "a39", "a40", "a41", "a42", "a43", "a44", "a45", "a46", "a47", "a48", "a49", "a50", "a51", "a52", "a53", "a54", "a55", "a56", "a57", "a58", "a59", "a60", "a61", "a62", "a63", "a64", "a65", "a66", "a67", "a68", "a69", "a70", "a71", "a72", "a73", "a74", "a75", "a76", "a77", "a78", "a79", "a80", "a81", "a82", "a83", "a84", "a85", "a86", "a87", "a88", "a89", "a90", "a91", "a92", "a93", "a94", "a95", "a96", "a97", "a98", "a99", "a100", "a101", "a102", "a103", "a104", "a105", "a106", "a107", "a108", "a109", "a110", "a111", "a112", "a113", "a114", "a115", "a116", "a117", "a118", "a119", "a120", "a121", "a122", "a123", "a124", "a125", "a126", "a127", "a128", "a129", "a130", "a131", "a132", "a133", "a134", "a135", "a136", "a137", "a138", "a139", "a140", "a141", "a142", "a143", "a144", "a145", "a146", "a147", "a148", "a149", "a150", "a151", "a152", "a153", "a154", "a155", "a156", "a157", "a158", "a159", "a160", "a161", "a162", "a163", "a164", "a165", "a166", "a167", "a168", "a169", "a170", "a171", "a172", "a173", "a174", "a175", "a176", "a177", "a178", "a179", "a180", "a181", "a182", "a183", "a184", "a185", "a186", "a187", "a188", "a189", "a190", "a191", "a192", "a193", "a194", "a195", "a196", "a197", "a198", "a199", "a200", "a201", "a202", "a203", "a204", "a205", "a206", "a207", "a208", "a209", "a210", "a211", "a212", "a213", "a214", "a215", "a216", "a217", "a218", "a219", "a220", "a221", "a222", "a223", "a224", "a225", "a226", "a227", "a228", "a229", "a230", "a231", "a232", "a233", "a234", "a235", "a236", "a237", "a238", "a239", "a240", "a241", "a242", "a243", "a244", "a245", "a246", "a247", "a248", "a249", "a250", "a251", "a252", "a253", "a254", "a255");
    LANE();
    STAMP(9);
    if (bt < 5) ffn3(R, bep, bt, w, l);
    LANE();
    STAMP(10);
    {
        const char* sxr = ws;
        asm volatile(
        "s_nop 4\n"
        "s_mov_b32 s81, 0x40000\n"
        "v_add_u32 v58, s54, %[vl16]\n"
        "v_lshrrev_b32 v59, 1, %[vl16]\n"
        "ds_read_b128 v[32:35], v58\n"
        "v_add_u32 v59, 156224, v59\n"
        "ds_read_b64 v[40:41], v59\n"
        "ds_read_b64 v[44:45], v59\n"
        "ds_read_b64 v[46:47], v59\n"
        "v_add_u32 v58, 161408, %[vl16]\n"
        "ds_read_b128 v[36:39], v58\n"
        "v_mov_b32 v42, 0\n"
        "v_mov_b32 v43, 0\n"
        "v_add_u32 v48, s40, %[vl16]\n"
        "v_add_u32 v49, s41, %[vl16]\n"
        "v_add_u32 v50, s42, %[vl16]\n"
        "v_add_u32 v51, s43, %[vl16]\n"
        "v_mov_b32 v63, s57\n"
        "s_waitcnt lgkmcnt(0)\n"
        "v_add_u32 v32, 0x20000, v32\n"
        "v_add_u32 v34, 0x20000, v34\n"
        "s_bfe_u32 s59, s48, 262156\n"
        "s_lshl_b32 s59, s59, 14\n"
        "s_add_u32 s59, s59, s24\n"
        "v_add_u32 v56, s59, %[vl16]\n"
        "v_add_u32 v57, 8192, v56\n"
        "s_waitcnt vmcnt(14)\n"
        "v_max3_u32 v59, v96, v97, v98\n"
        "v_max3_u32 v59, v99, v100, v59\n"
        "v_max3_u32 v59, v101, v102, v59\n"
        "v_max_u32 v59, v103, v59\n"
        "v_cmp_eq_u32 vcc, -1, v59\n"
        "s_cbranch_vccnz Rab24_%=\n"
        "Cab24_%=:\n"
        "v_mfma_f32_16x16x32_bf16 v[192:195], v[96:99], a[0:3], 0\n"
        "v_mfma_f32_16x16x32_bf16 v[196:199], v[100:103], a[0:3], 0\n"
        "v_cvt_pk_bf16_f32 v144, v144, v145\n"
        "v_cvt_pk_bf16_f32 v145, v146, v147\n"
        "v_mfma_f32_16x16x32_bf16 v[200:203], v[96:99], a[4:7], 0\n"
        "v_cvt_pk_bf16_f32 v146, v148, v149\n"
        "v_cvt_pk_bf16_f32 v147, v150, v151\n"
        "v_mfma_f32_16x16x32_bf16 v[204:207], v[100:103], a[4:7], 0\n"
        "ds_write_b128 v48, v[144:147] offset:0\n"
        "v_cvt_pk_bf16_f32 v152, v152, v153\n"
        "v_cvt_pk_bf16_f32 v153, v154, v155\n"
        "v_mfma_f32_16x16x32_bf16 v[208:211], v[96:99], a[8:11], 0\n"
        "v_cvt_pk_bf16_f32 v154, v156, v157\n"
        "v_cvt_pk_bf16_f32 v155, v158, v159\n"
        "v_mfma_f32_16x16x32_bf16 v[212:215], v[100:103], a[8:11], 0\n"
        "ds_write_b128 v48, v[152:155] offset:1024\n"
        "v_cvt_pk_bf16_f32 v160, v160, v161\n"
        "v_cvt_pk_bf16_f32 v161, v162, v163\n"
        "v_mfma_f32_16x16x32_bf16 v[216:219], v[96:99], a[12:15], 0\n"
        "v_cvt_pk_bf16_f32 v162, v164, v165\n"
        "v_cvt_pk_bf16_f32 v163, v166, v167\n"
        "v_mfma_f32_16x16x32_bf16 v[220:223], v[100:103], a[12:15], 0\n"
        "ds_write_b128 v49, v[160:163] offset:0\n"
        "v_cvt_pk_bf16_f32 v168, v168, v169\n"
        "v_cvt_pk_bf16_f32 v169, v170, v171\n"
        "v_mfma_f32_16x16x32_bf16 v[224:227], v[96:99], a[16:19], 0\n"
        "v_cvt_pk_bf16_f32 v170, v172, v173\n"
        "v_cvt_pk_bf16_f32 v171, v174, v175\n"
        "v_mfma_f32_16x16x32_bf16 v[228:231], v[100:103], a[16:19], 0\n"
        "ds_write_b128 v49, v[168:171] offset:1024\n"
        "v_cvt_pk_bf16_f32 v176, v176, v177\n"
        "v_cvt_pk_bf16_f32 v177, v178, v179\n"
        "v_mfma_f32_16x16x32_bf16 v[232:235], v[96:99], a[20:23], 0\n"
        "v_cvt_pk_bf16_f32 v178, v180, v181\n"
        "v_cvt_pk_bf16_f32 v179, v182, v183\n"
        "v_mfma_f32_16x16x32_bf16 v[236:239], v[100:103], a[20:23], 0\n"
        "ds_write_b128 v50, v[176:179] offset:0\n"
        "v_cvt_pk_bf16_f32 v184, v184, v185\n"
        "v_cvt_pk_bf16_f32 v185, v186, v187\n"
        "v_mfma_f32_16x16x32_bf16 v[240:243], v[96:99], a[24:27], 0\n"
        "v_cvt_pk_bf16_f32 v186, v188, v189\n"
        "v_cvt_pk_bf16_f32 v187, v190, v191\n"
        "v_mfma_f32_16x16x32_bf16 v[244:247], v[100:103], a[24:27], 0\n"
        "ds_write_b128 v50, v[184:187] offset:1024\n"
        "v_mfma_f32_16x16x32_bf16 v[248:251], v[96:99], a[28:31], 0\n"
        "global_load_dwordx4 v[88:91], v56, %[sws] offset:3072 sc1\n"
        "v_mfma_f32_16x16x32_bf16 v[252:255], v[100:103], a[28:31], 0\n"
        "s_waitcnt lgkmcnt(0)\n"
        "s_barrier\n"
        "global_load_dwordx4 v[92:95], v57, %[sws] offset:3072 sc1\n"
        "s_waitcnt vmcnt(14)\n"
        "v_max3_u32 v59, v104, v105, v106\n"
        "v_max3_u32 v59, v107, v108, v59\n"
        "v_max3_u32 v59, v109, v110, v59\n"
        "v_max_u32 v59, v111, v59\n"
        "v_cmp_eq_u32 vcc, -1, v59\n"
        "s_cbranch_vccnz Rab25_%=\n"
        "Cab25_%=:\n"
        "v_mfma_f32_16x16x32_bf16 v[192:195], v[104:107], a[32:35], v[192:195]\n"
        "ds_read_b128 v[144:147], v51 offset:0\n"
        "v_mfma_f32_16x16x32_bf16 v[196:199], v[108:111], a[32:35], v[196:199]\n"
        "ds_read_b128 v[148:151], v51 offset:1024\n"
        "v_mfma_f32_16x16x32_bf16 v[200:203], v[104:107], a[36:39], v[200:203]\n"
        "ds_read_b128 v[152:155], v51 offset:2048\n"
        "v_mfma_f32_16x16x32_bf16 v[204:207], v[108:111], a[36:39], v[204:207]\n"
        "ds_read_b128 v[156:159], v51 offset:3072\n"
        "v_mfma_f32_16x16x32_bf16 v[208:211], v[104:107], a[40:43], v[208:211]\n"
        "ds_read_b128 v[160:163], v51 offset:4096\n"
        "v_mfma_f32_16x16x32_bf16 v[212:215], v[108:111], a[40:43], v[212:215]\n"
        "ds_read_b128 v[164:167], v51 offset:5120\n"
        "v_mfma_f32_16x16x32_bf16 v[216:219], v[104:107], a[44:47], v[216:219]\n"
        "v_mfma_f32_16x16x32_bf16 v[220:223], v[108:111], a[44:47], v[220:223]\n"
        "s_waitcnt lgkmcnt(5)\n"
        "v_mfma_f32_16x16x32_bf16 v[128:131], v[40:43], v[144:147], v[128:131]\n"
        "v_mfma_f32_16x16x32_bf16 v[224:227], v[104:107], a[48:51], v[224:227]\n"
        "v_mfma_f32_16x16x32_bf16 v[132:135], v[42:45], v[144:147], v[132:135]\n"
        "v_mfma_f32_16x16x32_bf16 v[228:231], v[108:111], a[48:51], v[228:231]\n"
        "s_waitcnt lgkmcnt(4)\n"
        "v_mfma_f32_16x16x32_bf16 v[136:139], v[40:43], v[148:151], v[136:139]\n"
        "v_mfma_f32_16x16x32_bf16 v[232:235], v[104:107], a[52:55], v[232:235]\n"
        "v_mfma_f32_16x16x32_bf16 v[140:143], v[42:45], v[148:151], v[140:143]\n"
        "v_mfma_f32_16x16x32_bf16 v[236:239], v[108:111], a[52:55], v[236:239]\n"
        "s_waitcnt lgkmcnt(3)\n"
        "v_mfma_f32_16x16x32_bf16 v[128:131], v[40:43], v[152:155], v[128:131]\n"
        "v_mfma_f32_16x16x32_bf16 v[240:243], v[104:107], a[56:59], v[240:243]\n"
        "v_mfma_f32_16x16x32_bf16 v[132:135], v[42:45], v[152:155], v[132:135]\n"
        "v_mfma_f32_16x16x32_bf16 v[244:247], v[108:111], a[56:59], v[244:247]\n"
        "s_waitcnt lgkmcnt(2)\n"
        "v_mfma_f32_16x16x32_bf16 v[136:139], v[40:43], v[156:159], v[136:139]\n"
        "v_mfma_f32_16x16x32_bf16 v[248:251], v[104:107], a[60:63], v[248:251]\n"
        "s_waitcnt vmcnt(12)\n"
        "v_mov_b32 v62, s56\n"
        "v_add_u32 v62, 2, v62\n"
        "s_mov_b64 exec, 1\n"
        "v_mov_b32 v31, s34\n"
        "global_store_dwordx2 v31, v[62:63], %[sws] sc1\n"
        "s_mov_b64 exec, -1\n"
        "v_mfma_f32_16x16x32_bf16 v[140:143], v[42:45], v[156:159], v[140:143]\n"
        "s_bfe_u32 s59, s48, 262160\n"
        "s_lshl_b32 s59, s59, 14\n"
        "s_add_u32 s59, s59, s24\n"
        "v_add_u32 v56, s59, %[vl16]\n"
        "v_add_u32 v57, 8192, v56\n"
        "global_load_dwordx4 v[96:99], v56, %[sws] offset:-4096 sc1\n"
        "v_mfma_f32_16x16x32_bf16 v[252:255], v[108:111], a[60:63], v[252:255]\n"
        "s_waitcnt lgkmcnt(1)\n"
        "v_mfma_f32_16x16x32_bf16 v[128:131], v[40:43], v[160:163], v[128:131]\n"
        "global_load_dwordx4 v[100:103], v57, %[sws] offset:-4096 sc1\n"
        "s_waitcnt vmcnt(13)\n"
        "v_max3_u32 v59, v112, v113, v114\n"
        "v_max3_u32 v59, v115, v116, v59\n"
        "v_max3_u32 v59, v117, v118, v59\n"
        "v_max_u32 v59, v119, v59\n"
        "v_cmp_eq_u32 vcc, -1, v59\n"
        "s_cbranch_vccnz Rab26_%=\n"
        "Cab26_%=:\n"
        "v_mfma_f32_16x16x32_bf16 v[192:195], v[112:115], a[64:67], v[192:195]\n"
        "v_mfma_f32_16x16x32_bf16 v[132:135], v[42:45], v[160:163], v[132:135]\n"
        "v_mfma_f32_16x16x32_bf16 v[196:199], v[116:119], a[64:67], v[196:199]\n"
        "s_waitcnt lgkmcnt(0)\n"
        "v_mfma_f32_16x16x32_bf16 v[136:139], v[40:43], v[164:167], v[136:139]\n"
        "v_mfma_f32_16x16x32_bf16 v[200:203], v[112:115], a[68:71], v[200:203]\n"
        "v_mfma_f32_16x16x32_bf16 v[140:143], v[42:45], v[164:167], v[140:143]\n"
        "v_mfma_f32_16x16x32_bf16 v[204:207], v[116:119], a[68:71], v[204:207]\n"
        "s_barrier\n"
        "v_mfma_f32_16x16x32_bf16 v[208:211], v[112:115], a[72:75], v[208:211]\n"
        "v_mfma_f32_16x16x32_bf16 v[212:215], v[116:119], a[72:75], v[212:215]\n"
        "v_mfma_f32_16x16x32_bf16 v[216:219], v[112:115], a[76:79], v[216:219]\n"
        "v_mfma_f32_16x16x32_bf16 v[220:223], v[116:119], a[76:79], v[220:223]\n"
        "v_mfma_f32_16x16x32_bf16 v[224:227], v[112:115], a[80:83], v[224:227]\n"
        "v_mfma_f32_16x16x32_bf16 v[228:231], v[116:119], a[80:83], v[228:231]\n"
        "v_cvt_pk_bf16_f32 v144, v128, v129\n"
        "v_cvt_pk_bf16_f32 v145, v130, v131\n"
        "v_mfma_f32_16x16x32_bf16 v[232:235], v[112:115], a[84:87], v[232:235]\n"
        "v_cvt_pk_bf16_f32 v146, v132, v133\n"
        "v_cvt_pk_bf16_f32 v147, v134, v135\n"
        "v_mfma_f32_16x16x32_bf16 v[236:239], v[116:119], a[84:87], v[236:239]\n"
        "v_cvt_pk_bf16_f32 v148, v136, v137\n"
        "v_cvt_pk_bf16_f32 v149, v138, v139\n"
        "v_mfma_f32_16x16x32_bf16 v[240:243], v[112:115], a[88:91], v[240:243]\n"
        "v_cvt_pk_bf16_f32 v150, v140, v141\n"
        "v_cvt_pk_bf16_f32 v151, v142, v143\n"
        "v_add_u32 v58, s46, %[vl16]\n"
        "v_mfma_f32_16x16x32_bf16 v[244:247], v[116:119], a[88:91], v[244:247]\n"
        "v_mfma_f32_16x16x32_bf16 v[248:251], v[112:115], a[92:95], v[248:251]\n"
        "global_store_dwordx4 v58, v[144:147], %[sws] sc1\n"
        "global_load_dwordx4 v[104:107], v56, %[sws] offset:-3072 sc1\n"
        "v_mfma_f32_16x16x32_bf16 v[252:255], v[116:119], a[92:95], v[252:255]\n"
        "global_load_dwordx4 v[108:111], v57, %[sws] offset:-3072 sc1\n"
        "s_waitcnt vmcnt(14)\n"
        "v_max3_u32 v59, v120, v121, v122\n"
        "v_max3_u32 v59, v123, v124, v59\n"
        "v_max3_u32 v59, v125, v126, v59\n"
        "v_max_u32 v59, v127, v59\n"
        "v_cmp_eq_u32 vcc, -1, v59\n"
        "s_cbranch_vccnz Rab27_%=\n"
        "Cab27_%=:\n"
        "v_mfma_f32_16x16x32_bf16 v[192:195], v[120:123], a[96:99], v[192:195]\n"
        "global_store_dwordx4 v58, v[148:151], %[sws] offset:1024 sc1\n"
        "v_mfma_f32_16x16x32_bf16 v[196:199], v[124:127], a[96:99], v[196:199]\n"
        "v_mfma_f32_16x16x32_bf16 v[200:203], v[120:123], a[100:103], v[200:203]\n"
        "v_mfma_f32_16x16x32_bf16 v[204:207], v[124:127], a[100:103], v[204:207]\n"
        "v_mfma_f32_16x16x32_bf16 v[208:211], v[120:123], a[104:107], v[208:211]\n"
        "v_mfma_f32_16x16x32_bf16 v[212:215], v[124:127], a[104:107], v[212:215]\n"
        "v_mfma_f32_16x16x32_bf16 v[216:219], v[120:123], a[108:111], v[216:219]\n"
        "v_mfma_f32_16x16x32_bf16 v[220:223], v[124:127], a[108:111], v[220:223]\n"
        "v_mfma_f32_16x16x32_bf16 v[224:227], v[120:123], a[112:115], v[224:227]\n"
        "v_mfma_f32_16x16x32_bf16 v[228:231], v[124:127], a[112:115], v[228:231]\n"
        "v_mfma_f32_16x16x32_bf16 v[232:235], v[120:123], a[116:119], v[232:235]\n"
        "v_mfma_f32_16x16x32_bf16 v[236:239], v[124:127], a[116:119], v[236:239]\n"
        "v_mfma_f32_16x16x32_bf16 v[240:243], v[120:123], a[120:123], v[240:243]\n"
        "v_mov_b32 v24, s39\n"
        "global_load_dwordx4 v[16:19], v24, %[sws] offset:0 sc1\n"
        "global_load_dwordx4 v[20:23], v24, %[sws] offset:16 sc1\n"
        "v_mfma_f32_16x16x32_bf16 v[244:247], v[124:127], a[120:123], v[244:247]\n"
        "v_mfma_f32_16x16x32_bf16 v[248:251], v[120:123], a[124:127], v[248:251]\n"
        "global_load_dwordx4 v[112:115], v56, %[sws] offset:-2048 sc1\n"
        "v_mfma_f32_16x16x32_bf16 v[252:255], v[124:127], a[124:127], v[252:255]\n"
        "global_load_dwordx4 v[116:119], v57, %[sws] offset:-2048 sc1\n"
        "s_waitcnt vmcnt(17)\n"
        "v_max3_u32 v59, v64, v65, v66\n"
        "v_max3_u32 v59, v67, v68, v59\n"
        "v_max3_u32 v59, v69, v70, v59\n"
        "v_max_u32 v59, v71, v59\n"
        "v_cmp_eq_u32 vcc, -1, v59\n"
        "s_cbranch_vccnz Rab28_%=\n"
        "Cab28_%=:\n"
        "v_mfma_f32_16x16x32_bf16 v[192:195], v[64:67], a[128:131], v[192:195]\n"
        "v_mfma_f32_16x16x32_bf16 v[196:199], v[68:71], a[128:131], v[196:199]\n"
        "v_mfma_f32_16x16x32_bf16 v[200:203], v[64:67], a[132:135], v[200:203]\n"
        "v_mfma_f32_16x16x32_bf16 v[204:207], v[68:71], a[132:135], v[204:207]\n"
        "v_mfma_f32_16x16x32_bf16 v[208:211], v[64:67], a[136:139], v[208:211]\n"
        "v_mfma_f32_16x16x32_bf16 v[212:215], v[68:71], a[136:139], v[212:215]\n"
        "v_mfma_f32_16x16x32_bf16 v[216:219], v[64:67], a[140:143], v[216:219]\n"
        "v_mfma_f32_16x16x32_bf16 v[220:223], v[68:71], a[140:143], v[220:223]\n"
        "v_mfma_f32_16x16x32_bf16 v[224:227], v[64:67], a[144:147], v[224:227]\n"
        "v_mfma_f32_16x16x32_bf16 v[228:231], v[68:71], a[144:147], v[228:231]\n"
        "v_mfma_f32_16x16x32_bf16 v[232:235], v[64:67], a[148:151], v[232:235]\n"
        "v_mfma_f32_16x16x32_bf16 v[236:239], v[68:71], a[148:151], v[236:239]\n"
        "v_mfma_f32_16x16x32_bf16 v[240:243], v[64:67], a[152:155], v[240:243]\n"
        "v_mfma_f32_16x16x32_bf16 v[244:247], v[68:71], a[152:155], v[244:247]\n"
        "v_mfma_f32_16x16x32_bf16 v[248:251], v[64:67], a[156:159], v[248:251]\n"
        "global_load_dwordx4 v[120:123], v56, %[sws] offset:-1024 sc1\n"
        "v_mfma_f32_16x16x32_bf16 v[252:255], v[68:71], a[156:159], v[252:255]\n"
        "global_load_dwordx4 v[124:127], v57, %[sws] offset:-1024 sc1\n"
        "s_waitcnt vmcnt(17)\n"
        "v_max3_u32 v59, v72, v73, v74\n"
        "v_max3_u32 v59, v75, v76, v59\n"
        "v_max3_u32 v59, v77, v78, v59\n"
        "v_max_u32 v59, v79, v59\n"
        "v_cmp_eq_u32 vcc, -1, v59\n"
        "s_cbranch_vccnz Rab29_%=\n"
        "Cab29_%=:\n"
        "v_mfma_f32_16x16x32_bf16 v[192:195], v[72:75], a[160:163], v[192:195]\n"
        "v_mfma_f32_16x16x32_bf16 v[196:199], v[76:79], a[160:163], v[196:199]\n"
        "v_mfma_f32_16x16x32_bf16 v[200:203], v[72:75], a[164:167], v[200:203]\n"
        "v_mfma_f32_16x16x32_bf16 v[204:207], v[76:79], a[164:167], v[204:207]\n"
        "v_mfma_f32_16x16x32_bf16 v[208:211], v[72:75], a[168:171], v[208:211]\n"
        "v_mfma_f32_16x16x32_bf16 v[212:215], v[76:79], a[168:171], v[212:215]\n"
        "v_mfma_f32_16x16x32_bf16 v[216:219], v[72:75], a[172:175], v[216:219]\n"
        "v_mfma_f32_16x16x32_bf16 v[220:223], v[76:79], a[172:175], v[220:223]\n"
        "v_mfma_f32_16x16x32_bf16 v[224:227], v[72:75], a[176:179], v[224:227]\n"
        "global_load_dwordx2 v[60:61], v34, %[sws] sc1\n"
        "v_mfma_f32_16x16x32_bf16 v[228:231], v[76:79], a[176:179], v[228:231]\n"
        "v_mfma_f32_16x16x32_bf16 v[232:235], v[72:75], a[180:183], v[232:235]\n"
        "v_mfma_f32_16x16x32_bf16 v[236:239], v[76:79], a[180:183], v[236:239]\n"
        "v_mfma_f32_16x16x32_bf16 v[240:243], v[72:75], a[184:187], v[240:243]\n"
        "v_mfma_f32_16x16x32_bf16 v[244:247], v[76:79], a[184:187], v[244:247]\n"
        "v_mfma_f32_16x16x32_bf16 v[248:251], v[72:75], a[188:191], v[248:251]\n"
        "global_load_dwordx4 v[64:67], v56, %[sws] offset:0 sc1\n"
        "v_mfma_f32_16x16x32_bf16 v[252:255], v[76:79], a[188:191], v[252:255]\n"
        "global_load_dwordx4 v[68:71], v57, %[sws] offset:0 sc1\n"
        "s_waitcnt vmcnt(18)\n"
        "v_max3_u32 v59, v80, v81, v82\n"
        "v_max3_u32 v59, v83, v84, v59\n"
        "v_max3_u32 v59, v85, v86, v59\n"
        "v_max_u32 v59, v87, v59\n"
        "v_cmp_eq_u32 vcc, -1, v59\n"
        "s_cbranch_vccnz Rab30_%=\n"
        "Cab30_%=:\n"
        "v_mfma_f32_16x16x32_bf16 v[192:195], v[80:83], a[192:195], v[192:195]\n"
        "v_mfma_f32_16x16x32_bf16 v[196:199], v[84:87], a[192:195], v[196:199]\n"
        "v_mfma_f32_16x16x32_bf16 v[200:203], v[80:83], a[196:199], v[200:203]\n"
        "v_mfma_f32_16x16x32_bf16 v[204:207], v[84:87], a[196:199], v[204:207]\n"
        "v_mfma_f32_16x16x32_bf16 v[208:211], v[80:83], a[200:203], v[208:211]\n"
        "v_mfma_f32_16x16x32_bf16 v[212:215], v[84:87], a[200:203], v[212:215]\n"
        "v_mfma_f32_16x16x32_bf16 v[216:219], v[80:83], a[204:207], v[216:219]\n"
        "v_mfma_f32_16x16x32_bf16 v[220:223], v[84:87], a[204:207], v[220:223]\n"
        "v_mfma_f32_16x16x32_bf16 v[224:227], v[80:83], a[208:211], v[224:227]\n"
        "v_mfma_f32_16x16x32_bf16 v[228:231], v[84:87], a[208:211], v[228:231]\n"
        "v_mfma_f32_16x16x32_bf16 v[232:235], v[80:83], a[212:215], v[232:235]\n"
        "v_mfma_f32_16x16x32_bf16 v[236:239], v[84:87], a[212:215], v[236:239]\n"
        "v_mfma_f32_16x16x32_bf16 v[240:243], v[80:83], a[216:219], v[240:243]\n"
        "v_mfma_f32_16x16x32_bf16 v[244:247], v[84:87], a[216:219], v[244:247]\n"
        "v_mfma_f32_16x16x32_bf16 v[248:251], v[80:83], a[220:223], v[248:251]\n"
        "s_waitcnt vmcnt(7)\n"
        "v_max3_u32 v59, v16, v17, v18\n"
        "v_max3_u32 v59, v19, v20, v59\n"
        "v_max3_u32 v59, v21, v22, v59\n"
        "v_max_u32 v59, v23, v59\n"
        "v_cmp_eq_u32 vcc, -1, v59\n"
        "s_cbranch_vccnz Rix_%=\n"
        "Cix_%=:\n"
        "global_load_dwordx4 v[72:75], v56, %[sws] offset:1024 sc1\n"
        "v_mfma_f32_16x16x32_bf16 v[252:255], v[84:87], a[220:223], v[252:255]\n"
        "v_add_u32 v25, 16, v35\n"
        "v_mov_b32 v26, 8\n"
        "v_mov_b32 v27, 8\n"
        "global_load_dwordx4 v[76:79], v57, %[sws] offset:1024 sc1\n"
        "v_max3_u32 v59, v88, v89, v90\n"
        "v_max3_u32 v59, v91, v92, v59\n"
        "v_max3_u32 v59, v93, v94, v59\n"
        "v_max_u32 v59, v95, v59\n"
        "v_cmp_eq_u32 vcc, -1, v59\n"
        "s_cbranch_vccnz Rab31_%=\n"
        "Cab31_%=:\n"
        "v_mfma_f32_16x16x32_bf16 v[192:195], v[88:91], a[224:227], v[192:195]\n"
        "v_cmp_eq_u32 s[60:61], v16, v35\n"
        "v_cmp_eq_u32 s[62:63], v16, v25\n"
        "v_mfma_f32_16x16x32_bf16 v[196:199], v[92:95], a[224:227], v[196:199]\n"
        "v_cndmask_b32 v26, v26, 0, s[60:61]\n"
        "v_cndmask_b32 v27, v27, 0, s[62:63]\n"
        "v_cmp_eq_u32 s[60:61], v17, v35\n"
        "v_cmp_eq_u32 s[62:63], v17, v25\n"
        "v_mfma_f32_16x16x32_bf16 v[200:203], v[88:91], a[228:231], v[200:203]\n"
        "v_cndmask_b32 v26, v26, 1, s[60:61]\n"
        "v_cndmask_b32 v27, v27, 1, s[62:63]\n"
        "v_cmp_eq_u32 s[60:61], v18, v35\n"
        "v_cmp_eq_u32 s[62:63], v18, v25\n"
        "v_mfma_f32_16x16x32_bf16 v[204:207], v[92:95], a[228:231], v[204:207]\n"
        "v_cndmask_b32 v26, v26, 2, s[60:61]\n"
        "v_cndmask_b32 v27, v27, 2, s[62:63]\n"
        "v_cmp_eq_u32 s[60:61], v19, v35\n"
        "v_cmp_eq_u32 s[62:63], v19, v25\n"
        "v_mfma_f32_16x16x32_bf16 v[208:211], v[88:91], a[232:235], v[208:211]\n"
        "v_cndmask_b32 v26, v26, 3, s[60:61]\n"
        "v_cndmask_b32 v27, v27, 3, s[62:63]\n"
        "v_cmp_eq_u32 s[60:61], v20, v35\n"
        "v_cmp_eq_u32 s[62:63], v20, v25\n"
        "v_mfma_f32_16x16x32_bf16 v[212:215], v[92:95], a[232:235], v[212:215]\n"
        "v_cndmask_b32 v26, v26, 4, s[60:61]\n"
        "v_cndmask_b32 v27, v27, 4, s[62:63]\n"
        "v_cmp_eq_u32 s[60:61], v21, v35\n"
        "v_cmp_eq_u32 s[62:63], v21, v25\n"
        "v_mfma_f32_16x16x32_bf16 v[216:219], v[88:91], a[236:239], v[216:219]\n"
        "v_cndmask_b32 v26, v26, 5, s[60:61]\n"
        "v_cndmask_b32 v27, v27, 5, s[62:63]\n"
        "v_cmp_eq_u32 s[60:61], v22, v35\n"
        "v_cmp_eq_u32 s[62:63], v22, v25\n"
        "v_mfma_f32_16x16x32_bf16 v[220:223], v[92:95], a[236:239], v[220:223]\n"
        "v_cndmask_b32 v26, v26, 6, s[60:61]\n"
        "v_cndmask_b32 v27, v27, 6, s[62:63]\n"
        "v_cmp_eq_u32 s[60:61], v23, v35\n"
        "v_cmp_eq_u32 s[62:63], v23, v25\n"
        "v_mfma_f32_16x16x32_bf16 v[224:227], v[88:91], a[240:243], v[224:227]\n"
        "v_cndmask_b32 v26, v26, 7, s[60:61]\n"
        "v_cndmask_b32 v27, v27, 7, s[62:63]\n"
        "v_mfma_f32_16x16x32_bf16 v[228:231], v[92:95], a[240:243], v[228:231]\n"
        "v_mfma_f32_16x16x32_bf16 v[232:235], v[88:91], a[244:247], v[232:235]\n"
        "v_cmp_gt_u32 s[60:61], 8, v26\n"
        "v_cmp_gt_u32 s[62:63], 8, v27\n"
        "v_mfma_f32_16x16x32_bf16 v[236:239], v[92:95], a[244:247], v[236:239]\n"
        "v_and_b32 v26, 7, v26\n"
        "v_and_b32 v27, 7, v27\n"
        "v_add_u32 v25, s38, v38\n"
        "v_mfma_f32_16x16x32_bf16 v[240:243], v[88:91], a[248:251], v[240:243]\n"
        "v_mov_b32 v62, s56\n"
        "v_add_u32 v62, 3, v62\n"
        "s_mov_b64 exec, 1\n"
        "v_mov_b32 v31, s34\n"
        "global_store_dwordx2 v31, v[62:63], %[sws] sc1\n"
        "s_mov_b64 exec, -1\n"
        "v_mad_u32_u24 v16, v26, s52, v25\n"
        "v_mad_u32_u24 v17, v27, s52, v25\n"
        "v_mfma_f32_16x16x32_bf16 v[244:247], v[92:95], a[248:251], v[244:247]\n"
        "s_cmp_lt_u32 s49, 2\n"
        "s_cselect_b64 s[64:65], s[60:61], s[62:63]\n"
        "s_cselect_b64 s[82:83], -1, 0\n"
        "v_mfma_f32_16x16x32_bf16 v[248:251], v[88:91], a[252:255], v[248:251]\n"
        "v_cndmask_b32 v18, v17, v16, s[82:83]\n"
        "global_load_dwordx4 v[80:83], v56, %[sws] offset:2048 sc1\n"
        "v_mfma_f32_16x16x32_bf16 v[252:255], v[92:95], a[252:255], v[252:255]\n"
        "v_add_u32 v18, s50, v18\n"
        "global_load_dwordx4 v[84:87], v57, %[sws] offset:2048 sc1\n"
        "v_max3_u32 v59, v96, v97, v98\n"
        "v_max3_u32 v59, v99, v100, v59\n"
        "v_max3_u32 v59, v101, v102, v59\n"
        "v_max_u32 v59, v103, v59\n"
        "v_cmp_eq_u32 vcc, -1, v59\n"
        "s_cbranch_vccnz Rab32_%=\n"
        "Cab32_%=:\n"
        "v_mfma_f32_16x16x32_bf16 v[144:147], v[96:99], a[8:11], 0\n"
        "v_mfma_f32_16x16x32_bf16 v[148:151], v[100:103], a[8:11], 0\n"
        "v_cvt_pk_bf16_f32 v208, v208, v209\n"
        "v_cvt_pk_bf16_f32 v209, v210, v211\n"
        "v_and_b32 v59, 127, v35\n"
        "v_mfma_f32_16x16x32_bf16 v[152:155], v[96:99], a[12:15], 0\n"
        "v_cvt_pk_bf16_f32 v210, v212, v213\n"
        "v_cvt_pk_bf16_f32 v211, v214, v215\n"
        "v_lshlrev_b32 v59, 2, v59\n"
        "v_mfma_f32_16x16x32_bf16 v[156:159], v[100:103], a[12:15], 0\n"
        "ds_write_b128 v48, v[208:211] offset:0\n"
        "v_cvt_pk_bf16_f32 v216, v216, v217\n"
        "v_cvt_pk_bf16_f32 v217, v218, v219\n"
        "v_add_u32 v59, 152064, v59\n"
        "v_mfma_f32_16x16x32_bf16 v[160:163], v[96:99], a[16:19], 0\n"
        "v_cvt_pk_bf16_f32 v218, v220, v221\n"
        "v_cvt_pk_bf16_f32 v219, v222, v223\n"
        "ds_read_b32 v20, v59 offset:0\n"
        "v_mfma_f32_16x16x32_bf16 v[164:167], v[100:103], a[16:19], 0\n"
        "ds_write_b128 v48, v[216:219] offset:1024\n"
        "v_cvt_pk_bf16_f32 v224, v224, v225\n"
        "v_cvt_pk_bf16_f32 v225, v226, v227\n"
        "ds_read_b32 v21, v59 offset:512\n"
        "v_mfma_f32_16x16x32_bf16 v[168:171], v[96:99], a[20:23], 0\n"
        "v_cvt_pk_bf16_f32 v226, v228, v229\n"
        "v_cvt_pk_bf16_f32 v227, v230, v231\n"
        "ds_read_b32 v22, v59 offset:64\n"
        "v_mfma_f32_16x16x32_bf16 v[172:175], v[100:103], a[20:23], 0\n"
        "ds_write_b128 v49, v[224:227] offset:0\n"
        "v_cvt_pk_bf16_f32 v232, v232, v233\n"
        "v_cvt_pk_bf16_f32 v233, v234, v235\n"
        "ds_read_b32 v23, v59 offset:576\n"
        "v_mfma_f32_16x16x32_bf16 v[176:179], v[96:99], a[24:27], 0\n"
        "v_cvt_pk_bf16_f32 v234, v236, v237\n"
        "v_cvt_pk_bf16_f32 v235, v238, v239\n"
        "v_mfma_f32_16x16x32_bf16 v[180:183], v[100:103], a[24:27], 0\n"
        "ds_write_b128 v49, v[232:235] offset:1024\n"
        "v_cvt_pk_bf16_f32 v240, v240, v241\n"
        "v_cvt_pk_bf16_f32 v241, v242, v243\n"
        "v_mfma_f32_16x16x32_bf16 v[184:187], v[96:99], a[28:31], 0\n"
        "v_cvt_pk_bf16_f32 v242, v244, v245\n"
        "v_cvt_pk_bf16_f32 v243, v246, v247\n"
        "v_mfma_f32_16x16x32_bf16 v[188:191], v[100:103], a[28:31], 0\n"
        "ds_write_b128 v50, v[240:243] offset:0\n"
        "v_cvt_pk_bf16_f32 v248, v248, v249\n"
        "v_cvt_pk_bf16_f32 v249, v250, v251\n"
        "global_load_dwordx4 v[88:91], v56, %[sws] offset:3072 sc1\n"
        "global_load_dwordx4 v[92:95], v57, %[sws] offset:3072 sc1\n"
        "v_max3_u32 v59, v104, v105, v106\n"
        "v_max3_u32 v59, v107, v108, v59\n"
        "v_max3_u32 v59, v109, v110, v59\n"
        "v_max_u32 v59, v111, v59\n"
        "v_cmp_eq_u32 vcc, -1, v59\n"
        "s_cbranch_vccnz Rab33_%=\n"
        "Cab33_%=:\n"
        "v_mfma_f32_16x16x32_bf16 v[144:147], v[104:107], a[40:43], v[144:147]\n"
        "v_cvt_pk_bf16_f32 v250, v252, v253\n"
        "v_cvt_pk_bf16_f32 v251, v254, v255\n"
        "v_mfma_f32_16x16x32_bf16 v[148:151], v[108:111], a[40:43], v[148:151]\n"
        "ds_write_b128 v50, v[248:251] offset:1024\n"
        "v_mfma_f32_16x16x32_bf16 v[152:155], v[104:107], a[44:47], v[152:155]\n"
        "v_mfma_f32_16x16x32_bf16 v[156:159], v[108:111], a[44:47], v[156:159]\n"
        "s_waitcnt lgkmcnt(0)\n"
        "s_barrier\n"
        "v_mfma_f32_16x16x32_bf16 v[160:163], v[104:107], a[48:51], v[160:163]\n"
        "ds_read_b128 v[208:211], v51 offset:0\n"
        "v_mfma_f32_16x16x32_bf16 v[164:167], v[108:111], a[48:51], v[164:167]\n"
        "ds_read_b128 v[212:215], v51 offset:1024\n"
        "v_mfma_f32_16x16x32_bf16 v[168:171], v[104:107], a[52:55], v[168:171]\n"
        "ds_read_b128 v[216:219], v51 offset:2048\n"
        "v_mfma_f32_16x16x32_bf16 v[172:175], v[108:111], a[52:55], v[172:175]\n"
        "ds_read_b128 v[220:223], v51 offset:3072\n"
        "v_mfma_f32_16x16x32_bf16 v[176:179], v[104:107], a[56:59], v[176:179]\n"
        "ds_read_b128 v[224:227], v51 offset:4096\n"
        "v_mfma_f32_16x16x32_bf16 v[180:183], v[108:111], a[56:59], v[180:183]\n"
        "ds_read_b128 v[228:231], v51 offset:5120\n"
        "s_waitcnt vmcnt(9)\n"
        "v_bfe_u32 v59, v33, 20, 4\n"
        "v_subrev_u32 v60, s56, v60\n"
        "v_sub_u32 v60, v60, v59\n"
        "v_cmp_lt_u32 vcc, 7, v60\n"
        "v_cmp_ne_u32 s[82:83], s57, v61\n"
        "s_or_b64 vcc, vcc, s[82:83]\n"
        "s_cbranch_vccnz Rxfc_%=\n"
        "Cxfc_%=:\n"
        "v_mfma_f32_16x16x32_bf16 v[184:187], v[104:107], a[60:63], v[184:187]\n"
        "s_cmp_lg_u32 s51, 0\n"
        "s_cbranch_scc1 G2K_%=\n"
        "v_mov_b32 v62, s56\n"
        "s_mov_b64 exec, 1\n"
        "v_mov_b32 v31, s35\n"
        "global_store_dwordx2 v31, v[62:63], %[sws] sc1\n"
        "s_mov_b64 exec, -1\n"
        "G2K_%=:\n"
        "v_add_u32 v31, s27, v36\n"
        "global_load_dwordx2 v[232:233], v31, %[sxr] offset:0 sc1\n"
        "v_mfma_f32_16x16x32_bf16 v[188:191], v[108:111], a[60:63], v[188:191]\n"
        "s_waitcnt lgkmcnt(5)\n"
        "v_mfma_f32_16x16x32_bf16 v[192:195], v[40:43], v[208:211], v[192:195]\n"
        "v_max3_u32 v59, v112, v113, v114\n"
        "v_max3_u32 v59, v115, v116, v59\n"
        "v_max3_u32 v59, v117, v118, v59\n"
        "v_max_u32 v59, v119, v59\n"
        "v_cmp_eq_u32 vcc, -1, v59\n"
        "s_cbranch_vccnz Rab34_%=\n"
        "Cab34_%=:\n"
        "v_mfma_f32_16x16x32_bf16 v[144:147], v[112:115], a[72:75], v[144:147]\n"
        "v_mfma_f32_16x16x32_bf16 v[196:199], v[42:45], v[208:211], v[196:199]\n"
        "global_load_dwordx2 v[234:235], v31, %[sxr] offset:512 sc1\n"
        "v_mfma_f32_16x16x32_bf16 v[148:151], v[116:119], a[72:75], v[148:151]\n"
        "s_waitcnt lgkmcnt(4)\n"
        "v_mfma_f32_16x16x32_bf16 v[200:203], v[40:43], v[212:215], v[200:203]\n"
        "v_mfma_f32_16x16x32_bf16 v[152:155], v[112:115], a[76:79], v[152:155]\n"
        "v_mfma_f32_16x16x32_bf16 v[204:207], v[42:45], v[212:215], v[204:207]\n"
        "v_add_u32 v31, 20480, v31\n"
        "global_load_dwordx2 v[236:237], v31, %[sxr] offset:0 sc1\n"
        "v_mfma_f32_16x16x32_bf16 v[156:159], v[116:119], a[76:79], v[156:159]\n"
        "s_waitcnt lgkmcnt(3)\n"
        "v_mfma_f32_16x16x32_bf16 v[192:195], v[40:43], v[216:219], v[192:195]\n"
        "v_mfma_f32_16x16x32_bf16 v[160:163], v[112:115], a[80:83], v[160:163]\n"
        "v_mfma_f32_16x16x32_bf16 v[196:199], v[42:45], v[216:219], v[196:199]\n"
        "global_load_dwordx2 v[238:239], v31, %[sxr] offset:512 sc1\n"
        "v_mfma_f32_16x16x32_bf16 v[164:167], v[116:119], a[80:83], v[164:167]\n"
        "s_waitcnt lgkmcnt(2)\n"
        "v_mfma_f32_16x16x32_bf16 v[200:203], v[40:43], v[220:223], v[200:203]\n"
        "v_mfma_f32_16x16x32_bf16 v[168:171], v[112:115], a[84:87], v[168:171]\n"
        "v_mfma_f32_16x16x32_bf16 v[204:207], v[42:45], v[220:223], v[204:207]\n"
        "v_add_u32 v31, s28, v36\n"
        "global_load_dwordx2 v[240:241], v31, %[sxr] offset:0 sc1\n"
        "v_mfma_f32_16x16x32_bf16 v[172:175], v[116:119], a[84:87], v[172:175]\n"
        "s_waitcnt lgkmcnt(1)\n"
        "v_mfma_f32_16x16x32_bf16 v[192:195], v[40:43], v[224:227], v[192:195]\n"
        "v_mfma_f32_16x16x32_bf16 v[176:179], v[112:115], a[88:91], v[176:179]\n"
        "v_mfma_f32_16x16x32_bf16 v[196:199], v[42:45], v[224:227], v[196:199]\n"
        "v_lshl_add_u32 v31, v38, 1, s29\n"
        "global_load_dwordx4 v[244:247], v31, %[swr] offset:0\n"
        "v_mfma_f32_16x16x32_bf16 v[180:183], v[116:119], a[88:91], v[180:183]\n"
        "s_waitcnt lgkmcnt(0)\n"
        "v_mfma_f32_16x16x32_bf16 v[200:203], v[40:43], v[228:231], v[200:203]\n"
        "v_mfma_f32_16x16x32_bf16 v[184:187], v[112:115], a[92:95], v[184:187]\n"
        "v_mfma_f32_16x16x32_bf16 v[204:207], v[42:45], v[228:231], v[204:207]\n"
        "v_add_u32 v31, s30, v38\n"
        "global_load_dwordx2 v[242:243], v31, %[swr] offset:0\n"
        "v_mfma_f32_16x16x32_bf16 v[188:191], v[116:119], a[92:95], v[188:191]\n"
        "s_barrier\n"
        "v_max3_u32 v59, v120, v121, v122\n"
        "v_max3_u32 v59, v123, v124, v59\n"
        "v_max3_u32 v59, v125, v126, v59\n"
        "v_max_u32 v59, v127, v59\n"
        "v_cmp_eq_u32 vcc, -1, v59\n"
        "s_cbranch_vccnz Rab35_%=\n"
        "Cab35_%=:\n"
        "v_mfma_f32_16x16x32_bf16 v[144:147], v[120:123], a[104:107], v[144:147]\n"
        "v_add_u32 v31, s25, %[vl16]\n"
        "global_load_dwordx4 v[248:251], v31, %[sws] offset:0 sc1\n"
        "v_mfma_f32_16x16x32_bf16 v[148:151], v[124:127], a[104:107], v[148:151]\n"
        "v_mfma_f32_16x16x32_bf16 v[152:155], v[120:123], a[108:111], v[152:155]\n"
        "global_load_dwordx4 v[252:255], v31, %[sws] offset:1024 sc1\n"
        "v_mfma_f32_16x16x32_bf16 v[156:159], v[124:127], a[108:111], v[156:159]\n"
        "v_mfma_f32_16x16x32_bf16 v[160:163], v[120:123], a[112:115], v[160:163]\n"
        "v_lshrrev_b32 v31, 1, %[vl16]\n"
        "v_add_u32 v31, s26, v31\n"
        "global_load_dwordx2 v[216:217], v31, %[sws] offset:-4096 sc1\n"
        "s_cmp_lt_u32 s49, 2\n"
        "s_cselect_b64 s[82:83], -1, 0\n"
        "v_mfma_f32_16x16x32_bf16 v[164:167], v[124:127], a[112:115], v[164:167]\n"
        "v_cvt_pk_bf16_f32 v208, v192, v193\n"
        "v_cvt_pk_bf16_f32 v209, v194, v195\n"
        "global_load_dwordx2 v[218:219], v31, %[sws] offset:-2048 sc1\n"
        "v_mfma_f32_16x16x32_bf16 v[168:171], v[120:123], a[116:119], v[168:171]\n"
        "v_cvt_pk_bf16_f32 v210, v196, v197\n"
        "v_cvt_pk_bf16_f32 v211, v198, v199\n"
        "global_load_dwordx2 v[220:221], v31, %[sws] offset:0 sc1\n"
        "v_cndmask_b32 v24, v22, v20, s[82:83]\n"
        "v_cndmask_b32 v25, v23, v21, s[82:83]\n"
        "v_mfma_f32_16x16x32_bf16 v[172:175], v[124:127], a[116:119], v[172:175]\n"
        "v_cvt_pk_bf16_f32 v212, v200, v201\n"
        "v_cvt_pk_bf16_f32 v213, v202, v203\n"
        "global_load_dwordx2 v[222:223], v31, %[sws] offset:2048 sc1\n"
        "v_mfma_f32_16x16x32_bf16 v[176:179], v[120:123], a[120:123], v[176:179]\n"
        "v_cvt_pk_bf16_f32 v214, v204, v205\n"
        "v_cvt_pk_bf16_f32 v215, v206, v207\n"
        "v_add_u32 v58, s47, %[vl16]\n"
        "global_load_dwordx2 v[60:61], v34, %[sws] sc1\n"
        "v_mfma_f32_16x16x32_bf16 v[180:183], v[124:127], a[120:123], v[180:183]\n"
        "v_min_u32 v208, -2, v208\n"
        "v_min_u32 v209, -2, v209\n"
        "v_min_u32 v210, -2, v210\n"
        "v_min_u32 v211, -2, v211\n"
        "v_mfma_f32_16x16x32_bf16 v[184:187], v[120:123], a[124:127], v[184:187]\n"
        "global_store_dwordx4 v58, v[208:211], %[sws] sc1\n"
        "v_mfma_f32_16x16x32_bf16 v[188:191], v[124:127], a[124:127], v[188:191]\n"
        "v_min_u32 v212, -2, v212\n"
        "v_min_u32 v213, -2, v213\n"
        "v_min_u32 v214, -2, v214\n"
        "v_min_u32 v215, -2, v215\n"
        "s_waitcnt vmcnt(22)\n"
        "v_max3_u32 v59, v64, v65, v66\n"
        "v_max3_u32 v59, v67, v68, v59\n"
        "v_max3_u32 v59, v69, v70, v59\n"
        "v_max_u32 v59, v71, v59\n"
        "v_cmp_eq_u32 vcc, -1, v59\n"
        "s_cbranch_vccnz Rab36_%=\n"
        "Cab36_%=:\n"
        "v_mfma_f32_16x16x32_bf16 v[144:147], v[64:67], a[136:139], v[144:147]\n"
        "global_store_dwordx4 v58, v[212:215], %[sws] offset:1024 sc1\n"
        "v_mfma_f32_16x16x32_bf16 v[148:151], v[68:71], a[136:139], v[148:151]\n"
        "v_mfma_f32_16x16x32_bf16 v[152:155], v[64:67], a[140:143], v[152:155]\n"
        "v_mfma_f32_16x16x32_bf16 v[156:159], v[68:71], a[140:143], v[156:159]\n"
        "v_mfma_f32_16x16x32_bf16 v[160:163], v[64:67], a[144:147], v[160:163]\n"
        "v_mfma_f32_16x16x32_bf16 v[164:167], v[68:71], a[144:147], v[164:167]\n"
        "v_mfma_f32_16x16x32_bf16 v[168:171], v[64:67], a[148:151], v[168:171]\n"
        "v_mfma_f32_16x16x32_bf16 v[172:175], v[68:71], a[148:151], v[172:175]\n"
        "v_mfma_f32_16x16x32_bf16 v[176:179], v[64:67], a[152:155], v[176:179]\n"
        "v_mfma_f32_16x16x32_bf16 v[180:183], v[68:71], a[152:155], v[180:183]\n"
        "v_mfma_f32_16x16x32_bf16 v[184:187], v[64:67], a[156:159], v[184:187]\n"
        "v_mfma_f32_16x16x32_bf16 v[188:191], v[68:71], a[156:159], v[188:191]\n"
        "s_waitcnt vmcnt(21)\n"
        "v_max3_u32 v59, v72, v73, v74\n"
        "v_max3_u32 v59, v75, v76, v59\n"
        "v_max3_u32 v59, v77, v78, v59\n"
        "v_max_u32 v59, v79, v59\n"
        "v_cmp_eq_u32 vcc, -1, v59\n"
        "s_cbranch_vccnz Rab37_%=\n"
        "Cab37_%=:\n"
        "v_mfma_f32_16x16x32_bf16 v[144:147], v[72:75], a[168:171], v[144:147]\n"
        "v_mfma_f32_16x16x32_bf16 v[148:151], v[76:79], a[168:171], v[148:151]\n"
        "v_mfma_f32_16x16x32_bf16 v[152:155], v[72:75], a[172:175], v[152:155]\n"
        "v_mfma_f32_16x16x32_bf16 v[156:159], v[76:79], a[172:175], v[156:159]\n"
        "v_mfma_f32_16x16x32_bf16 v[160:163], v[72:75], a[176:179], v[160:163]\n"
        "v_mfma_f32_16x16x32_bf16 v[164:167], v[76:79], a[176:179], v[164:167]\n"
        "v_mfma_f32_16x16x32_bf16 v[168:171], v[72:75], a[180:183], v[168:171]\n"
        "v_mfma_f32_16x16x32_bf16 v[172:175], v[76:79], a[180:183], v[172:175]\n"
        "v_mfma_f32_16x16x32_bf16 v[176:179], v[72:75], a[184:187], v[176:179]\n"
        "v_mfma_f32_16x16x32_bf16 v[180:183], v[76:79], a[184:187], v[180:183]\n"
        "v_mfma_f32_16x16x32_bf16 v[184:187], v[72:75], a[188:191], v[184:187]\n"
        "v_mfma_f32_16x16x32_bf16 v[188:191], v[76:79], a[188:191], v[188:191]\n"
        "s_waitcnt vmcnt(18)\n"
        "v_max3_u32 v59, v80, v81, v82\n"
        "v_max3_u32 v59, v83, v84, v59\n"
        "v_max3_u32 v59, v85, v86, v59\n"
        "v_max_u32 v59, v87, v59\n"
        "v_cmp_eq_u32 vcc, -1, v59\n"
        "s_cbranch_vccnz Rab38_%=\n"
        "Cab38_%=:\n"
        "v_mfma_f32_16x16x32_bf16 v[144:147], v[80:83], a[200:203], v[144:147]\n"
        "v_mfma_f32_16x16x32_bf16 v[148:151], v[84:87], a[200:203], v[148:151]\n"
        "v_mfma_f32_16x16x32_bf16 v[152:155], v[80:83], a[204:207], v[152:155]\n"
        "v_mfma_f32_16x16x32_bf16 v[156:159], v[84:87], a[204:207], v[156:159]\n"
        "v_mfma_f32_16x16x32_bf16 v[160:163], v[80:83], a[208:211], v[160:163]\n"
        "v_mfma_f32_16x16x32_bf16 v[164:167], v[84:87], a[208:211], v[164:167]\n"
        "v_mfma_f32_16x16x32_bf16 v[168:171], v[80:83], a[212:215], v[168:171]\n"
        "v_mfma_f32_16x16x32_bf16 v[172:175], v[84:87], a[212:215], v[172:175]\n"
        "v_mfma_f32_16x16x32_bf16 v[176:179], v[80:83], a[216:219], v[176:179]\n"
        "v_mfma_f32_16x16x32_bf16 v[180:183], v[84:87], a[216:219], v[180:183]\n"
        "v_mfma_f32_16x16x32_bf16 v[184:187], v[80:83], a[220:223], v[184:187]\n"
        "v_mfma_f32_16x16x32_bf16 v[188:191], v[84:87], a[220:223], v[188:191]\n"
        "s_waitcnt vmcnt(16)\n"
        "v_max3_u32 v59, v88, v89, v90\n"
        "v_max3_u32 v59, v91, v92, v59\n"
        "v_max3_u32 v59, v93, v94, v59\n"
        "v_max_u32 v59, v95, v59\n"
        "v_cmp_eq_u32 vcc, -1, v59\n"
        "s_cbranch_vccnz Rab39_%=\n"
        "Cab39_%=:\n"
        "v_mfma_f32_16x16x32_bf16 v[144:147], v[88:91], a[232:235], v[144:147]\n"
        "v_mfma_f32_16x16x32_bf16 v[148:151], v[92:95], a[232:235], v[148:151]\n"
        "v_mfma_f32_16x16x32_bf16 v[152:155], v[88:91], a[236:239], v[152:155]\n"
        "s_waitcnt vmcnt(2)\n"
        "v_bfe_u32 v59, v33, 24, 4\n"
        "v_subrev_u32 v60, s56, v60\n"
        "v_sub_u32 v60, v60, v59\n"
        "v_cmp_lt_u32 vcc, 7, v60\n"
        "v_cmp_ne_u32 s[82:83], s57, v61\n"
        "s_or_b64 vcc, vcc, s[82:83]\n"
        "s_cbranch_vccnz Rxfd_%=\n"
        "Cxfd_%=:\n"
        "v_add_u32 v31, s25, %[vl16]\n"
        "v_add_u32 v31, 8192, v31\n"
        "global_load_dwordx4 v[224:227], v31, %[sws] offset:0 sc1\n"
        "global_load_dwordx4 v[228:231], v31, %[sws] offset:1024 sc1\n"
        "v_mfma_f32_16x16x32_bf16 v[156:159], v[92:95], a[236:239], v[156:159]\n"
        "v_mfma_f32_16x16x32_bf16 v[160:163], v[88:91], a[240:243], v[160:163]\n"
        "v_mfma_f32_16x16x32_bf16 v[164:167], v[92:95], a[240:243], v[164:167]\n"
        "v_mfma_f32_16x16x32_bf16 v[168:171], v[88:91], a[244:247], v[168:171]\n"
        "v_mfma_f32_16x16x32_bf16 v[172:175], v[92:95], a[244:247], v[172:175]\n"
        "v_mfma_f32_16x16x32_bf16 v[176:179], v[88:91], a[248:251], v[176:179]\n"
        "v_mfma_f32_16x16x32_bf16 v[180:183], v[92:95], a[248:251], v[180:183]\n"
        "v_mfma_f32_16x16x32_bf16 v[184:187], v[88:91], a[252:255], v[184:187]\n"
        "v_mfma_f32_16x16x32_bf16 v[188:191], v[92:95], a[252:255], v[188:191]\n"
        "v_mfma_f32_16x16x32_bf16 v[128:131], v[96:99], a[0:3], 0\n"
        "v_mfma_f32_16x16x32_bf16 v[132:135], v[100:103], a[0:3], 0\n"
        "v_mfma_f32_16x16x32_bf16 v[136:139], v[96:99], a[4:7], 0\n"
        "v_mfma_f32_16x16x32_bf16 v[140:143], v[100:103], a[4:7], 0\n"
        "v_mfma_f32_16x16x32_bf16 v[128:131], v[104:107], a[32:35], v[128:131]\n"
        "v_cvt_pk_bf16_f32 v144, v144, v145\n"
        "v_cvt_pk_bf16_f32 v145, v146, v147\n"
        "v_mfma_f32_16x16x32_bf16 v[128:131], v[40:43], v[248:251], v[128:131]\n"
        "v_mfma_f32_16x16x32_bf16 v[132:135], v[108:111], a[32:35], v[132:135]\n"
        "v_cvt_pk_bf16_f32 v146, v148, v149\n"
        "v_cvt_pk_bf16_f32 v147, v150, v151\n"
        "v_mfma_f32_16x16x32_bf16 v[132:135], v[42:45], v[248:251], v[132:135]\n"
        "v_mfma_f32_16x16x32_bf16 v[136:139], v[104:107], a[36:39], v[136:139]\n"
        "ds_write_b128 v48, v[144:147] offset:0\n"
        "v_mfma_f32_16x16x32_bf16 v[136:139], v[40:43], v[252:255], v[136:139]\n"
        "v_mfma_f32_16x16x32_bf16 v[140:143], v[108:111], a[36:39], v[140:143]\n"
        "v_mfma_f32_16x16x32_bf16 v[140:143], v[42:45], v[252:255], v[140:143]\n"
        "v_mfma_f32_16x16x32_bf16 v[128:131], v[112:115], a[64:67], v[128:131]\n"
        "v_cvt_pk_bf16_f32 v152, v152, v153\n"
        "v_cvt_pk_bf16_f32 v153, v154, v155\n"
        "v_lshlrev_b32 v26, 16, v232\n"
        "v_and_b32 v27, 0xffff0000, v232\n"
        "v_fma_f32 v26, v26, v20, v21\n"
        "v_mfma_f32_16x16x32_bf16 v[132:135], v[116:119], a[64:67], v[132:135]\n"
        "v_cvt_pk_bf16_f32 v154, v156, v157\n"
        "v_cvt_pk_bf16_f32 v155, v158, v159\n"
        "v_fma_f32 v27, v27, v20, v21\n"
        "v_cvt_pk_bf16_f32 v232, v26, v27\n"
        "v_mfma_f32_16x16x32_bf16 v[136:139], v[112:115], a[68:71], v[136:139]\n"
        "ds_write_b128 v48, v[152:155] offset:1024\n"
        "v_lshlrev_b32 v26, 16, v233\n"
        "v_and_b32 v27, 0xffff0000, v233\n"
        "v_fma_f32 v26, v26, v20, v21\n"
        "v_mfma_f32_16x16x32_bf16 v[140:143], v[116:119], a[68:71], v[140:143]\n"
        "v_fma_f32 v27, v27, v20, v21\n"
        "v_cvt_pk_bf16_f32 v233, v26, v27\n"
        "v_mfma_f32_16x16x32_bf16 v[128:131], v[120:123], a[96:99], v[128:131]\n"
        "v_cvt_pk_bf16_f32 v160, v160, v161\n"
        "v_cvt_pk_bf16_f32 v161, v162, v163\n"
        "v_lshlrev_b32 v26, 16, v234\n"
        "v_and_b32 v27, 0xffff0000, v234\n"
        "v_mfma_f32_16x16x32_bf16 v[132:135], v[124:127], a[96:99], v[132:135]\n"
        "v_cvt_pk_bf16_f32 v162, v164, v165\n"
        "v_cvt_pk_bf16_f32 v163, v166, v167\n"
        "v_fma_f32 v26, v26, v20, v21\n"
        "v_fma_f32 v27, v27, v20, v21\n"
        "v_cvt_pk_bf16_f32 v234, v26, v27\n"
        "v_mfma_f32_16x16x32_bf16 v[192:195], v[44:47], v[216:219], 0\n"
        "v_mfma_f32_16x16x32_bf16 v[136:139], v[120:123], a[100:103], v[136:139]\n"
        "ds_write_b128 v49, v[160:163] offset:0\n"
        "v_lshlrev_b32 v26, 16, v235\n"
        "v_and_b32 v27, 0xffff0000, v235\n"
        "v_mfma_f32_16x16x32_bf16 v[192:195], v[44:47], v[220:223], v[192:195]\n"
        "v_mfma_f32_16x16x32_bf16 v[140:143], v[124:127], a[100:103], v[140:143]\n"
        "v_fma_f32 v26, v26, v20, v21\n"
        "v_fma_f32 v27, v27, v20, v21\n"
        "v_cvt_pk_bf16_f32 v235, v26, v27\n"
        "v_mfma_f32_16x16x32_bf16 v[128:131], v[64:67], a[128:131], v[128:131]\n"
        "v_cvt_pk_bf16_f32 v168, v168, v169\n"
        "v_cvt_pk_bf16_f32 v169, v170, v171\n"
        "v_lshlrev_b32 v26, 16, v236\n"
        "v_and_b32 v27, 0xffff0000, v236\n"
        "v_mfma_f32_16x16x32_bf16 v[132:135], v[68:71], a[128:131], v[132:135]\n"
        "v_cvt_pk_bf16_f32 v170, v172, v173\n"
        "v_cvt_pk_bf16_f32 v171, v174, v175\n"
        "v_fma_f32 v26, v26, v22, v23\n"
        "v_fma_f32 v27, v27, v22, v23\n"
        "v_mfma_f32_16x16x32_bf16 v[136:139], v[64:67], a[132:135], v[136:139]\n"
        "ds_write_b128 v49, v[168:171] offset:1024\n"
        "v_cvt_pk_bf16_f32 v236, v26, v27\n"
        "v_lshlrev_b32 v26, 16, v237\n"
        "v_and_b32 v27, 0xffff0000, v237\n"
        "v_mfma_f32_16x16x32_bf16 v[140:143], v[68:71], a[132:135], v[140:143]\n"
        "v_fma_f32 v26, v26, v22, v23\n"
        "v_fma_f32 v27, v27, v22, v23\n"
        "v_mfma_f32_16x16x32_bf16 v[128:131], v[72:75], a[160:163], v[128:131]\n"
        "v_cvt_pk_bf16_f32 v176, v176, v177\n"
        "v_cvt_pk_bf16_f32 v177, v178, v179\n"
        "v_cvt_pk_bf16_f32 v237, v26, v27\n"
        "v_lshlrev_b32 v26, 16, v238\n"
        "v_mfma_f32_16x16x32_bf16 v[132:135], v[76:79], a[160:163], v[132:135]\n"
        "v_cvt_pk_bf16_f32 v178, v180, v181\n"
        "v_cvt_pk_bf16_f32 v179, v182, v183\n"
        "v_and_b32 v27, 0xffff0000, v238\n"
        "v_fma_f32 v26, v26, v22, v23\n"
        "v_fma_f32 v27, v27, v22, v23\n"
        "v_mfma_f32_16x16x32_bf16 v[136:139], v[72:75], a[164:167], v[136:139]\n"
        "ds_write_b128 v50, v[176:179] offset:0\n"
        "v_cvt_pk_bf16_f32 v238, v26, v27\n"
        "v_lshlrev_b32 v26, 16, v239\n"
        "v_mfma_f32_16x16x32_bf16 v[140:143], v[76:79], a[164:167], v[140:143]\n"
        "v_and_b32 v27, 0xffff0000, v239\n"
        "v_fma_f32 v26, v26, v22, v23\n"
        "v_fma_f32 v27, v27, v22, v23\n"
        "v_mfma_f32_16x16x32_bf16 v[128:131], v[80:83], a[192:195], v[128:131]\n"
        "v_cvt_pk_bf16_f32 v184, v184, v185\n"
        "v_cvt_pk_bf16_f32 v185, v186, v187\n"
        "v_cvt_pk_bf16_f32 v239, v26, v27\n"
        "v_lshlrev_b32 v26, 16, v240\n"
        "v_mfma_f32_16x16x32_bf16 v[132:135], v[84:87], a[192:195], v[132:135]\n"
        "v_cvt_pk_bf16_f32 v186, v188, v189\n"
        "v_cvt_pk_bf16_f32 v187, v190, v191\n"
        "v_and_b32 v27, 0xffff0000, v240\n"
        "v_fma_f32 v26, v26, v24, v25\n"
        "v_mfma_f32_16x16x32_bf16 v[136:139], v[80:83], a[196:199], v[136:139]\n"
        "ds_write_b128 v50, v[184:187] offset:1024\n"
        "v_fma_f32 v27, v27, v24, v25\n"
        "v_cvt_pk_bf16_f32 v240, v26, v27\n"
        "v_lshlrev_b32 v26, 16, v241\n"
        "v_add_u32 v31, s25, %[vl16]\n"
        "v_add_u32 v31, 16384, v31\n"
        "global_load_dwordx4 v[208:211], v31, %[sws] offset:0 sc1\n"
        "global_load_dwordx4 v[212:215], v31, %[sws] offset:1024 sc1\n"
        "v_add_u32 v31, s36, v39\n"
        "global_load_dwordx4 v[96:99], v31, %[sws] offset:0 sc1\n"
        "global_load_dwordx4 v[100:103], v31, %[sws] offset:256 sc1\n"
        "v_lshl_add_u32 v31, s50, 3, v31\n"
        "global_load_dwordx4 v[104:107], v31, %[sws] sc1\n"
        "v_add_u32 v31, s37, %[vl16]\n"
        "global_load_dwordx4 v[108:111], v31, %[sws] offset:0 sc1\n"
        "global_load_dwordx4 v[112:115], v31, %[sws] offset:1024 sc1\n"
        "global_load_dwordx2 v[116:117], v16, %[sws] offset:0 sc1\n"
        "global_load_dwordx2 v[118:119], v16, %[sws] offset:32 sc1\n"
        "global_load_dwordx2 v[120:121], v17, %[sws] offset:0 sc1\n"
        "global_load_dwordx2 v[122:123], v17, %[sws] offset:32 sc1\n"
        "global_load_dwordx2 v[124:125], v18, %[sws] sc1\n"
        "v_mfma_f32_16x16x32_bf16 v[140:143], v[84:87], a[196:199], v[140:143]\n"
        "v_and_b32 v27, 0xffff0000, v241\n"
        "v_fma_f32 v26, v26, v24, v25\n"
        "v_mfma_f32_16x16x32_bf16 v[128:131], v[88:91], a[224:227], v[128:131]\n"
        "v_fma_f32 v27, v27, v24, v25\n"
        "v_cvt_pk_bf16_f32 v241, v26, v27\n"
        "v_mfma_f32_16x16x32_bf16 v[132:135], v[92:95], a[224:227], v[132:135]\n"
        "v_mfma_f32_16x16x32_bf16 v[136:139], v[88:91], a[228:231], v[136:139]\n"
        "v_mfma_f32_16x16x32_bf16 v[140:143], v[92:95], a[228:231], v[140:143]\n"
        "s_waitcnt lgkmcnt(0)\n"
        "s_barrier\n"
        "ds_read_b128 v[64:67], v51 offset:0\n"
        "ds_read_b128 v[68:71], v51 offset:1024\n"
        "ds_read_b128 v[72:75], v51 offset:2048\n"
        "ds_read_b128 v[76:79], v51 offset:3072\n"
        "ds_read_b128 v[80:83], v51 offset:4096\n"
        "ds_read_b128 v[84:87], v51 offset:5120\n"
        "s_waitcnt vmcnt(12)\n"
        "v_mfma_f32_16x16x32_bf16 v[128:131], v[40:43], v[224:227], v[128:131]\n"
        "v_mfma_f32_16x16x32_bf16 v[132:135], v[42:45], v[224:227], v[132:135]\n"
        "v_mfma_f32_16x16x32_bf16 v[136:139], v[40:43], v[228:231], v[136:139]\n"
        "v_mfma_f32_16x16x32_bf16 v[140:143], v[42:45], v[228:231], v[140:143]\n"
        "s_waitcnt lgkmcnt(5)\n"
        "v_mfma_f32_16x16x32_bf16 v[128:131], v[40:43], v[64:67], v[128:131]\n"
        "v_mfma_f32_16x16x32_bf16 v[132:135], v[42:45], v[64:67], v[132:135]\n"
        "s_waitcnt lgkmcnt(4)\n"
        "v_mfma_f32_16x16x32_bf16 v[136:139], v[40:43], v[68:71], v[136:139]\n"
        "v_mfma_f32_16x16x32_bf16 v[140:143], v[42:45], v[68:71], v[140:143]\n"
        "s_waitcnt lgkmcnt(3)\n"
        "v_mfma_f32_16x16x32_bf16 v[128:131], v[40:43], v[72:75], v[128:131]\n"
        "v_mfma_f32_16x16x32_bf16 v[132:135], v[42:45], v[72:75], v[132:135]\n"
        "s_waitcnt lgkmcnt(2)\n"
        "v_mfma_f32_16x16x32_bf16 v[136:139], v[40:43], v[76:79], v[136:139]\n"
        "v_mfma_f32_16x16x32_bf16 v[140:143], v[42:45], v[76:79], v[140:143]\n"
        "s_waitcnt lgkmcnt(1)\n"
        "v_mfma_f32_16x16x32_bf16 v[128:131], v[40:43], v[80:83], v[128:131]\n"
        "v_mfma_f32_16x16x32_bf16 v[132:135], v[42:45], v[80:83], v[132:135]\n"
        "s_waitcnt lgkmcnt(0)\n"
        "v_mfma_f32_16x16x32_bf16 v[136:139], v[40:43], v[84:87], v[136:139]\n"
        "v_mfma_f32_16x16x32_bf16 v[140:143], v[42:45], v[84:87], v[140:143]\n"
        "s_waitcnt vmcnt(0)\n"
        "v_max3_u32 v59, v208, v209, v210\n"
        "v_max3_u32 v59, v211, v212, v59\n"
        "v_max3_u32 v59, v213, v214, v59\n"
        "v_max3_u32 v59, v215, v96, v59\n"
        "v_max3_u32 v59, v97, v98, v59\n"
        "v_max3_u32 v59, v99, v100, v59\n"
        "v_max3_u32 v59, v101, v102, v59\n"
        "v_max3_u32 v59, v103, v104, v59\n"
        "v_max3_u32 v59, v105, v106, v59\n"
        "v_max3_u32 v59, v107, v108, v59\n"
        "v_max3_u32 v59, v109, v110, v59\n"
        "v_max3_u32 v59, v111, v112, v59\n"
        "v_max3_u32 v59, v113, v114, v59\n"
        "v_max_u32 v59, v115, v59\n"
        "v_cmp_eq_u32 vcc, -1, v59\n"
        "v_max_u32 v59, v116, v117\n"
        "v_max3_u32 v59, v118, v119, v59\n"
        "v_cmp_eq_u32 s[82:83], -1, v59\n"
        "s_and_b64 s[82:83], s[82:83], s[60:61]\n"
        "s_or_b64 vcc, vcc, s[82:83]\n"
        "v_max_u32 v59, v120, v121\n"
        "v_max3_u32 v59, v122, v123, v59\n"
        "v_cmp_eq_u32 s[82:83], -1, v59\n"
        "s_and_b64 s[82:83], s[82:83], s[62:63]\n"
        "s_or_b64 vcc, vcc, s[82:83]\n"
        "v_max_u32 v59, v124, v125\n"
        "v_cmp_eq_u32 s[82:83], -1, v59\n"
        "s_and_b64 s[82:83], s[82:83], s[64:65]\n"
        "s_or_b64 vcc, vcc, s[82:83]\n"
        "s_cbranch_vccnz Rlate_%=\n"
        "Clate_%=:\n"
        "v_cndmask_b32 v232, v232, v116, s[60:61]\n"
        "v_cndmask_b32 v233, v233, v117, s[60:61]\n"
        "v_cndmask_b32 v234, v234, v118, s[60:61]\n"
        "v_cndmask_b32 v235, v235, v119, s[60:61]\n"
        "v_cndmask_b32 v236, v236, v120, s[62:63]\n"
        "v_cndmask_b32 v237, v237, v121, s[62:63]\n"
        "v_cndmask_b32 v238, v238, v122, s[62:63]\n"
        "v_cndmask_b32 v239, v239, v123, s[62:63]\n"
        "v_cndmask_b32 v240, v240, v124, s[64:65]\n"
        "v_cndmask_b32 v241, v241, v125, s[64:65]\n"
        "s_cmp_lt_u32 s49, 2\n"
        "s_cselect_b64 s[82:83], -1, 0\n"
        "v_mfma_f32_16x16x32_bf16 v[128:131], v[40:43], v[208:211], v[128:131]\n"
        "v_mfma_f32_16x16x32_bf16 v[132:135], v[42:45], v[208:211], v[132:135]\n"
        "v_mfma_f32_16x16x32_bf16 v[136:139], v[40:43], v[212:215], v[136:139]\n"
        "v_mfma_f32_16x16x32_bf16 v[140:143], v[42:45], v[212:215], v[140:143]\n"
        "v_cndmask_b32 v116, v112, v108, s[82:83]\n"
        "v_cndmask_b32 v117, v113, v109, s[82:83]\n"
        "v_cndmask_b32 v118, v114, v110, s[82:83]\n"
        "v_cndmask_b32 v119, v115, v111, s[82:83]\n"
        "v_mfma_f32_16x16x32_bf16 v[128:131], v[96:99], v[108:111], v[128:131]\n"
        "v_mfma_f32_16x16x32_bf16 v[132:135], v[100:103], v[108:111], v[132:135]\n"
        "v_mfma_f32_16x16x32_bf16 v[136:139], v[96:99], v[112:115], v[136:139]\n"
        "v_mfma_f32_16x16x32_bf16 v[140:143], v[100:103], v[112:115], v[140:143]\n"
        "v_mfma_f32_16x16x32_bf16 v[128:131], v[40:43], v[232:235], v[128:131]\n"
        "v_mfma_f32_16x16x32_bf16 v[132:135], v[42:45], v[232:235], v[132:135]\n"
        "v_mfma_f32_16x16x32_bf16 v[136:139], v[40:43], v[236:239], v[136:139]\n"
        "v_mfma_f32_16x16x32_bf16 v[140:143], v[42:45], v[236:239], v[140:143]\n"
        "v_mfma_f32_16x16x32_bf16 v[192:195], v[104:107], v[116:119], v[192:195]\n"
        "v_mfma_f32_16x16x32_bf16 v[192:195], v[40:43], v[240:243], v[192:195]\n"
        "s_barrier\n"
        "v_mov_b32 v64, 0x3f803f80\n"
        "v_mov_b32 v65, 0x3f803f80\n"
        "v_mov_b32 v66, 0x3f803f80\n"
        "v_mov_b32 v67, 0x3f803f80\n"
        "s_nop 2\n"
        "v_cvt_pk_bf16_f32 v120, v128, v129\n"
        "v_cvt_pk_bf16_f32 v121, v130, v131\n"
        "v_cvt_pk_bf16_f32 v122, v132, v133\n"
        "v_cvt_pk_bf16_f32 v123, v134, v135\n"
        "v_cvt_pk_bf16_f32 v124, v136, v137\n"
        "v_cvt_pk_bf16_f32 v125, v138, v139\n"
        "v_cvt_pk_bf16_f32 v126, v140, v141\n"
        "v_cvt_pk_bf16_f32 v127, v142, v143\n"
        "v_cvt_pk_bf16_f32 v118, v192, v193\n"
        "v_cvt_pk_bf16_f32 v119, v194, v195\n"
        "v_permlane32_swap_b32 v120, v122\n"
        "v_permlane32_swap_b32 v121, v123\n"
        "v_permlane32_swap_b32 v124, v126\n"
        "v_permlane32_swap_b32 v125, v127\n"
        "v_min_u32 v118, -2, v118\n"
        "v_min_u32 v119, -2, v119\n"
        "v_permlane16_swap_b32 v120, v122\n"
        "v_permlane16_swap_b32 v121, v123\n"
        "v_permlane16_swap_b32 v124, v126\n"
        "v_permlane16_swap_b32 v125, v127\n"
        "v_add_u32 v62, 6144, %[vl16]\n"
        "global_load_dwordx4 v[224:227], v62, %[sws] offset:0 sc1\n"
        "global_load_dwordx4 v[228:231], v62, %[sws] offset:1024 sc1\n"
        "v_add_u32 v31, s32, v36\n"
        "global_store_dwordx2 v31, v[118:119], %[sws] sc1\n"
        "v_add_u32 v31, s31, %[vl16]\n"
        "v_min_u32 v120, -2, v120\n"
        "v_min_u32 v121, -2, v121\n"
        "v_min_u32 v122, -2, v122\n"
        "v_min_u32 v123, -2, v123\n"
        "v_min_u32 v124, -2, v124\n"
        "v_min_u32 v125, -2, v125\n"
        "v_min_u32 v126, -2, v126\n"
        "v_min_u32 v127, -2, v127\n"
        "v_add_u32 v58, 20480, v31\n"
        "global_store_dwordx4 v31, v[120:123], %[sws] sc1\n"
        "global_store_dwordx4 v58, v[124:127], %[sws] sc1\n"
        "v_mfma_f32_16x16x16_bf16 v[196:199], v[118:119], v[64:65], 0\n"
        "v_mfma_f32_16x16x16_bf16 v[200:203], v[118:119], v[118:119], 0\n"
        "v_mfma_f32_16x16x16_bf16 v[204:207], v[118:119], v[242:243], 0\n"
        "v_mfma_f32_16x16x32_bf16 v[128:131], v[120:123], v[64:67], 0\n"
        "v_mfma_f32_16x16x32_bf16 v[132:135], v[120:123], v[120:123], 0\n"
        "v_mfma_f32_16x16x32_bf16 v[136:139], v[120:123], v[244:247], 0\n"
        "v_mfma_f32_16x16x32_bf16 v[140:143], v[124:127], v[64:67], 0\n"
        "v_mfma_f32_16x16x32_bf16 v[144:147], v[124:127], v[124:127], 0\n"
        "v_mfma_f32_16x16x32_bf16 v[148:151], v[124:127], v[244:247], 0\n"
        "s_mov_b32 s60, 0xaaaaaaaa\n"
        "s_mov_b32 s61, 0xaaaaaaaa\n"
        "s_mov_b32 s62, 0xcccccccc\n"
        "s_mov_b32 s63, 0xcccccccc\n"
        "s_cmp_lt_u32 s49, 2\n"
        "s_cselect_b32 s64, 1.0, 0\n"
        "s_cselect_b32 s65, 0, 1.0\n"
        "v_cndmask_b32 v196, v196, v197, s[60:61]\n"
        "v_cndmask_b32 v198, v198, v199, s[60:61]\n"
        "v_cndmask_b32 v196, v196, v198, s[62:63]\n"
        "v_cndmask_b32 v200, v200, v201, s[60:61]\n"
        "v_cndmask_b32 v202, v202, v203, s[60:61]\n"
        "v_cndmask_b32 v200, v200, v202, s[62:63]\n"
        "v_cndmask_b32 v204, v204, v205, s[60:61]\n"
        "v_cndmask_b32 v206, v206, v207, s[60:61]\n"
        "v_cndmask_b32 v204, v204, v206, s[62:63]\n"
        "v_cndmask_b32 v128, v128, v129, s[60:61]\n"
        "v_cndmask_b32 v130, v130, v131, s[60:61]\n"
        "v_cndmask_b32 v160, v128, v130, s[62:63]\n"
        "v_cndmask_b32 v132, v132, v133, s[60:61]\n"
        "v_cndmask_b32 v134, v134, v135, s[60:61]\n"
        "v_cndmask_b32 v161, v132, v134, s[62:63]\n"
        "v_cndmask_b32 v136, v136, v137, s[60:61]\n"
        "v_cndmask_b32 v138, v138, v139, s[60:61]\n"
        "v_cndmask_b32 v162, v136, v138, s[62:63]\n"
        "v_fmac_f32 v160, s64, v196\n"
        "v_fmac_f32 v161, s64, v200\n"
        "v_fmac_f32 v162, s64, v204\n"
        "v_min_u32 v160, -2, v160\n"
        "v_min_u32 v161, -2, v161\n"
        "v_min_u32 v162, -2, v162\n"
        "v_mov_b32 v163, 0\n"
        "v_cndmask_b32 v140, v140, v141, s[60:61]\n"
        "v_cndmask_b32 v142, v142, v143, s[60:61]\n"
        "v_cndmask_b32 v164, v140, v142, s[62:63]\n"
        "v_cndmask_b32 v144, v144, v145, s[60:61]\n"
        "v_cndmask_b32 v146, v146, v147, s[60:61]\n"
        "v_cndmask_b32 v165, v144, v146, s[62:63]\n"
        "v_cndmask_b32 v148, v148, v149, s[60:61]\n"
        "v_cndmask_b32 v150, v150, v151, s[60:61]\n"
        "v_cndmask_b32 v166, v148, v150, s[62:63]\n"
        "v_fmac_f32 v164, s65, v196\n"
        "v_fmac_f32 v165, s65, v200\n"
        "v_fmac_f32 v166, s65, v204\n"
        "v_min_u32 v164, -2, v164\n"
        "v_min_u32 v165, -2, v165\n"
        "v_min_u32 v166, -2, v166\n"
        "v_mov_b32 v167, 0\n"
        "v_and_b32 v58, 0xf0, %[vl16]\n"
        "v_add_u32 v58, s33, v58\n"
        "s_mov_b32 exec_lo, 0x00f0000f\n"
        "s_mov_b32 exec_hi, 0xf0000f00\n"
        "global_store_dwordx4 v58, v[160:163], %[sws] sc1\n"
        "global_store_dwordx4 v58, v[164:167], %[sws] offset:256 sc1\n"
        "s_mov_b64 exec, -1\n"
        "s_waitcnt vmcnt(5)\n"
        "v_cmp_ne_u32 vcc, s56, v224\n"
        "v_cmp_ne_u32 s[82:83], s57, v225\n"
        "s_or_b64 vcc, vcc, s[82:83]\n"
        "v_cmp_ne_u32 s[82:83], s56, v226\n"
        "s_or_b64 vcc, vcc, s[82:83]\n"
        "v_cmp_ne_u32 s[82:83], s57, v227\n"
        "s_or_b64 vcc, vcc, s[82:83]\n"
        "v_cmp_ne_u32 s[82:83], s56, v228\n"
        "s_or_b64 vcc, vcc, s[82:83]\n"
        "v_cmp_ne_u32 s[82:83], s57, v229\n"
        "s_or_b64 vcc, vcc, s[82:83]\n"
        "v_cmp_ne_u32 s[82:83], s56, v230\n"
        "s_or_b64 vcc, vcc, s[82:83]\n"
        "v_cmp_ne_u32 s[82:83], s57, v231\n"
        "s_or_b64 vcc, vcc, s[82:83]\n"
        "s_cbranch_vccnz Rcf_%=\n"
        "Ccf_%=:\n"
        "s_nop 1\n"
        "s_branch END_%=\n"
        "Rab24_%=:\n"
        "s_sub_u32 s81, s81, 1\n"
        "s_cbranch_scc1 Cab24_%=\n"
        "s_sleep 1\n"
        "s_bfe_u32 s59, s48, 262156\n"
        "s_lshl_b32 s59, s59, 14\n"
        "s_add_u32 s59, s59, s24\n"
        "v_add_u32 v62, s59, %[vl16]\n"
        "global_load_dwordx4 v[96:99], v62, %[sws] offset:-4096 sc1\n"
        "v_add_u32 v62, 8192, v62\n"
        "global_load_dwordx4 v[100:103], v62, %[sws] offset:-4096 sc1\n"
        "s_waitcnt vmcnt(0)\n"
        "v_max3_u32 v59, v96, v97, v98\n"
        "v_max3_u32 v59, v99, v100, v59\n"
        "v_max3_u32 v59, v101, v102, v59\n"
        "v_max_u32 v59, v103, v59\n"
        "v_cmp_eq_u32 vcc, -1, v59\n"
        "s_cbranch_vccnz Rab24_%=\n"
        "s_branch Cab24_%=\n"
        "Rab25_%=:\n"
        "s_sub_u32 s81, s81, 1\n"
        "s_cbranch_scc1 Cab25_%=\n"
        "s_sleep 1\n"
        "s_bfe_u32 s59, s48, 262156\n"
        "s_lshl_b32 s59, s59, 14\n"
        "s_add_u32 s59, s59, s24\n"
        "v_add_u32 v62, s59, %[vl16]\n"
        "global_load_dwordx4 v[104:107], v62, %[sws] offset:-3072 sc1\n"
        "v_add_u32 v62, 8192, v62\n"
        "global_load_dwordx4 v[108:111], v62, %[sws] offset:-3072 sc1\n"
        "s_waitcnt vmcnt(0)\n"
        "v_max3_u32 v59, v104, v105, v106\n"
        "v_max3_u32 v59, v107, v108, v59\n"
        "v_max3_u32 v59, v109, v110, v59\n"
        "v_max_u32 v59, v111, v59\n"
        "v_cmp_eq_u32 vcc, -1, v59\n"
        "s_cbranch_vccnz Rab25_%=\n"
        "s_branch Cab25_%=\n"
        "Rab26_%=:\n"
        "s_sub_u32 s81, s81, 1\n"
        "s_cbranch_scc1 Cab26_%=\n"
        "s_sleep 1\n"
        "s_bfe_u32 s59, s48, 262156\n"
        "s_lshl_b32 s59, s59, 14\n"
        "s_add_u32 s59, s59, s24\n"
        "v_add_u32 v62, s59, %[vl16]\n"
        "global_load_dwordx4 v[112:115], v62, %[sws] offset:-2048 sc1\n"
        "v_add_u32 v62, 8192, v62\n"
        "global_load_dwordx4 v[116:119], v62, %[sws] offset:-2048 sc1\n"
        "s_waitcnt vmcnt(0)\n"
        "v_max3_u32 v59, v112, v113, v114\n"
        "v_max3_u32 v59, v115, v116, v59\n"
        "v_max3_u32 v59, v117, v118, v59\n"
        "v_max_u32 v59, v119, v59\n"
        "v_cmp_eq_u32 vcc, -1, v59\n"
        "s_cbranch_vccnz Rab26_%=\n"
        "s_branch Cab26_%=\n"
        "Rab27_%=:\n"
        "s_sub_u32 s81, s81, 1\n"
        "s_cbranch_scc1 Cab27_%=\n"
        "s_sleep 1\n"
        "s_bfe_u32 s59, s48, 262156\n"
        "s_lshl_b32 s59, s59, 14\n"
        "s_add_u32 s59, s59, s24\n"
        "v_add_u32 v62, s59, %[vl16]\n"
        "global_load_dwordx4 v[120:123], v62, %[sws] offset:-1024 sc1\n"
        "v_add_u32 v62, 8192, v62\n"
        "global_load_dwordx4 v[124:127], v62, %[sws] offset:-1024 sc1\n"
        "s_waitcnt vmcnt(0)\n"
        "v_max3_u32 v59, v120, v121, v122\n"
        "v_max3_u32 v59, v123, v124, v59\n"
        "v_max3_u32 v59, v125, v126, v59\n"
        "v_max_u32 v59, v127, v59\n"
        "v_cmp_eq_u32 vcc, -1, v59\n"
        "s_cbranch_vccnz Rab27_%=\n"
        "s_branch Cab27_%=\n"
        "Rab28_%=:\n"
        "s_sub_u32 s81, s81, 1\n"
        "s_cbranch_scc1 Cab28_%=\n"
        "s_sleep 1\n"
        "s_bfe_u32 s59, s48, 262156\n"
        "s_lshl_b32 s59, s59, 14\n"
        "s_add_u32 s59, s59, s24\n"
        "v_add_u32 v62, s59, %[vl16]\n"
        "global_load_dwordx4 v[64:67], v62, %[sws] offset:0 sc1\n"
        "v_add_u32 v62, 8192, v62\n"
        "global_load_dwordx4 v[68:71], v62, %[sws] offset:0 sc1\n"
        "s_waitcnt vmcnt(0)\n"
        "v_max3_u32 v59, v64, v65, v66\n"
        "v_max3_u32 v59, v67, v68, v59\n"
        "v_max3_u32 v59, v69, v70, v59\n"
        "v_max_u32 v59, v71, v59\n"
        "v_cmp_eq_u32 vcc, -1, v59\n"
        "s_cbranch_vccnz Rab28_%=\n"
        "s_branch Cab28_%=\n"
        "Rab29_%=:\n"
        "s_sub_u32 s81, s81, 1\n"
        "s_cbranch_scc1 Cab29_%=\n"
        "s_sleep 1\n"
        "s_bfe_u32 s59, s48, 262156\n"
        "s_lshl_b32 s59, s59, 14\n"
        "s_add_u32 s59, s59, s24\n"
        "v_add_u32 v62, s59, %[vl16]\n"
        "global_load_dwordx4 v[72:75], v62, %[sws] offset:1024 sc1\n"
        "v_add_u32 v62, 8192, v62\n"
        "global_load_dwordx4 v[76:79], v62, %[sws] offset:1024 sc1\n"
        "s_waitcnt vmcnt(0)\n"
        "v_max3_u32 v59, v72, v73, v74\n"
        "v_max3_u32 v59, v75, v76, v59\n"
        "v_max3_u32 v59, v77, v78, v59\n"
        "v_max_u32 v59, v79, v59\n"
        "v_cmp_eq_u32 vcc, -1, v59\n"
        "s_cbranch_vccnz Rab29_%=\n"
        "s_branch Cab29_%=\n"
        "Rab30_%=:\n"
        "s_sub_u32 s81, s81, 1\n"
        "s_cbranch_scc1 Cab30_%=\n"
        "s_sleep 1\n"
        "s_bfe_u32 s59, s48, 262156\n"
        "s_lshl_b32 s59, s59, 14\n"
        "s_add_u32 s59, s59, s24\n"
        "v_add_u32 v62, s59, %[vl16]\n"
        "global_load_dwordx4 v[80:83], v62, %[sws] offset:2048 sc1\n"
        "v_add_u32 v62, 8192, v62\n"
        "global_load_dwordx4 v[84:87], v62, %[sws] offset:2048 sc1\n"
        "s_waitcnt vmcnt(0)\n"
        "v_max3_u32 v59, v80, v81, v82\n"
        "v_max3_u32 v59, v83, v84, v59\n"
        "v_max3_u32 v59, v85, v86, v59\n"
        "v_max_u32 v59, v87, v59\n"
        "v_cmp_eq_u32 vcc, -1, v59\n"
        "s_cbranch_vccnz Rab30_%=\n"
        "s_branch Cab30_%=\n"
        "Rix_%=:\n"
        "s_sub_u32 s81, s81, 1\n"
        "s_cbranch_scc1 Cix_%=\n"
        "s_sleep 1\n"
        "global_load_dwordx4 v[16:19], v24, %[sws] offset:0 sc1\n"
        "global_load_dwordx4 v[20:23], v24, %[sws] offset:16 sc1\n"
        "s_waitcnt vmcnt(0)\n"
        "v_max3_u32 v59, v16, v17, v18\n"
        "v_max3_u32 v59, v19, v20, v59\n"
        "v_max3_u32 v59, v21, v22, v59\n"
        "v_max_u32 v59, v23, v59\n"
        "v_cmp_eq_u32 vcc, -1, v59\n"
        "s_cbranch_vccnz Rix_%=\n"
        "s_branch Cix_%=\n"
        "Rab31_%=:\n"
        "s_sub_u32 s81, s81, 1\n"
        "s_cbranch_scc1 Cab31_%=\n"
        "s_sleep 1\n"
        "s_bfe_u32 s59, s48, 262156\n"
        "s_lshl_b32 s59, s59, 14\n"
        "s_add_u32 s59, s59, s24\n"
        "v_add_u32 v62, s59, %[vl16]\n"
        "global_load_dwordx4 v[88:91], v62, %[sws] offset:3072 sc1\n"
        "v_add_u32 v62, 8192, v62\n"
        "global_load_dwordx4 v[92:95], v62, %[sws] offset:3072 sc1\n"
        "s_waitcnt vmcnt(0)\n"
        "v_max3_u32 v59, v88, v89, v90\n"
        "v_max3_u32 v59, v91, v92, v59\n"
        "v_max3_u32 v59, v93, v94, v59\n"
        "v_max_u32 v59, v95, v59\n"
        "v_cmp_eq_u32 vcc, -1, v59\n"
        "s_cbranch_vccnz Rab31_%=\n"
        "s_branch Cab31_%=\n"
        "Rab32_%=:\n"
        "s_sub_u32 s81, s81, 1\n"
        "s_cbranch_scc1 Cab32_%=\n"
        "s_sleep 1\n"
        "s_bfe_u32 s59, s48, 262160\n"
        "s_lshl_b32 s59, s59, 14\n"
        "s_add_u32 s59, s59, s24\n"
        "v_add_u32 v62, s59, %[vl16]\n"
        "global_load_dwordx4 v[96:99], v62, %[sws] offset:-4096 sc1\n"
        "v_add_u32 v62, 8192, v62\n"
        "global_load_dwordx4 v[100:103], v62, %[sws] offset:-4096 sc1\n"
        "s_waitcnt vmcnt(0)\n"
        "v_max3_u32 v59, v96, v97, v98\n"
        "v_max3_u32 v59, v99, v100, v59\n"
        "v_max3_u32 v59, v101, v102, v59\n"
        "v_max_u32 v59, v103, v59\n"
        "v_cmp_eq_u32 vcc, -1, v59\n"
        "s_cbranch_vccnz Rab32_%=\n"
        "s_branch Cab32_%=\n"
        "Rab33_%=:\n"
        "s_sub_u32 s81, s81, 1\n"
        "s_cbranch_scc1 Cab33_%=\n"
        "s_sleep 1\n"
        "s_bfe_u32 s59, s48, 262160\n"
        "s_lshl_b32 s59, s59, 14\n"
        "s_add_u32 s59, s59, s24\n"
        "v_add_u32 v62, s59, %[vl16]\n"
        "global_load_dwordx4 v[104:107], v62, %[sws] offset:-3072 sc1\n"
        "v_add_u32 v62, 8192, v62\n"
        "global_load_dwordx4 v[108:111], v62, %[sws] offset:-3072 sc1\n"
        "s_waitcnt vmcnt(0)\n"
        "v_max3_u32 v59, v104, v105, v106\n"
        "v_max3_u32 v59, v107, v108, v59\n"
        "v_max3_u32 v59, v109, v110, v59\n"
        "v_max_u32 v59, v111, v59\n"
        "v_cmp_eq_u32 vcc, -1, v59\n"
        "s_cbranch_vccnz Rab33_%=\n"
        "s_branch Cab33_%=\n"
        "Rxfc_%=:\n"
        "s_sub_u32 s81, s81, 1\n"
        "s_cbranch_scc1 Cxfc_%=\n"
        "s_sleep 1\n"
        "global_load_dwordx2 v[60:61], v34, %[sws] sc1\n"
        "s_waitcnt vmcnt(0)\n"
        "v_bfe_u32 v59, v33, 20, 4\n"
        "v_subrev_u32 v60, s56, v60\n"
        "v_sub_u32 v60, v60, v59\n"
        "v_cmp_lt_u32 vcc, 7, v60\n"
        "v_cmp_ne_u32 s[82:83], s57, v61\n"
        "s_or_b64 vcc, vcc, s[82:83]\n"
        "s_cbranch_vccnz Rxfc_%=\n"
        "s_branch Cxfc_%=\n"
        "Rab34_%=:\n"
        "s_sub_u32 s81, s81, 1\n"
        "s_cbranch_scc1 Cab34_%=\n"
        "s_sleep 1\n"
        "s_bfe_u32 s59, s48, 262160\n"
        "s_lshl_b32 s59, s59, 14\n"
        "s_add_u32 s59, s59, s24\n"
        "v_add_u32 v62, s59, %[vl16]\n"
        "global_load_dwordx4 v[112:115], v62, %[sws] offset:-2048 sc1\n"
        "v_add_u32 v62, 8192, v62\n"
        "global_load_dwordx4 v[116:119], v62, %[sws] offset:-2048 sc1\n"
        "s_waitcnt vmcnt(0)\n"
        "v_max3_u32 v59, v112, v113, v114\n"
        "v_max3_u32 v59, v115, v116, v59\n"
        "v_max3_u32 v59, v117, v118, v59\n"
        "v_max_u32 v59, v119, v59\n"
        "v_cmp_eq_u32 vcc, -1, v59\n"
        "s_cbranch_vccnz Rab34_%=\n"
        "s_branch Cab34_%=\n"
        "Rab35_%=:\n"
        "s_sub_u32 s81, s81, 1\n"
        "s_cbranch_scc1 Cab35_%=\n"
        "s_sleep 1\n"
        "s_bfe_u32 s59, s48, 262160\n"
        "s_lshl_b32 s59, s59, 14\n"
        "s_add_u32 s59, s59, s24\n"
        "v_add_u32 v62, s59, %[vl16]\n"
        "global_load_dwordx4 v[120:123], v62, %[sws] offset:-1024 sc1\n"
        "v_add_u32 v62, 8192, v62\n"
        "global_load_dwordx4 v[124:127], v62, %[sws] offset:-1024 sc1\n"
        "s_waitcnt vmcnt(0)\n"
        "v_max3_u32 v59, v120, v121, v122\n"
        "v_max3_u32 v59, v123, v124, v59\n"
        "v_max3_u32 v59, v125, v126, v59\n"
        "v_max_u32 v59, v127, v59\n"
        "v_cmp_eq_u32 vcc, -1, v59\n"
        "s_cbranch_vccnz Rab35_%=\n"
        "s_branch Cab35_%=\n"
        "Rab36_%=:\n"
        "s_sub_u32 s81, s81, 1\n"
        "s_cbranch_scc1 Cab36_%=\n"
        "s_sleep 1\n"
        "s_bfe_u32 s59, s48, 262160\n"
        "s_lshl_b32 s59, s59, 14\n"
        "s_add_u32 s59, s59, s24\n"
        "v_add_u32 v62, s59, %[vl16]\n"
        "global_load_dwordx4 v[64:67], v62, %[sws] offset:0 sc1\n"
        "v_add_u32 v62, 8192, v62\n"
        "global_load_dwordx4 v[68:71], v62, %[sws] offset:0 sc1\n"
        "s_waitcnt vmcnt(0)\n"
        "v_max3_u32 v59, v64, v65, v66\n"
        "v_max3_u32 v59, v67, v68, v59\n"
        "v_max3_u32 v59, v69, v70, v59\n"
        "v_max_u32 v59, v71, v59\n"
        "v_cmp_eq_u32 vcc, -1, v59\n"
        "s_cbranch_vccnz Rab36_%=\n"
        "s_branch Cab36_%=\n"
        "Rab37_%=:\n"
        "s_sub_u32 s81, s81, 1\n"
        "s_cbranch_scc1 Cab37_%=\n"
        "s_sleep 1\n"
        "s_bfe_u32 s59, s48, 262160\n"
        "s_lshl_b32 s59, s59, 14\n"
        "s_add_u32 s59, s59, s24\n"
        "v_add_u32 v62, s59, %[vl16]\n"
        "global_load_dwordx4 v[72:75], v62, %[sws] offset:1024 sc1\n"
        "v_add_u32 v62, 8192, v62\n"
        "global_load_dwordx4 v[76:79], v62, %[sws] offset:1024 sc1\n"
        "s_waitcnt vmcnt(0)\n"
        "v_max3_u32 v59, v72, v73, v74\n"
        "v_max3_u32 v59, v75, v76, v59\n"
        "v_max3_u32 v59, v77, v78, v59\n"
        "v_max_u32 v59, v79, v59\n"
        "v_cmp_eq_u32 vcc, -1, v59\n"
        "s_cbranch_vccnz Rab37_%=\n"
        "s_branch Cab37_%=\n"
        "Rab38_%=:\n"
        "s_sub_u32 s81, s81, 1\n"
        "s_cbranch_scc1 Cab38_%=\n"
        "s_sleep 1\n"
        "s_bfe_u32 s59, s48, 262160\n"
        "s_lshl_b32 s59, s59, 14\n"
        "s_add_u32 s59, s59, s24\n"
        "v_add_u32 v62, s59, %[vl16]\n"
        "global_load_dwordx4 v[80:83], v62, %[sws] offset:2048 sc1\n"
        "v_add_u32 v62, 8192, v62\n"
        "global_load_dwordx4 v[84:87], v62, %[sws] offset:2048 sc1\n"
        "s_waitcnt vmcnt(0)\n"
        "v_max3_u32 v59, v80, v81, v82\n"
        "v_max3_u32 v59, v83, v84, v59\n"
        "v_max3_u32 v59, v85, v86, v59\n"
        "v_max_u32 v59, v87, v59\n"
        "v_cmp_eq_u32 vcc, -1, v59\n"
        "s_cbranch_vccnz Rab38_%=\n"
        "s_branch Cab38_%=\n"
        "Rab39_%=:\n"
        "s_sub_u32 s81, s81, 1\n"
        "s_cbranch_scc1 Cab39_%=\n"
        "s_sleep 1\n"
        "s_bfe_u32 s59, s48, 262160\n"
        "s_lshl_b32 s59, s59, 14\n"
        "s_add_u32 s59, s59, s24\n"
        "v_add_u32 v62, s59, %[vl16]\n"
        "global_load_dwordx4 v[88:91], v62, %[sws] offset:3072 sc1\n"
        "v_add_u32 v62, 8192, v62\n"
        "global_load_dwordx4 v[92:95], v62, %[sws] offset:3072 sc1\n"
        "s_waitcnt vmcnt(0)\n"
        "v_max3_u32 v59, v88, v89, v90\n"
        "v_max3_u32 v59, v91, v92, v59\n"
        "v_max3_u32 v59, v93, v94, v59\n"
        "v_max_u32 v59, v95, v59\n"
        "v_cmp_eq_u32 vcc, -1, v59\n"
        "s_cbranch_vccnz Rab39_%=\n"
        "s_branch Cab39_%=\n"
        "Rxfd_%=:\n"
        "s_sub_u32 s81, s81, 1\n"
        "s_cbranch_scc1 Cxfd_%=\n"
        "s_sleep 1\n"
        "global_load_dwordx2 v[60:61], v34, %[sws] sc1\n"
        "s_waitcnt vmcnt(0)\n"
        "v_bfe_u32 v59, v33, 24, 4\n"
        "v_subrev_u32 v60, s56, v60\n"
        "v_sub_u32 v60, v60, v59\n"
        "v_cmp_lt_u32 vcc, 7, v60\n"
        "v_cmp_ne_u32 s[82:83], s57, v61\n"
        "s_or_b64 vcc, vcc, s[82:83]\n"
        "s_cbranch_vccnz Rxfd_%=\n"
        "s_branch Cxfd_%=\n"
        "Rlate_%=:\n"
        "s_sub_u32 s81, s81, 1\n"
        "s_cbranch_scc1 Clate_%=\n"
        "s_sleep 1\n"
        "v_add_u32 v31, s25, %[vl16]\n"
        "v_add_u32 v31, 16384, v31\n"
        "global_load_dwordx4 v[208:211], v31, %[sws] offset:0 sc1\n"
        "global_load_dwordx4 v[212:215], v31, %[sws] offset:1024 sc1\n"
        "v_add_u32 v31, s36, v39\n"
        "global_load_dwordx4 v[96:99], v31, %[sws] offset:0 sc1\n"
        "global_load_dwordx4 v[100:103], v31, %[sws] offset:256 sc1\n"
        "v_lshl_add_u32 v31, s50, 3, v31\n"
        "global_load_dwordx4 v[104:107], v31, %[sws] sc1\n"
        "v_add_u32 v31, s37, %[vl16]\n"
        "global_load_dwordx4 v[108:111], v31, %[sws] offset:0 sc1\n"
        "global_load_dwordx4 v[112:115], v31, %[sws] offset:1024 sc1\n"
        "global_load_dwordx2 v[116:117], v16, %[sws] offset:0 sc1\n"
        "global_load_dwordx2 v[118:119], v16, %[sws] offset:32 sc1\n"
        "global_load_dwordx2 v[120:121], v17, %[sws] offset:0 sc1\n"
        "global_load_dwordx2 v[122:123], v17, %[sws] offset:32 sc1\n"
        "global_load_dwordx2 v[124:125], v18, %[sws] sc1\n"
        "s_waitcnt vmcnt(0)\n"
        "v_max3_u32 v59, v208, v209, v210\n"
        "v_max3_u32 v59, v211, v212, v59\n"
        "v_max3_u32 v59, v213, v214, v59\n"
        "v_max3_u32 v59, v215, v96, v59\n"
        "v_max3_u32 v59, v97, v98, v59\n"
        "v_max3_u32 v59, v99, v100, v59\n"
        "v_max3_u32 v59, v101, v102, v59\n"
        "v_max3_u32 v59, v103, v104, v59\n"
        "v_max3_u32 v59, v105, v106, v59\n"
        "v_max3_u32 v59, v107, v108, v59\n"
        "v_max3_u32 v59, v109, v110, v59\n"
        "v_max3_u32 v59, v111, v112, v59\n"
        "v_max3_u32 v59, v113, v114, v59\n"
        "v_max_u32 v59, v115, v59\n"
        "v_cmp_eq_u32 vcc, -1, v59\n"
        "v_max_u32 v59, v116, v117\n"
        "v_max3_u32 v59, v118, v119, v59\n"
        "v_cmp_eq_u32 s[82:83], -1, v59\n"
        "s_and_b64 s[82:83], s[82:83], s[60:61]\n"
        "s_or_b64 vcc, vcc, s[82:83]\n"
        "v_max_u32 v59, v120, v121\n"
        "v_max3_u32 v59, v122, v123, v59\n"
        "v_cmp_eq_u32 s[82:83], -1, v59\n"
        "s_and_b64 s[82:83], s[82:83], s[62:63]\n"
        "s_or_b64 vcc, vcc, s[82:83]\n"
        "v_max_u32 v59, v124, v125\n"
        "v_cmp_eq_u32 s[82:83], -1, v59\n"
        "s_and_b64 s[82:83], s[82:83], s[64:65]\n"
        "s_or_b64 vcc, vcc, s[82:83]\n"
        "s_cbranch_vccnz Rlate_%=\n"
        "s_branch Clate_%=\n"
        "Rcf_%=:\n"
        "s_sub_u32 s81, s81, 1\n"
        "s_cbranch_scc1 Ccf_%=\n"
        "s_sleep 1\n"
        "v_add_u32 v62, 6144, %[vl16]\n"
        "global_load_dwordx4 v[224:227], v62, %[sws] offset:0 sc1\n"
        "global_load_dwordx4 v[228:231], v62, %[sws] offset:1024 sc1\n"
        "s_waitcnt vmcnt(0)\n"
        "v_cmp_ne_u32 vcc, s56, v224\n"
        "v_cmp_ne_u32 s[82:83], s57, v225\n"
        "s_or_b64 vcc, vcc, s[82:83]\n"
        "v_cmp_ne_u32 s[82:83], s56, v226\n"
        "s_or_b64 vcc, vcc, s[82:83]\n"
        "v_cmp_ne_u32 s[82:83], s57, v227\n"
        "s_or_b64 vcc, vcc, s[82:83]\n"
        "v_cmp_ne_u32 s[82:83], s56, v228\n"
        "s_or_b64 vcc, vcc, s[82:83]\n"
        "v_cmp_ne_u32 s[82:83], s57, v229\n"
        "s_or_b64 vcc, vcc, s[82:83]\n"
        "v_cmp_ne_u32 s[82:83], s56, v230\n"
        "s_or_b64 vcc, vcc, s[82:83]\n"
        "v_cmp_ne_u32 s[82:83], s57, v231\n"
        "s_or_b64 vcc, vcc, s[82:83]\n"
        "s_cbranch_vccnz Rcf_%=\n"
        "s_branch Ccf_%=\n"
        "END_%=:\n"
        : "+{v[64:95]}"(W0), "+{v[96:127]}"(W1), "+{v[128:159]}"(P2a), "+{v[160:191]}"(P2b)
        : [vl16] "v"(l * 16),
          [sws] "s"(ws),
          [sxr] "s"(sxr),
          [swr] "s"(ARG(5)),
          "{s[56:57]}"(tag),
          "{s[24:39]}"(KP),
          "{s[40:43]}"(LB),
          "{s[44:47]}"(DP),
          "{s[48:55]}"(MP)
        : "scc", "vcc", "memory", "s59", "s60", "s61", "s62", "s63", "s64", "s65", "s81", "s82", "s83", "v16", "v17", "v18", "v19", "v20", "v21", "v22", "v23", "v24", "v25", "v26", "v27", "v28", "v29", "v30", "v31", "v32", "v33", "v34", "v35", "v36", "v37", "v38", "v39", "v40", "v41", "v42", "v43", "v44", "v45", "v46", "v47", "v48", "v49", "v50", "v51", "v52", "v53", "v54", "v55", "v56", "v57", "v58", "v59", "v60", "v61", "v62", "v63", "v192", "v193", "v194", "v195", "v196", "v197", "v198", "v199", "v200", "v201", "v202", "v203", "v204", "v205", "v206", "v207", "v208", "v209", "v210", "v211", "v212", "v213", "v214", "v215", "v216", "v217", "v218", "v219", "v220", "v221", "v222", "v223", "v224", "v225", "v226", "v227", "v228", "v229", "v230", "v231", "v232", "v233", "v234", "v235", "v236", "v237", "v238", "v239", "v240", "v241", "v242", "v243", "v244", "v245", "v246", "v247", "v248", "v249", "v250", "v251", "v252", "v253", "v254", "v255", "a0", "a1", "a2", "a3", "a4", "a5", "a6", "a7", "a8", "a9", "a10", "a11", "a12", "a13", "a14", "a15", "a16", "a17", "a18", "a19", "a20", "a21", "a22", "a23", "a24", "a25", "a26", "a27", "a28", "a29", "a30", "a31", "a32", "a33", "a34", "a35", "a36", "a37", "a38", "a39", "a40", "a41", "a42", "a43", "a44", "a45", "a46", "a47", "a48", "a49", "a50", "a51", "a52", "a53", "a54", "a55", "a56", "a57", "a58", "a59", "a60", "a61", "a62", "a63", "a64", "a65", "a66", "a67", "a68", "a69", "a70", "a71", "a72", "a73", "a74", "a75", "a76", "a77", "a78", "a79", "a80", "a81", "a82", "a83", "a84", "a85", "a86", "a87", "a88", "a89", "a90", "a91", "a92", "a93", "a94", "a95", "a96", "a97", "a98", "a99", "a100", "a101", "a102", "a103", "a104", "a105", "a106", "a107", "a108", "a109", "a110", "a111", "a112", "a113", "a114", "a115", "a116", "a117", "a118", "a119", "a120", "a121", "a122", "a123", "a124", "a125", "a126", "a127", "a128", "a129", "a130", "a131", "a132", "a133", "a134", "a135", "a136", "a137", "a138", "a139", "a140", "a141", "a142", "a143", "a144", "a145", "a146", "a147", "a148", "a149", "a150", "a151", "a152", "a153", "a154", "a155", "a156", "a157", "a158", "a159", "a160", "a161", "a162", "a163", "a164", "a165", "a166", "a167", "a168", "a169", "a170", "a171", "a172", "a173", "a174", "a175", "a176", "a177", "a178", "a179", "a180", "a181", "a182", "a183", "a184", "a185", "a186", "a187", "a188", "a189", "a190", "a191", "a192", "a193", "a194", "a195", "a196", "a197", "a198", "a199", "a200", "a201", "a202", "a203", "a204", "a205", "a206", "a207", "a208", "a209", "a210", "a211", "a212", "a213", "a214", "a215", "a216", "a217", "a218", "a219", "a220", "a221", "a222", "a223", "a224", "a225", "a226", "a227", "a228", "a229", "a230", "a231", "a232", "a233", "a234", "a235", "a236", "a237", "a238", "a239", "a240", "a241", "a242", "a243", "a244", "a245", "a246", "a247", "a248", "a249", "a250", "a251", "a252", "a253", "a254", "a255");
        for (int i_ = 0; i_ < 16; ++i_) XO[i_] = __float_as_uint(W1[16 + i_]);
        ASTAMP_G2B;
    }
    LANE();
    STAMP(11);
    float g3, c3;
    {
        float s1, s2, s3;
        SPIN(n) {
            unsigned mx = 0; s1 = s2 = s3 = 0;
#pragma unroll
            for (int i = 0; i < 8; ++i) {
                const u32x4 v = ld16(R, A_ST + ST_SZ + (((bt * 16 + (l >> 5) * 8 + i) * 128) + 32 * w + (l & 31)) * 16);
                mx = mx4(v, mx); s1 += __uint_as_float(v.x); s2 += __uint_as_float(v.y); s3 += __uint_as_float(v.z);
            }
            if (__all(mx != SENT) || giveup(n)) break;
        }
        STAMP(12);
        s1 = sum32(s1); s2 = sum32(s2); s3 = sum32(s3);
        const float mu = s1 * (1.f / 640);
        float m, rs;
        ln_params(mu, s2 * (1.f / 640) - mu * mu, m, rs);
        if (bep == 0 && l < 32) {
            const float r = rbf((s3 - m * wsum) * rs);
            const unsigned u = __float_as_uint(r), row = bt * 128 + w * 32 + l;
            st4(R, A_RB + 8192 + row * 4, (r == r ? (u ^ ((unsigned)((int)u >> 31) | 0x80000000u)) & 0xffff0000u : 0u) | (0xffffu - row));
            st16(R, A_MS + 32768 + row * 16, u32x4{nosent(__float_as_uint(m)), nosent(__float_as_uint(rs)), 0u, 0u});
        }
        g3 = 1.f + rs; c3 = -m * rs;
    }
    STAMP(13);
    ldsw<float>(T_G + (w * 32 + (l & 31)) * 4, g3); ldsw<float>(T_C + (w * 32 + (l & 31)) * 4, c3);
    if (f1 >= 0 || f2 >= 0) {
        u32x4 ka, kb;
        SPIN(n) {
            ka = ld16(R, A_RB + 8192 + tid * 32); kb = ld16(R, A_RB + 8192 + tid * 32 + 16);
            if (__all(mx4(ka, mx4(kb)) != SENT) || giveup(n)) break;
        }
        FST(26);
        unsigned rank;
        const unsigned me = topk(ka, kb, T_CAND + 256, w, l, rank);
        STAMP(14);
        if (f1 >= 0) ffn1<1>(R, me, rank, f1, LD + 24576, LD, nullptr, nullptr, w, l, ws, tid);
        else { bar(); if (f2 == 0 && tid < 8) st4(R, A_IDX + 64 + l * 4, ldsr<unsigned>(T_SEL + l * 4)); }
    } else {
        unsigned ix;
        SPIN(n) {
            __builtin_amdgcn_s_sleep(8);
            ix = ld4(R, A_IDX + 64 + (l & 7) * 4);
            if (__all(ix != SENT) || giveup(n)) break;
        }
        if (w == 0 && l < 8) ldsw<unsigned>(T_SEL + l * 4, ix & 2047u);
        bar();
        STAMP(14);
    }
    STAMP(15);
    out = (u16*)ARG(9);
    if (f2 >= 0) ffn2<1>(R, f2, LD, out, w, l);
    {
        const u32x4 i0 = ldsr<u32x4>(T_SEL), i1 = ldsr<u32x4>(T_SEL + 16);
#pragma unroll
        for (int m = 0; m < 2; ++m) {
            const unsigned rl = w * 32 + m * 16 + (l & 15), row = bt * 128 + rl;
            const float g = ldsr<float>(T_G + rl * 4), c = ldsr<float>(T_C + rl * 4);
            bool h = false;
#pragma unroll
            for (int i = 0; i < 4; ++i) h |= (i0[i] == row) | (i1[i] == row);
            if (!h) {
                *(u32x4*)(out + (size_t)row * 640 + be4 * 160 + q * 32 + (l >> 4) * 8) =
                    u32x4{affine2(XO[8 + 4 * m], g, c), affine2(XO[9 + 4 * m], g, c), affine2(XO[10 + 4 * m], g, c), affine2(XO[11 + 4 * m], g, c)};
                if ((q >> 1) == m) *(u32x2*)(out + (size_t)row * 640 + be4 * 160 + shc + (l >> 4) * 4) = u32x2{affine2(XO[6], g, c), affine2(XO[7], g, c)};
            }
        }
    }
    STAMP(16);
}

extern "C" void kernel_launch(void* const* d_in, const int* in_sizes, int n_in, void* d_out, int out_size,
                              void* d_ws, size_t ws_size, hipStream_t stream) {
    (void)hipFuncSetAttribute((const void*)fnet_mod_mk, hipFuncAttributeMaxDynamicSharedMemorySize, 163840);
    fnet_mod_mk<<<256, 256, 163840, stream>>>((const u16*)d_in[0], (const u16*)d_in[1], (const u16*)d_in[2], (const u16*)d_in[3], (const u16*)d_in[4],
                                              (const u16*)d_in[6], (const u16*)d_in[7], (const u16*)d_in[8], (const u16*)d_in[9], (u16*)d_out, (char*)d_ws);
}
```
